```python
import torch

def _softmax(x, axis=-1):
    return torch.softmax(x, dim=axis)
def _gelu(x):
    return torch.nn.functional.gelu(x, approximate="tanh")
def _silu(x):
    return torch.nn.functional.silu(x)
def _sigmoid(x):
    return torch.sigmoid(x)
def _layernorm(x, axis=-1, eps=1e-5):
    m = x.mean(dim=axis, keepdim=True)
    v = x.var(dim=axis, keepdim=True, unbiased=False)
    return (x - m) / torch.sqrt(v + eps)
def _scatter_add(tgt, idx, src, axis=0):
    return tgt.clone().index_add_(axis, idx, src)
def _segment_sum(src, seg, n):
    out = torch.zeros((n,) + src.shape[1:], dtype=src.dtype)
    return out.index_add_(0, seg, src)
def _pad(x, pw):
    flat = tuple(v for pair in reversed(pw) for v in pair)
    return torch.nn.functional.pad(x, flat)

kernel_name = 'standalone__gqa_block_cached__near_ridge'

def setup_inputs():

    _w = (lambda a: (lambda t: t.to(torch.bfloat16) if t.is_floating_point() else t)(torch.as_tensor(a)))
    B, Sq, D = 8, 160, 448
    Hq, Hkv, Sk, Dh = 16, 8, 2048, 256
    assert Hq % Hkv == 0, (Hq, Hkv)
    return {
        "x": _w((lambda *s: torch.randn(*s, dtype=torch.bfloat16) * 0.8)(B, Sq, D)),
        "k_cache": _w((lambda *s: torch.randn(*s, dtype=torch.bfloat16) * 0.8)(B, Hkv, Sk, Dh)),
        "v_cache": _w((lambda *s: torch.randn(*s, dtype=torch.bfloat16) * 0.05)(B, Hkv, Sk, Dh)),
        "wq": _w((lambda *s: torch.randn(*s, dtype=torch.bfloat16) * 0.8)(D, Hq * Dh) * (D ** -0.5)),
        "wo": _w((lambda *s: torch.randn(*s, dtype=torch.bfloat16) * 0.05)(Hq * Dh, D) * ((Hq * Dh) ** -0.5)),
    }

def reference(x, k_cache, v_cache, wq, wo):
    B, Sq, D = x.shape
    Hq, Dh = 16, 256
    Hkv = k_cache.shape[1]
    rep = Hq // Hkv
    q = (lambda x, *d: x.permute(*d))((x @ wq).reshape(B, Sq, Hq, Dh), 0, 2, 1, 3)
    k = (lambda x, n, axis: torch.repeat_interleave(x, n, dim=axis))(k_cache, rep, axis=1)
    v = (lambda x, n, axis: torch.repeat_interleave(x, n, dim=axis))(v_cache, rep, axis=1)
    scores = torch.einsum("bhqd,bhkd->bhqk", q, k) * (Dh ** -0.5)
    p = _softmax(scores, axis=-1)
    o = torch.einsum("bhqk,bhkd->bhqd", p, v)
    return (lambda x, *d: x.permute(*d))(o, 0, 2, 1, 3).reshape(B, Sq, Hq * Dh) @ wo
```

```cpp
#include <hip/hip_runtime.h>
#include <hip/hip_bf16.h>
#include <stdint.h>
typedef unsigned short u16;
static __device__ __forceinline__ float b2f(u16 v){ return __uint_as_float(((unsigned)v)<<16); }
static __device__ __forceinline__ u16 f2b(float f){ unsigned u=__float_as_uint(f); u += 0x7fffu + ((u>>16)&1u); return (u16)(u>>16); }
constexpr int B=8,SQ=160,D=448,HQ=16,HKV=8,SK=2048,DH=256;
__global__ void gemm_naive(const u16* A,const u16* Bm,u16* C,int M,int N,int K){
  int n = blockIdx.x*blockDim.x+threadIdx.x; int m = blockIdx.y;
  if(n>=N) return;
  float acc=0.f;
  for(int k=0;k<K;k++) acc += b2f(A[(size_t)m*K+k])*b2f(Bm[(size_t)k*N+n]);
  C[(size_t)m*N+n]=f2b(acc);
}
__global__ void attn_naive(const u16* Q,const u16* Kc,const u16* Vc,u16* O){
  __shared__ float qs[DH]; __shared__ float p[SK]; __shared__ float red[256];
  int q = blockIdx.x, h = blockIdx.y, b = blockIdx.z; int t=threadIdx.x; int kvh=h/2;
  qs[t]=b2f(Q[((size_t)(b*SQ+q))*HQ*DH + h*DH + t]); __syncthreads();
  const u16* Kb = Kc + ((size_t)(b*HKV+kvh))*SK*DH; const u16* Vb = Vc + ((size_t)(b*HKV+kvh))*SK*DH;
  float mx=-1e30f;
  for(int k=t;k<SK;k+=256){ float s=0; for(int d=0;d<DH;d++) s+=qs[d]*b2f(Kb[(size_t)k*DH+d]); s=b2f(f2b(s)); s=b2f(f2b(s*0.0625f)); p[k]=s; mx=fmaxf(mx,s);}
  red[t]=mx; __syncthreads(); for(int s=128;s>0;s>>=1){ if(t<s) red[t]=fmaxf(red[t],red[t+s]); __syncthreads(); } mx=red[0]; __syncthreads();
  float sm=0; for(int k=t;k<SK;k+=256){ float e=expf(p[k]-mx); p[k]=e; sm+=e; }
  red[t]=sm; __syncthreads(); for(int s=128;s>0;s>>=1){ if(t<s) red[t]+=red[t+s]; __syncthreads(); } sm=red[0]; __syncthreads();
  for(int k=t;k<SK;k+=256) p[k]=b2f(f2b(p[k]/sm)); __syncthreads();
  float o=0; for(int k=0;k<SK;k++) o+=p[k]*b2f(Vb[(size_t)k*DH+t]);
  O[((size_t)(b*SQ+q))*HQ*DH + h*DH + t]=f2b(o);
}
extern "C" void kernel_launch(void* const* d_in, const int* in_sizes, int n_in, void* d_out, int out_size, void* d_ws, size_t ws_size, hipStream_t stream){
  const u16* x=(const u16*)d_in[0]; const u16* kc=(const u16*)d_in[1]; const u16* vc=(const u16*)d_in[2]; const u16* wq=(const u16*)d_in[3]; const u16* wo=(const u16*)d_in[4];
  u16* q=(u16*)d_ws; u16* o=q+(size_t)B*SQ*HQ*DH;
  gemm_naive<<<dim3(HQ*DH/256,B*SQ),256,0,stream>>>(x,wq,q,B*SQ,HQ*DH,D);
  attn_naive<<<dim3(SQ,HQ,B),256,0,stream>>>(q,kc,vc,o);
  gemm_naive<<<dim3((D+255)/256,B*SQ),256,0,stream>>>(o,wo,(u16*)d_out,B*SQ,D,HQ*DH);
}
```

```cpp
#include <hip/hip_runtime.h>
#include <hip/hip_bf16.h>
#include <stdint.h>
typedef unsigned short u16;
typedef float f32x4 __attribute__((ext_vector_type(4)));
typedef float f32x2 __attribute__((ext_vector_type(2)));
typedef __bf16 bf16x8 __attribute__((ext_vector_type(8)));
typedef __bf16 bf16x2 __attribute__((ext_vector_type(2)));
typedef short s16x4 __attribute__((ext_vector_type(4)));
typedef short s16x8 __attribute__((ext_vector_type(8)));
typedef unsigned u32x4 __attribute__((ext_vector_type(4)));
typedef unsigned u32x2 __attribute__((ext_vector_type(2)));
#define LDSP(T,p) ((__attribute__((address_space(3))) T*)(p))
#define DI __device__ __forceinline__
constexpr int B=8,SQ=160,D=448,HQ=16,HKV=8,SK=2048,DH=256;
DI unsigned cvtpk(float lo,float hi){ f32x2 v={lo,hi}; bf16x2 b=__builtin_convertvector(v,bf16x2); return __builtin_bit_cast(unsigned,b);}
DI f32x4 mfma16(s16x8 a,s16x8 b,f32x4 c){ return __builtin_amdgcn_mfma_f32_16x16x32_bf16(__builtin_bit_cast(bf16x8,a),__builtin_bit_cast(bf16x8,b),c,0,0,0);}
DI s16x4 trread(const char* p){ return __builtin_amdgcn_ds_read_tr16_b64_v4i16(LDSP(s16x4,p)); }
#define PROBE 0
#define KVNT 1
__global__ __launch_bounds__(256) void gemm64(const u16* __restrict__ A,const u16* __restrict__ Bm,u16* __restrict__ C,int M,int N,int K,float scale){
  __shared__ __attribute__((aligned(16))) char sA[64*80];
  __shared__ __attribute__((aligned(16))) char sB[32*144];
  const int t=threadIdx.x, l=t&63, w=t>>6, g=l>>4, r=l&15;
  const int m0=blockIdx.y*64, n0=blockIdx.x*64, wm=(w>>1)*32, wn=(w&1)*32;
  f32x4 acc[2][2]; for(int i=0;i<2;i++) for(int j=0;j<2;j++) acc[i][j]=(f32x4){0,0,0,0};
  for(int k0=0;k0<K;k0+=32){
    u32x4 va=*(const u32x4*)(A+(size_t)(m0+(t>>2))*K+k0+(t&3)*8);
    u32x4 vb=*(const u32x4*)(Bm+(size_t)(k0+(t>>3))*N+n0+(t&7)*8);
    __syncthreads();
    *(u32x4*)(sA+(t>>2)*80+(t&3)*16)=va; *(u32x4*)(sB+(t>>3)*144+(t&7)*16)=vb;
    __syncthreads();
    s16x8 af[2],bf[2];
    for(int i=0;i<2;i++) af[i]=*(const s16x8*)(sA+(wm+16*i+r)*80+g*16);
    for(int j=0;j<2;j++){ s16x4 lo=trread(sB+(8*g+(r>>2))*144+(wn+16*j+4*(r&3))*2), hi=trread(sB+(8*g+4+(r>>2))*144+(wn+16*j+4*(r&3))*2); bf[j]=__builtin_shufflevector(lo,hi,0,1,2,3,4,5,6,7);}
    for(int i=0;i<2;i++) for(int j=0;j<2;j++) acc[i][j]=mfma16(bf[j],af[i],acc[i][j]);
  }
  for(int i=0;i<2;i++) for(int j=0;j<2;j++){ u32x2 o; o[0]=cvtpk(acc[i][j][0]*scale,acc[i][j][1]*scale); o[1]=cvtpk(acc[i][j][2]*scale,acc[i][j][3]*scale);
    *(u32x2*)(C+(size_t)(m0+wm+16*i+r)*N+n0+wn+16*j+4*g)=o; }
}
__global__ __launch_bounds__(320) void attn1(const u16* __restrict__ Q,const u16* __restrict__ Kc,const u16* __restrict__ Vc,u16* __restrict__ O){
  __shared__ __attribute__((aligned(16))) char sK[32*528];
  __shared__ __attribute__((aligned(16))) char sV[32*544];
  const int t=threadIdx.x, l=t&63, w=t>>6, g=l>>4, r=l&15;
  const int bid=blockIdx.x, kvh=bid&7, j=bid>>3, b=j>>2, h=kvh*2+((j>>1)&1), half=j&1;
  const int row=half*80+w*16+r;
  const u16* Qp=Q+((size_t)(b*SQ+row))*HQ*DH+h*DH;
  s16x8 qf[8]; for(int ks=0;ks<8;ks++) qf[ks]=*(const s16x8*)(Qp+32*ks+8*g);
  const u16* Kb=Kc+((size_t)(b*HKV+kvh))*SK*DH; const u16* Vb=Vc+((size_t)(b*HKV+kvh))*SK*DH;
  f32x4 o[16]; for(int i=0;i<16;i++) o[i]=(f32x4){0,0,0,0};
  float m=-1e30f, lsum=0.f;
  for(int k0=0;k0<SK;k0+=32){
    u32x4 kv[4],vv[4];
    for(int i=0;i<4;i++){ int c=t+320*i; if(c<1024){
      if(KVNT){ kv[i]=__builtin_nontemporal_load((const u32x4*)(Kb+(size_t)k0*DH)+c); vv[i]=__builtin_nontemporal_load((const u32x4*)(Vb+(size_t)k0*DH)+c);}
      else { kv[i]=((const u32x4*)(Kb+(size_t)k0*DH))[c]; vv[i]=((const u32x4*)(Vb+(size_t)k0*DH))[c]; } } }
    __syncthreads();
    for(int i=0;i<4;i++){ int c=t+320*i; if(c<1024){ *(u32x4*)(sK+(c>>5)*528+(c&31)*16)=kv[i]; *(u32x4*)(sV+(c>>5)*544+(c&31)*16)=vv[i]; } }
    __syncthreads();
    f32x4 s[2];
    for(int kt=0;kt<2;kt++){ s[kt]=(f32x4){0,0,0,0}; for(int ks=0;ks<8;ks++){ s16x8 kf=*(const s16x8*)(sK+(16*kt+r)*528+64*ks+16*g); s[kt]=mfma16(kf,qf[ks],s[kt]); } }
    float mx=fmaxf(fmaxf(fmaxf(s[0][0],s[0][1]),fmaxf(s[0][2],s[0][3])),fmaxf(fmaxf(s[1][0],s[1][1]),fmaxf(s[1][2],s[1][3])));
    mx=fmaxf(mx,__shfl_xor(mx,16)); mx=fmaxf(mx,__shfl_xor(mx,32));
    float mn=fmaxf(m,mx), al=__builtin_amdgcn_exp2f(m-mn); m=mn;
    float p[8]; float ps=0; for(int i=0;i<8;i++){ p[i]=__builtin_amdgcn_exp2f(s[i>>2][i&3]-mn); ps+=p[i]; }
    lsum=lsum*al+ps;
    u32x4 pw; for(int i=0;i<4;i++) pw[i]=cvtpk(p[2*i],p[2*i+1]);
    s16x8 pf=__builtin_bit_cast(s16x8,pw);
    for(int dt=0;dt<16;dt++){
      s16x4 lo=trread(sV+(4*g+(r>>2))*544+(16*dt+4*(r&3))*2), hi=trread(sV+(16+4*g+(r>>2))*544+(16*dt+4*(r&3))*2);
      s16x8 vf=__builtin_shufflevector(lo,hi,0,1,2,3,4,5,6,7);
      o[dt]*=al; o[dt]=mfma16(vf,pf,o[dt]);
    }
  }
  lsum+=__shfl_xor(lsum,16); lsum+=__shfl_xor(lsum,32);
  float inv=1.f/lsum;
  u16* Op=O+((size_t)(b*SQ+row))*HQ*DH+h*DH;
  for(int dt=0;dt<16;dt++){ u32x2 ow; ow[0]=cvtpk(o[dt][0]*inv,o[dt][1]*inv); ow[1]=cvtpk(o[dt][2]*inv,o[dt][3]*inv); *(u32x2*)(Op+16*dt+4*g)=ow; }
}
template<int NTL>
__global__ __launch_bounds__(256) void probe_k(const u32x4* __restrict__ K,const u32x4* __restrict__ V,size_t n, unsigned* sink){
  size_t i = (size_t)blockIdx.x*256*8+threadIdx.x; u32x4 acc={0,0,0,0};
  for(; i<n; i+=(size_t)gridDim.x*256*8){
    u32x4 v[8],w[8];
    #pragma unroll
    for(int u=0;u<8;u++){ v[u]= NTL? __builtin_nontemporal_load(&K[i+u*256]) : K[i+u*256]; w[u]= NTL? __builtin_nontemporal_load(&V[i+u*256]) : V[i+u*256]; }
    #pragma unroll
    for(int u=0;u<8;u++) acc ^= v[u]^w[u];
  }
  sink[blockIdx.x*256+threadIdx.x]=acc[0]^acc[1]^acc[2]^acc[3];
}
extern "C" void kernel_launch(void* const* d_in, const int* in_sizes, int n_in, void* d_out, int out_size, void* d_ws, size_t ws_size, hipStream_t stream){
  const u16* x=(const u16*)d_in[0]; const u16* kc=(const u16*)d_in[1]; const u16* vc=(const u16*)d_in[2]; const u16* wq=(const u16*)d_in[3]; const u16* wo=(const u16*)d_in[4];
  u16* q=(u16*)d_ws; u16* o=q+(size_t)B*SQ*HQ*DH; unsigned* sink=(unsigned*)(o+(size_t)B*SQ*HQ*DH);
  size_t n16=(size_t)B*HKV*SK*DH/8;
  if(PROBE==1||PROBE==3) probe_k<0><<<2048,256,0,stream>>>((const u32x4*)kc,(const u32x4*)vc,n16,sink);
  if(PROBE==2) probe_k<1><<<2048,256,0,stream>>>((const u32x4*)kc,(const u32x4*)vc,n16,sink);
  if(PROBE==3) probe_k<0><<<2048,256,0,stream>>>((const u32x4*)kc,(const u32x4*)vc,n16,sink);
  gemm64<<<dim3(HQ*DH/64,B*SQ/64),256,0,stream>>>(x,wq,q,B*SQ,HQ*DH,D,1.4426950408889634f*0.0625f);
  attn1<<<256,320,0,stream>>>(q,kc,vc,o);
  gemm64<<<dim3(D/64,B*SQ/64),256,0,stream>>>(o,wo,(u16*)d_out,B*SQ,D,HQ*DH,1.0f);
}
```

```cpp
#include <hip/hip_runtime.h>
#include <hip/hip_bf16.h>
#include <stdint.h>
#define LDSP(T,p) ((__attribute__((address_space(3))) T*)(p))
constexpr size_t GRP_STRIDE = 16384;
constexpr size_t PART_OFF = 64*GRP_STRIDE;
constexpr size_t PART_STRIDE = 16*73728+2048;

#define VCLOB "v0","v1","v2","v3","v4","v5","v6","v7","v8","v9","v10","v11","v12","v13","v14","v15","v16","v17","v18","v19","v20","v21","v22","v23","v24","v25","v26","v27","v28","v29","v30","v31","v32","v33","v34","v35","v36","v37","v38","v39","v40","v41","v42","v43","v44","v45","v46","v47","v48","v49","v50","v51","v52","v53","v54","v55","v56","v57","v58","v59","v60","v61","v62","v63","v64","v65","v66","v67","v68","v69","v70","v71","v72","v73","v74","v75","v76","v77","v78","v79","v80","v81","v82","v83","v84","v85","v86","v87","v88","v89","v90","v91","v92","v93","v94","v95","v96","v97","v98","v99","v100","v101","v102","v103","v104","v105","v106","v107","v108","v109","v110","v111","v112","v113","v114","v115","v116","v117","v118","v119","v120","v121","v122","v123","v124","v125","v126","v127","v128","v129","v130","v131","v132","v133","v134","v135","v136","v137","v138","v139","v140","v141","v142","v143","v144","v145","v146","v147","v148","v149","v150","v151","v152","v153","v154","v155","v156","v157","v158","v159","v160","v161","v162","v163","v164","v165","v166","v167","v168","v169","v170","v171","v172","v173","v174","v175","v176","v177","v178","v179","v180","v181","v182","v183","v184","v185","v186","v187","v188","v189","v190","v191","v192","v193","v194","v195","v196","v197","v198","v199","v200","v201","v202","v203","v204","v205","v206","v207","v208","v209","v210","v211","v212","v213","v214","v215","v216","v217","v218","v219","v220","v221","v222","v223","v224","v225","v226","v227","v228","v229","v230","v231","v232","v233","v234","v235","v236","v237","v238","v239","v240","v241","v242","v243","v244","v245","v246","v247","v248","v249","v250","v251","v252","v253","v254","v255"
#define ACLOB "a0","a1","a2","a3","a4","a5","a6","a7","a8","a9","a10","a11","a12","a13","a14","a15","a16","a17","a18","a19","a20","a21","a22","a23","a24","a25","a26","a27","a28","a29","a30","a31","a32","a33","a34","a35","a36","a37","a38","a39","a40","a41","a42","a43","a44","a45","a46","a47","a48","a49","a50","a51","a52","a53","a54","a55","a56","a57","a58","a59","a60","a61","a62","a63","a64","a65","a66","a67","a68","a69","a70","a71","a72","a73","a74","a75","a76","a77","a78","a79","a80","a81","a82","a83","a84","a85","a86","a87","a88","a89","a90","a91","a92","a93","a94","a95","a96","a97","a98","a99","a100","a101","a102","a103","a104","a105","a106","a107","a108","a109","a110","a111","a112","a113","a114","a115","a116","a117","a118","a119","a120","a121","a122","a123","a124","a125","a126","a127","a128","a129","a130","a131","a132","a133","a134","a135","a136","a137","a138","a139","a140","a141","a142","a143","a144","a145","a146","a147","a148","a149","a150","a151","a152","a153","a154","a155","a156","a157","a158","a159","a160","a161","a162","a163","a164","a165","a166","a167","a168","a169","a170","a171","a172","a173","a174","a175","a176","a177","a178","a179","a180","a181","a182","a183","a184","a185","a186","a187","a188","a189","a190","a191","a192","a193","a194","a195","a196","a197","a198","a199","a200","a201","a202","a203","a204","a205","a206","a207","a208","a209","a210","a211","a212","a213","a214","a215","a216","a217","a218","a219","a220","a221","a222","a223","a224","a225","a226","a227","a228","a229","a230","a231","a232","a233","a234","a235","a236","a237","a238","a239","a240","a241","a242","a243","a244","a245","a246","a247","a248","a249","a250","a251","a252","a253","a254","a255"
#define SCLOB "s8","s9","s10","s11","s12","s13","s14","s15","s16","s17","s18","s19","s20","s21","s22","s23","s24","s25","s26","s27","s28","s29","s30","s31","s36","s37","s38","s39","s40","s41","s42","s43","s44","s45","s46","s47","s48","s49","s50","s51","s52","s53","s54","s55","s56","s57","s58","s59","s60","s61","s62","s63","s64","s65","s66","s67","s68","s69","s70","s71","s72","s73","s74","s75","s76","s77","s78","s79","s80","s81","s82","s83","s84","s85","s86","s87","s88","s89","s90","s91","s92","s93","s94","s95","s96","s97","s98","s99"

__global__ __launch_bounds__(256,1) void attn_main(const char* __restrict__ x,const char* __restrict__ kc,const char* __restrict__ vc,const char* __restrict__ wq,const char* __restrict__ wo,char* __restrict__ ws,char* __restrict__ out){
  __shared__ __attribute__((aligned(1024))) char lds[163840];
  const unsigned wid=__builtin_amdgcn_readfirstlane(threadIdx.x>>6);
  const unsigned ldsb=(unsigned)(size_t)LDSP(char,lds);
  asm volatile(
    "s_load_dwordx8 s[8:15], %0, 0x0\n"
    "s_load_dwordx4 s[16:19], %0, 0x20\n"
    "s_load_dwordx2 s[20:21], %0, 0x30\n"
    "s_lshr_b32 s64, %2, 1\n"
    "s_and_b32 s65, %2, 1\n"
    "v_mbcnt_lo_u32_b32 v0, -1, 0\n"
    "v_mbcnt_hi_u32_b32 v0, -1, v0\n"
    "s_and_b32 s88, %1, 7\n"
    "s_lshr_b32 s23, %1, 3\n"
    "s_getpc_b64 s[58:59]\n"
    "s_and_b32 s79, s23, 1\n"
    "s_lshl_b32 s79, s79, 2\n"
    "s_add_u32 s79, s79, %2\n"
    "s_lshl_b32 s79, s79, 13\n"
    "v_lshlrev_b32 v61, 7, v0\n"
    "v_add_u32 v61, s79, v61\n"
    "v_min_u32 v61, 49152, v61\n"
    "global_load_dword v61, v61, s[58:59]\n"
    "s_lshr_b32 s89, %1, 5\n"
    "s_and_b32 s90, s23, 1\n"
    "s_lshl_b32 s87, s89, 1\n"
    "s_add_u32 s87, s87, s90\n"
    "s_mul_i32 s24, s87, 71680\n"
    "s_mov_b32 s26, 71680\n"
    "s_lshl_b32 s28, s88, 10\n"
    "s_sub_u32 s30, 3670016, s28\n"
    "s_mov_b32 s27, 0x20000\n"
    "s_mov_b32 s31, 0x20000\n"
    "v_lshrrev_b32 v14, 3, v0\n"
    "v_and_b32 v15, 7, v0\n"
    "v_and_b32 v16, 6, v14\n"
    "v_xor_b32 v16, v16, v15\n"
    "v_lshlrev_b32 v16, 4, v16\n"
    "v_mul_u32_u24 v40, 896, v14\n"
    "v_add_u32 v40, v40, v16\n"
    "s_mul_i32 s81, %2, 7168\n"
    "s_lshl_b32 s83, %2, 10\n"
    "s_add_u32 s83, s83, %3\n"
    "s_cmp_lt_u32 %2, 2\n"
    "s_cselect_b32 s60, 1, 0\n"
    "v_lshrrev_b32 v18, 1, v14\n"
    "v_and_b32 v17, 1, v18\n"
    "v_lshlrev_b32 v17, 2, v17\n"
    "v_xor_b32 v17, v17, v15\n"
    "v_lshlrev_b32 v17, 4, v17\n"
    "v_lshl_add_u32 v41, v14, 13, v17\n"
    "v_add_u32 v42, 64512, v41\n"
    "v_add_u32 v43, 129024, v41\n"
    "v_add_u32 v44, 193536, v41\n"
    "s_lshl_b32 s82, %2, 8\n"
    "s_lshl_b32 s84, %2, 14\n"
    "s_add_u32 s84, s84, %3\n"
    "s_add_u32 s84, s84, 90112\n"
    "s_mov_b32 s63, 153600\n"
    "s_bfe_u32 s91, s23, 0x10001\n"
    "s_and_b32 s22, s23, 3\n"
    "s_lshl_b32 s57, s88, 1\n"
    "s_add_u32 s57, s57, s91\n"
    "s_lshl_b32 s93, s89, 3\n"
    "s_add_u32 s93, s93, s88\n"
    "s_lshl_b32 s36, s93, 20\n"
    "s_lshl_b32 s37, s91, 19\n"
    "s_add_u32 s36, s36, s37\n"
    "s_mov_b32 s38, 524288\n"
    "s_mov_b32 s40, s36\n"
    "s_mov_b32 s42, 524288\n"
    "s_lshl_b32 s44, s93, 14\n"
    "s_mov_b32 s46, 16384\n"
    "s_mul_i32 s48, s88, 458752\n"
    "s_mov_b32 s50, 458752\n"
    "s_mul_i32 s52, s87, 1181696\n"
    "s_add_u32 s52, s52, 1048576\n"
    "s_mov_b32 s54, 1181696\n"
    "s_mul_i32 s56, s87, 71680\n"
    "s_mov_b32 s39, 0x20000\n"
    "s_mov_b32 s43, 0x20000\n"
    "s_mov_b32 s47, 0x20000\n"
    "s_mov_b32 s51, 0x20000\n"
    "s_mov_b32 s55, 0x20000\n"
    "v_and_b32 v17, 1, v14\n"
    "v_lshl_add_u32 v17, v18, 3, v17\n"
    "v_lshl_add_u32 v1, v17, 9, v16\n"
    "s_mov_b32 s61, 26624\n"
    "s_cmp_eq_u32 %2, 1\n"
    "s_cmov_b32 s61, 59392\n"
    "s_cmp_eq_u32 %2, 2\n"
    "s_cmov_b32 s61, 73728\n"
    "s_cmp_eq_u32 %2, 3\n"
    "s_cmov_b32 s61, 77824\n"
    "s_add_u32 s61, s61, %3\n"
    "v_and_b32 v14, 15, v0\n"
    "v_lshrrev_b32 v15, 4, v0\n"
    "v_bfe_u32 v16, v0, 2, 2\n"
    "v_and_b32 v17, 3, v0\n"
    "v_and_b32 v19, 6, v14\n"
    "v_xor_b32 v19, v19, v15\n"
    "v_lshlrev_b32 v18, 4, v19\n"
    "v_and_b32 v19, 7, v14\n"
    "v_lshl_add_u32 v18, v19, 7, v18\n"
    "v_lshrrev_b32 v19, 3, v14\n"
    "v_lshl_add_u32 v18, v19, 10, v18\n"
    "v_add_u32 v46, %3, v18\n"
    "v_xor_b32 v47, 64, v46\n"
    "v_add_u32 v50, 0x10000, v46\n"
    "v_add_u32 v51, 0x10000, v47\n"
    "s_lshl_b32 s79, s65, 15\n"
    "v_add_u32 v2, s79, v46\n"
    "v_xor_b32 v3, 64, v2\n"
    "v_lshrrev_b32 v19, 1, v16\n"
    "v_lshl_add_u32 v19, v19, 2, v17\n"
    "v_lshlrev_b32 v18, 4, v19\n"
    "v_lshl_add_u32 v18, v16, 7, v18\n"
    "v_lshl_add_u32 v18, v15, 10, v18\n"
    "v_add_u32 v48, s84, v18\n"
    "v_xor_b32 v49, 64, v48\n"
    "v_add_u32 v52, s61, v18\n"
    "v_xor_b32 v53, 64, v52\n"
    "v_lshrrev_b32 v60, 3, v0\n"
    "v_lshlrev_b32 v60, 13, v60\n"
    "v_and_b32 v59, 7, v0\n"
    "v_lshl_add_u32 v60, v59, 7, v60\n"
    "s_waitcnt lgkmcnt(0)\n"
    "s_add_u32 s24, s24, s8\n"
    "s_addc_u32 s25, s9, 0\n"
    "s_and_b32 s25, s25, 0xffff\n"
    "s_add_u32 s28, s28, s14\n"
    "s_addc_u32 s29, s15, 0\n"
    "s_and_b32 s29, s29, 0xffff\n"
    "s_add_u32 m0, s83, 0\n"
    "s_add_u32 s96, s81, 0\n"
    "buffer_load_dwordx4 v40, s[24:27], s96 offen lds\n"
    "s_add_u32 m0, s83, 4096\n"
    "s_add_u32 s96, s81, 28672\n"
    "buffer_load_dwordx4 v40, s[24:27], s96 offen lds\n"
    "s_cmp_eq_u32 s60, 1\n"
    "s_cselect_b32 s62, 8192, s63\n"
    "s_add_u32 m0, s83, s62\n"
    "s_add_u32 s96, s81, 57344\n"
    "buffer_load_dwordx4 v40, s[24:27], s96 offen lds\n"
    "s_add_u32 m0, s84, 0\n"
    "s_add_u32 s92, s82, 0\n"
    "buffer_load_dwordx4 v41, s[28:31], s92 offen lds\n"
    "buffer_load_dwordx4 v42, s[28:31], s92 offen offset:1024 lds\n"
    "buffer_load_dwordx4 v43, s[28:31], s92 offen offset:2048 lds\n"
    "buffer_load_dwordx4 v44, s[28:31], s92 offen offset:3072 lds\n"
    "s_mul_i32 s79, s23, 14\n"
    "s_lshl_b32 s80, s65, 3\n"
    "s_add_u32 s79, s79, s80\n"
    "s_lshl_b32 s79, s79, 13\n"
    "buffer_load_dword v60, v60, s[28:31], s79 offen\n"
    "s_add_u32 s44, s44, s18\n"
    "s_addc_u32 s45, s19, 0\n"
    "s_and_b32 s45, s45, 0xffff\n"
    "s_add_u32 s52, s52, s18\n"
    "s_addc_u32 s53, s19, 0\n"
    "s_and_b32 s53, s53, 0xffff\n"
    "s_lshl_b32 s97, s22, 2\n"
    "s_add_u32 s97, s97, 8192\n"
    "s_xor_b32 s98, s97, 8\n"
    "buffer_load_dword v56, off, s[44:47], s97 sc1\n"
    "v_and_b32 v54, 15, v0\n"
    "v_lshlrev_b32 v54, 2, v54\n"
    "s_lshl_b32 s79, s57, 6\n"
    "s_add_u32 s79, s79, 1179648\n"
    "buffer_load_dword v55, v54, s[52:55], s79 offen offset:1024 sc1\n"
    "s_add_u32 m0, s84, 4096\n"
    "s_add_u32 s92, s82, 128\n"
    "buffer_load_dwordx4 v41, s[28:31], s92 offen lds\n"
    "buffer_load_dwordx4 v42, s[28:31], s92 offen offset:1024 lds\n"
    "buffer_load_dwordx4 v43, s[28:31], s92 offen offset:2048 lds\n"
    "buffer_load_dwordx4 v44, s[28:31], s92 offen offset:3072 lds\n"
    "s_add_u32 m0, s83, 32768\n"
    "s_add_u32 s96, s81, 128\n"
    "buffer_load_dwordx4 v40, s[24:27], s96 offen lds\n"
    "s_add_u32 m0, s83, 36864\n"
    "s_add_u32 s96, s81, 28800\n"
    "buffer_load_dwordx4 v40, s[24:27], s96 offen lds\n"
    "s_cmp_eq_u32 s60, 1\n"
    "s_cselect_b32 s62, 40960, s63\n"
    "s_add_u32 m0, s83, s62\n"
    "s_add_u32 s96, s81, 57472\n"
    "buffer_load_dwordx4 v40, s[24:27], s96 offen lds\n"
    "s_add_u32 m0, s84, 8192\n"
    "s_add_u32 s92, s82, 262144\n"
    "buffer_load_dwordx4 v41, s[28:31], s92 offen lds\n"
    "buffer_load_dwordx4 v42, s[28:31], s92 offen offset:1024 lds\n"
    "buffer_load_dwordx4 v43, s[28:31], s92 offen offset:2048 lds\n"
    "buffer_load_dwordx4 v44, s[28:31], s92 offen offset:3072 lds\n"
    "s_add_u32 m0, s84, 12288\n"
    "s_add_u32 s92, s82, 262272\n"
    "buffer_load_dwordx4 v41, s[28:31], s92 offen lds\n"
    "buffer_load_dwordx4 v42, s[28:31], s92 offen offset:1024 lds\n"
    "buffer_load_dwordx4 v43, s[28:31], s92 offen offset:2048 lds\n"
    "buffer_load_dwordx4 v44, s[28:31], s92 offen offset:3072 lds\n"
    "s_waitcnt vmcnt(22)\n"
    "s_barrier\n"
    "s_waitcnt vmcnt(18)\n"
    "ds_read_b64_tr_b16 a[0:1], v48 offset:0\n"
    "ds_read_b64_tr_b16 a[2:3], v48 offset:512\n"
    "ds_read_b64_tr_b16 a[4:5], v48 offset:8\n"
    "ds_read_b64_tr_b16 a[6:7], v48 offset:520\n"
    "ds_read_b128 a[16:19], v46 offset:0\n"
    "ds_read_b128 a[20:23], v46 offset:2048\n"
    "ds_read_b128 a[24:27], v46 offset:4096\n"
    "ds_read_b128 a[28:31], v46 offset:6144\n"
    "ds_read_b128 a[32:35], v46 offset:8192\n"
    "s_waitcnt lgkmcnt(4)\n"
    "v_mfma_f32_16x16x32_bf16 v[96:99], a[0:3], a[16:19], 0\n"
    "s_add_u32 m0, s61, 0\n"
    "s_add_u32 s92, s82, 524288\n"
    "s_waitcnt lgkmcnt(3)\n"
    "v_mfma_f32_16x16x32_bf16 v[100:103], a[0:3], a[20:23], 0\n"
    "ds_read_b64_tr_b16 a[8:9], v49 offset:0\n"
    "ds_read_b64_tr_b16 a[10:11], v49 offset:512\n"
    "buffer_load_dwordx4 v41, s[28:31], s92 offen lds\n"
    "s_waitcnt lgkmcnt(4)\n"
    "v_mfma_f32_16x16x32_bf16 v[104:107], a[0:3], a[24:27], 0\n"
    "ds_read_b64_tr_b16 a[12:13], v49 offset:8\n"
    "ds_read_b64_tr_b16 a[14:15], v49 offset:520\n"
    "s_waitcnt lgkmcnt(5)\n"
    "v_mfma_f32_16x16x32_bf16 v[108:111], a[0:3], a[28:31], 0\n"
    "s_add_u32 s36, s36, s10\n"
    "s_addc_u32 s37, s11, 0\n"
    "s_waitcnt lgkmcnt(4)\n"
    "v_mfma_f32_16x16x32_bf16 v[112:115], a[0:3], a[32:35], 0\n"
    "s_waitcnt vmcnt(12)\n"
    "ds_read_b64_tr_b16 a[0:1], v48 offset:4096\n"
    "v_mfma_f32_16x16x32_bf16 v[116:119], a[4:7], a[16:19], 0\n"
    "buffer_load_dwordx4 v42, s[28:31], s92 offen offset:1024 lds\n"
    "v_mfma_f32_16x16x32_bf16 v[120:123], a[4:7], a[20:23], 0\n"
    "ds_read_b64_tr_b16 a[2:3], v48 offset:4608\n"
    "ds_read_b128 a[36:39], v47 offset:0\n"
    "v_mfma_f32_16x16x32_bf16 v[124:127], a[4:7], a[24:27], 0\n"
    "s_and_b32 s37, s37, 0xffff\n"
    "v_mfma_f32_16x16x32_bf16 v[128:131], a[4:7], a[28:31], 0\n"
    "s_add_u32 s40, s40, s12\n"
    "s_addc_u32 s41, s13, 0\n"
    "v_mfma_f32_16x16x32_bf16 v[132:135], a[4:7], a[32:35], 0\n"
    "buffer_load_dwordx4 v43, s[28:31], s92 offen offset:2048 lds\n"
    "s_waitcnt lgkmcnt(5)\n"
    "v_mfma_f32_16x16x32_bf16 v[136:139], a[8:11], a[16:19], 0\n"
    "ds_read_b64_tr_b16 a[4:5], v48 offset:4104\n"
    "ds_read_b64_tr_b16 a[6:7], v48 offset:4616\n"
    "v_mfma_f32_16x16x32_bf16 v[140:143], a[8:11], a[20:23], 0\n"
    "ds_read_b128 a[40:43], v47 offset:2048\n"
    "s_and_b32 s41, s41, 0xffff\n"
    "v_mfma_f32_16x16x32_bf16 v[144:147], a[8:11], a[24:27], 0\n"
    "s_add_u32 s48, s48, s16\n"
    "s_addc_u32 s49, s17, 0\n"
    "v_mfma_f32_16x16x32_bf16 v[148:151], a[8:11], a[28:31], 0\n"
    "s_and_b32 s49, s49, 0xffff\n"
    "v_mfma_f32_16x16x32_bf16 v[152:155], a[8:11], a[32:35], 0\n"
    "buffer_load_dwordx4 v44, s[28:31], s92 offen offset:3072 lds\n"
    "s_waitcnt lgkmcnt(4)\n"
    "v_mfma_f32_16x16x32_bf16 v[156:159], a[12:15], a[16:19], 0\n"
    "ds_read_b64_tr_b16 a[8:9], v49 offset:4096\n"
    "ds_read_b64_tr_b16 a[10:11], v49 offset:4608\n"
    "v_mfma_f32_16x16x32_bf16 v[160:163], a[12:15], a[20:23], 0\n"
    "ds_read_b128 a[44:47], v47 offset:4096\n"
    "v_mfma_f32_16x16x32_bf16 v[164:167], a[12:15], a[24:27], 0\n"
    "s_add_u32 m0, s84, 0\n"
    "s_add_u32 s92, s82, 524416\n"
    "v_mfma_f32_16x16x32_bf16 v[168:171], a[12:15], a[28:31], 0\n"
    "buffer_load_dwordx4 v41, s[28:31], s92 offen lds\n"
    "v_mfma_f32_16x16x32_bf16 v[172:175], a[12:15], a[32:35], 0\n"
    "ds_read_b64_tr_b16 a[12:13], v49 offset:4104\n"
    "ds_read_b64_tr_b16 a[14:15], v49 offset:4616\n"
    "v_mfma_f32_16x16x32_bf16 v[176:179], a[0:3], a[16:19], 0\n"
    "s_add_u32 s8, s56, s20\n"
    "s_addc_u32 s9, s21, 0\n"
    "v_mfma_f32_16x16x32_bf16 v[180:183], a[0:3], a[20:23], 0\n"
    "s_and_b32 s9, s9, 0xffff\n"
    "s_mov_b32 s10, 71680\n"
    "v_mfma_f32_16x16x32_bf16 v[184:187], a[0:3], a[24:27], 0\n"
    "ds_read_b128 a[48:51], v47 offset:6144\n"
    "s_mov_b32 s11, 0x20000\n"
    "v_mfma_f32_16x16x32_bf16 v[188:191], a[0:3], a[28:31], 0\n"
    "buffer_load_dwordx4 v42, s[28:31], s92 offen offset:1024 lds\n"
    "v_mfma_f32_16x16x32_bf16 v[192:195], a[0:3], a[32:35], 0\n"
    "s_waitcnt vmcnt(10)\n"
    "ds_read_b64_tr_b16 a[0:1], v48 offset:8192\n"
    "s_waitcnt lgkmcnt(5)\n"
    "v_mfma_f32_16x16x32_bf16 v[196:199], a[4:7], a[16:19], 0\n"
    "ds_read_b64_tr_b16 a[2:3], v48 offset:8704\n"
    "s_lshl_b32 s79, s65, 14\n"
    "v_mfma_f32_16x16x32_bf16 v[200:203], a[4:7], a[20:23], 0\n"
    "s_add_u32 s80, s79, %3\n"
    "v_and_b32 v19, 1, v15\n"
    "v_mfma_f32_16x16x32_bf16 v[204:207], a[4:7], a[24:27], 0\n"
    "buffer_load_dwordx4 v43, s[28:31], s92 offen offset:2048 lds\n"
    "v_mfma_f32_16x16x32_bf16 v[208:211], a[4:7], a[28:31], 0\n"
    "ds_read_b128 a[52:55], v47 offset:8192\n"
    "v_lshl_add_u32 v19, v19, 2, v16\n"
    "v_mfma_f32_16x16x32_bf16 v[212:215], a[4:7], a[32:35], 0\n"
    "ds_read_b64_tr_b16 a[4:5], v48 offset:8200\n"
    "ds_read_b64_tr_b16 a[6:7], v48 offset:8712\n"
    "v_mfma_f32_16x16x32_bf16 v[216:219], a[8:11], a[16:19], 0\n"
    "v_lshlrev_b32 v18, 7, v19\n"
    "v_and_b32 v19, 6, v19\n"
    "v_mfma_f32_16x16x32_bf16 v[220:223], a[8:11], a[20:23], 0\n"
    "buffer_load_dwordx4 v44, s[28:31], s92 offen offset:3072 lds\n"
    "v_mfma_f32_16x16x32_bf16 v[224:227], a[8:11], a[24:27], 0\n"
    "v_lshrrev_b32 v14, 1, v17\n"
    "v_xor_b32 v19, v19, v14\n"
    "v_mfma_f32_16x16x32_bf16 v[228:231], a[8:11], a[28:31], 0\n"
    "v_lshl_add_u32 v18, v19, 4, v18\n"
    "v_and_b32 v19, 1, v17\n"
    "v_mfma_f32_16x16x32_bf16 v[232:235], a[8:11], a[32:35], 0\n"
    "ds_read_b64_tr_b16 a[8:9], v49 offset:8192\n"
    "ds_read_b64_tr_b16 a[10:11], v49 offset:8704\n"
    "s_waitcnt lgkmcnt(5)\n"
    "v_mfma_f32_16x16x32_bf16 v[236:239], a[12:15], a[16:19], 0\n"
    "s_add_u32 m0, s83, 65536\n"
    "s_add_u32 s96, s81, 256\n"
    "v_mfma_f32_16x16x32_bf16 v[240:243], a[12:15], a[20:23], 0\n"
    "buffer_load_dwordx4 v40, s[24:27], s96 offen lds\n"
    "v_mfma_f32_16x16x32_bf16 v[244:247], a[12:15], a[24:27], 0\n"
    "v_lshl_add_u32 v18, v19, 3, v18\n"
    "v_lshrrev_b32 v19, 1, v15\n"
    "v_mfma_f32_16x16x32_bf16 v[248:251], a[12:15], a[28:31], 0\n"
    "v_lshl_add_u32 v18, v19, 10, v18\n"
    "s_add_u32 s79, s80, 65536\n"
    "v_mfma_f32_16x16x32_bf16 v[252:255], a[12:15], a[32:35], 0\n"
    "s_add_u32 m0, s84, 4096\n"
    "s_add_u32 s92, s82, 786432\n"
    "v_mfma_f32_16x16x32_bf16 v[96:99], a[0:3], a[36:39], v[96:99]\n"
    "buffer_load_dwordx4 v41, s[28:31], s92 offen lds\n"
    "v_mfma_f32_16x16x32_bf16 v[100:103], a[0:3], a[40:43], v[100:103]\n"
    "ds_read_b64_tr_b16 a[12:13], v49 offset:8200\n"
    "ds_read_b64_tr_b16 a[14:15], v49 offset:8712\n"
    "v_mfma_f32_16x16x32_bf16 v[104:107], a[0:3], a[44:47], v[104:107]\n"
    "s_waitcnt lgkmcnt(0)\n"
    "s_barrier\n"
    "v_add_u32 v4, s79, v18\n"
    "v_mfma_f32_16x16x32_bf16 v[108:111], a[0:3], a[48:51], v[108:111]\n"
    "v_xor_b32 v5, 32, v4\n"
    "v_xor_b32 v6, 64, v4\n"
    "v_mfma_f32_16x16x32_bf16 v[112:115], a[0:3], a[52:55], v[112:115]\n"
    "buffer_load_dwordx4 v42, s[28:31], s92 offen offset:1024 lds\n"
    "v_mfma_f32_16x16x32_bf16 v[116:119], a[4:7], a[36:39], v[116:119]\n"
    "s_waitcnt vmcnt(11)\n"
    "ds_read_b64_tr_b16 a[0:1], v48 offset:12288\n"
    "v_mfma_f32_16x16x32_bf16 v[120:123], a[4:7], a[40:43], v[120:123]\n"
    "ds_read_b64_tr_b16 a[2:3], v48 offset:12800\n"
    "ds_read_b128 a[16:19], v46 offset:32768\n"
    "v_mfma_f32_16x16x32_bf16 v[124:127], a[4:7], a[44:47], v[124:127]\n"
    "v_xor_b32 v7, 0x60, v4\n"
    "v_lshlrev_b32 v18, 4, v0\n"
    "v_mfma_f32_16x16x32_bf16 v[128:131], a[4:7], a[48:51], v[128:131]\n"
    "v_lshlrev_b32 v19, 2, v0\n"
    "s_mul_i32 s79, %2, 5120\n"
    "v_mfma_f32_16x16x32_bf16 v[132:135], a[4:7], a[52:55], v[132:135]\n"
    "buffer_load_dwordx4 v43, s[28:31], s92 offen offset:2048 lds\n"
    "v_mfma_f32_16x16x32_bf16 v[136:139], a[8:11], a[36:39], v[136:139]\n"
    "ds_read_b64_tr_b16 a[4:5], v48 offset:12296\n"
    "ds_read_b64_tr_b16 a[6:7], v48 offset:12808\n"
    "v_mfma_f32_16x16x32_bf16 v[140:143], a[8:11], a[40:43], v[140:143]\n"
    "ds_read_b128 a[20:23], v46 offset:34816\n"
    "s_add_u32 s79, s79, %3\n"
    "v_mfma_f32_16x16x32_bf16 v[144:147], a[8:11], a[44:47], v[144:147]\n"
    "s_add_u32 s79, s79, 131072\n"
    "v_add_u32 v8, s79, v18\n"
    "v_mfma_f32_16x16x32_bf16 v[148:151], a[8:11], a[48:51], v[148:151]\n"
    "buffer_load_dwordx4 v44, s[28:31], s92 offen offset:3072 lds\n"
    "v_mfma_f32_16x16x32_bf16 v[152:155], a[8:11], a[52:55], v[152:155]\n"
    "ds_read_b64_tr_b16 a[8:9], v49 offset:12288\n"
    "ds_read_b64_tr_b16 a[10:11], v49 offset:12800\n"
    "v_mfma_f32_16x16x32_bf16 v[156:159], a[12:15], a[36:39], v[156:159]\n"
    "s_xor_b32 s80, %2, 1\n"
    "s_mul_i32 s79, s80, 5120\n"
    "v_mfma_f32_16x16x32_bf16 v[160:163], a[12:15], a[40:43], v[160:163]\n"
    "ds_read_b128 a[24:27], v46 offset:36864\n"
    "s_add_u32 s79, s79, %3\n"
    "v_mfma_f32_16x16x32_bf16 v[164:167], a[12:15], a[44:47], v[164:167]\n"
    "s_add_u32 m0, s83, 69632\n"
    "s_add_u32 s96, s81, 28928\n"
    "v_mfma_f32_16x16x32_bf16 v[168:171], a[12:15], a[48:51], v[168:171]\n"
    "buffer_load_dwordx4 v40, s[24:27], s96 offen lds\n"
    "v_mfma_f32_16x16x32_bf16 v[172:175], a[12:15], a[52:55], v[172:175]\n"
    "ds_read_b64_tr_b16 a[12:13], v49 offset:12296\n"
    "ds_read_b64_tr_b16 a[14:15], v49 offset:12808\n"
    "s_waitcnt lgkmcnt(6)\n"
    "v_mfma_f32_16x16x32_bf16 v[176:179], a[0:3], a[36:39], v[176:179]\n"
    "s_add_u32 s79, s79, 131072\n"
    "v_add_u32 v9, s79, v18\n"
    "v_mfma_f32_16x16x32_bf16 v[180:183], a[0:3], a[40:43], v[180:183]\n"
    "s_cmp_eq_u32 s60, 1\n"
    "s_cselect_b32 s62, 81920, s63\n"
    "s_add_u32 m0, s83, s62\n"
    "s_add_u32 s96, s81, 57600\n"
    "v_mfma_f32_16x16x32_bf16 v[184:187], a[0:3], a[44:47], v[184:187]\n"
    "buffer_load_dwordx4 v40, s[24:27], s96 offen lds\n"
    "v_mfma_f32_16x16x32_bf16 v[188:191], a[0:3], a[48:51], v[188:191]\n"
    "ds_read_b128 a[28:31], v46 offset:38912\n"
    "s_mul_i32 s79, %2, 3072\n"
    "v_mfma_f32_16x16x32_bf16 v[192:195], a[0:3], a[52:55], v[192:195]\n"
    "s_waitcnt vmcnt(11)\n"
    "ds_read_b64_tr_b16 a[0:1], v52 offset:0\n"
    "v_mfma_f32_16x16x32_bf16 v[196:199], a[4:7], a[36:39], v[196:199]\n"
    "s_add_u32 m0, s84, 8192\n"
    "s_add_u32 s92, s82, 786560\n"
    "v_mfma_f32_16x16x32_bf16 v[200:203], a[4:7], a[40:43], v[200:203]\n"
    "buffer_load_dwordx4 v41, s[28:31], s92 offen lds\n"
    "v_mfma_f32_16x16x32_bf16 v[204:207], a[4:7], a[44:47], v[204:207]\n"
    "ds_read_b64_tr_b16 a[2:3], v52 offset:512\n"
    "s_add_u32 s79, s79, %3\n"
    "v_mfma_f32_16x16x32_bf16 v[208:211], a[4:7], a[48:51], v[208:211]\n"
    "ds_read_b128 a[32:35], v46 offset:40960\n"
    "s_add_u32 s79, s79, 151552\n"
    "v_mfma_f32_16x16x32_bf16 v[212:215], a[4:7], a[52:55], v[212:215]\n"
    "ds_read_b64_tr_b16 a[4:5], v52 offset:8\n"
    "ds_read_b64_tr_b16 a[6:7], v52 offset:520\n"
    "s_waitcnt lgkmcnt(6)\n"
    "v_mfma_f32_16x16x32_bf16 v[216:219], a[8:11], a[36:39], v[216:219]\n"
    "v_add_u32 v12, s79, v19\n"
    "s_mul_i32 s79, s80, 3072\n"
    "v_mfma_f32_16x16x32_bf16 v[220:223], a[8:11], a[40:43], v[220:223]\n"
    "buffer_load_dwordx4 v42, s[28:31], s92 offen offset:1024 lds\n"
    "v_mfma_f32_16x16x32_bf16 v[224:227], a[8:11], a[44:47], v[224:227]\n"
    "s_add_u32 s79, s79, %3\n"
    "s_add_u32 s79, s79, 151552\n"
    "v_mfma_f32_16x16x32_bf16 v[228:231], a[8:11], a[48:51], v[228:231]\n"
    "v_add_u32 v13, s79, v19\n"
    "v_add_u32 v94, s84, v18\n"
    "v_mfma_f32_16x16x32_bf16 v[232:235], a[8:11], a[52:55], v[232:235]\n"
    "ds_read_b64_tr_b16 a[8:9], v53 offset:0\n"
    "ds_read_b64_tr_b16 a[10:11], v53 offset:512\n"
    "v_mfma_f32_16x16x32_bf16 v[236:239], a[12:15], a[36:39], v[236:239]\n"
    "buffer_load_dwordx4 v43, s[28:31], s92 offen offset:2048 lds\n"
    "v_mfma_f32_16x16x32_bf16 v[240:243], a[12:15], a[40:43], v[240:243]\n"
    "v_add_u32 v95, s61, v18\n"
    "s_lshl_b32 s79, s64, 15\n"
    "v_mfma_f32_16x16x32_bf16 v[244:247], a[12:15], a[44:47], v[244:247]\n"
    "s_add_u32 s79, s79, %3\n"
    "s_add_u32 s79, s79, 90112\n"
    "v_mfma_f32_16x16x32_bf16 v[248:251], a[12:15], a[48:51], v[248:251]\n"
    "v_add_u32 v90, s79, v18\n"
    "v_add_u32 v92, 0x4000, v90\n"
    "v_mfma_f32_16x16x32_bf16 v[252:255], a[12:15], a[52:55], v[252:255]\n"
    "ds_read_b64_tr_b16 a[12:13], v53 offset:8\n"
    "ds_read_b64_tr_b16 a[14:15], v53 offset:520\n"
    "s_waitcnt lgkmcnt(4)\n"
    "v_mfma_f32_16x16x32_bf16 v[96:99], a[0:3], a[16:19], v[96:99]\n"
    "buffer_load_dwordx4 v44, s[28:31], s92 offen offset:3072 lds\n"
    "v_mfma_f32_16x16x32_bf16 v[100:103], a[0:3], a[20:23], v[100:103]\n"
    "s_mov_b32 s88, 73728\n"
    "s_mov_b32 s89, 77824\n"
    "v_mfma_f32_16x16x32_bf16 v[104:107], a[0:3], a[24:27], v[104:107]\n"
    "s_cmp_eq_u32 s64, 0\n"
    "s_cmov_b32 s88, 26624\n"
    "s_cmov_b32 s89, 59392\n"
    "v_mfma_f32_16x16x32_bf16 v[108:111], a[0:3], a[28:31], v[108:111]\n"
    "s_add_u32 m0, s84, 12288\n"
    "s_add_u32 s92, s82, 1048576\n"
    "v_mfma_f32_16x16x32_bf16 v[112:115], a[0:3], a[32:35], v[112:115]\n"
    "buffer_load_dwordx4 v41, s[28:31], s92 offen lds\n"
    "v_mfma_f32_16x16x32_bf16 v[116:119], a[4:7], a[16:19], v[116:119]\n"
    "s_waitcnt vmcnt(12)\n"
    "ds_read_b64_tr_b16 a[0:1], v48 offset:0\n"
    "v_mfma_f32_16x16x32_bf16 v[120:123], a[4:7], a[20:23], v[120:123]\n"
    "ds_read_b64_tr_b16 a[2:3], v48 offset:512\n"
    "ds_read_b128 a[36:39], v47 offset:32768\n"
    "v_mfma_f32_16x16x32_bf16 v[124:127], a[4:7], a[24:27], v[124:127]\n"
    "v_add_u32 v18, %3, v18\n"
    "v_add_u32 v91, s88, v18\n"
    "v_mfma_f32_16x16x32_bf16 v[128:131], a[4:7], a[28:31], v[128:131]\n"
    "v_add_u32 v93, s89, v18\n"
    "s_lshl_b32 s79, s65, 14\n"
    "v_mfma_f32_16x16x32_bf16 v[132:135], a[4:7], a[32:35], v[132:135]\n"
    "buffer_load_dwordx4 v42, s[28:31], s92 offen offset:1024 lds\n"
    "s_waitcnt lgkmcnt(3)\n"
    "v_mfma_f32_16x16x32_bf16 v[136:139], a[8:11], a[16:19], v[136:139]\n"
    "ds_read_b64_tr_b16 a[4:5], v48 offset:8\n"
    "ds_read_b64_tr_b16 a[6:7], v48 offset:520\n"
    "v_mfma_f32_16x16x32_bf16 v[140:143], a[8:11], a[20:23], v[140:143]\n"
    "ds_read_b128 a[40:43], v47 offset:34816\n"
    "s_lshl_b32 s80, s64, 8\n"
    "v_mfma_f32_16x16x32_bf16 v[144:147], a[8:11], a[24:27], v[144:147]\n"
    "s_add_u32 s66, s79, s80\n"
    "s_add_u32 s67, s66, 128\n"
    "v_mfma_f32_16x16x32_bf16 v[148:151], a[8:11], a[28:31], v[148:151]\n"
    "buffer_load_dwordx4 v43, s[28:31], s92 offen offset:2048 lds\n"
    "v_mfma_f32_16x16x32_bf16 v[152:155], a[8:11], a[32:35], v[152:155]\n"
    "ds_read_b64_tr_b16 a[8:9], v49 offset:0\n"
    "ds_read_b64_tr_b16 a[10:11], v49 offset:512\n"
    "v_mfma_f32_16x16x32_bf16 v[156:159], a[12:15], a[16:19], v[156:159]\n"
    "s_lshl_b32 s80, s65, 1\n"
    "s_add_u32 s80, s80, s64\n"
    "v_mfma_f32_16x16x32_bf16 v[160:163], a[12:15], a[20:23], v[160:163]\n"
    "ds_read_b128 a[44:47], v47 offset:36864\n"
    "s_lshl_b32 s80, s80, 7\n"
    "v_mfma_f32_16x16x32_bf16 v[164:167], a[12:15], a[24:27], v[164:167]\n"
    "buffer_load_dwordx4 v44, s[28:31], s92 offen offset:3072 lds\n"
    "v_mfma_f32_16x16x32_bf16 v[168:171], a[12:15], a[28:31], v[168:171]\n"
    "s_add_u32 s68, s79, s80\n"
    "s_xor_b32 s69, s68, 16384\n"
    "v_mfma_f32_16x16x32_bf16 v[172:175], a[12:15], a[32:35], v[172:175]\n"
    "ds_read_b64_tr_b16 a[12:13], v49 offset:8\n"
    "ds_read_b64_tr_b16 a[14:15], v49 offset:520\n"
    "s_waitcnt lgkmcnt(6)\n"
    "v_mfma_f32_16x16x32_bf16 v[176:179], a[0:3], a[16:19], v[176:179]\n"
    "s_lshl_b32 s80, s64, 12\n"
    "s_add_u32 s80, s80, s79\n"
    "v_mfma_f32_16x16x32_bf16 v[180:183], a[0:3], a[20:23], v[180:183]\n"
    "s_add_u32 m0, s61, 0\n"
    "s_add_u32 s92, s82, 1048704\n"
    "v_mfma_f32_16x16x32_bf16 v[184:187], a[0:3], a[24:27], v[184:187]\n"
    "buffer_load_dwordx4 v41, s[28:31], s92 offen lds\n"
    "v_mfma_f32_16x16x32_bf16 v[188:191], a[0:3], a[28:31], v[188:191]\n"
    "ds_read_b128 a[48:51], v47 offset:38912\n"
    "s_add_u32 s80, s80, %3\n"
    "v_mfma_f32_16x16x32_bf16 v[192:195], a[0:3], a[32:35], v[192:195]\n"
    "s_waitcnt vmcnt(11)\n"
    "ds_read_b64_tr_b16 a[0:1], v48 offset:4096\n"
    "v_mfma_f32_16x16x32_bf16 v[196:199], a[4:7], a[16:19], v[196:199]\n"
    "ds_read_b64_tr_b16 a[2:3], v48 offset:4608\n"
    "s_add_u32 s71, s80, 65536\n"
    "v_mfma_f32_16x16x32_bf16 v[200:203], a[4:7], a[20:23], v[200:203]\n"
    "buffer_load_dwordx4 v42, s[28:31], s92 offen offset:1024 lds\n"
    "v_mfma_f32_16x16x32_bf16 v[204:207], a[4:7], a[24:27], v[204:207]\n"
    "s_add_u32 s72, s80, 98304\n"
    "s_lshl_b32 s79, s65, 15\n"
    "v_mfma_f32_16x16x32_bf16 v[208:211], a[4:7], a[28:31], v[208:211]\n"
    "ds_read_b128 a[52:55], v47 offset:40960\n"
    "s_lshl_b32 s80, s64, 13\n"
    "v_mfma_f32_16x16x32_bf16 v[212:215], a[4:7], a[32:35], v[212:215]\n"
    "ds_read_b64_tr_b16 a[4:5], v48 offset:4104\n"
    "ds_read_b64_tr_b16 a[6:7], v48 offset:4616\n"
    "s_waitcnt lgkmcnt(6)\n"
    "v_mfma_f32_16x16x32_bf16 v[216:219], a[8:11], a[16:19], v[216:219]\n"
    "s_add_u32 s70, s79, s80\n"
    "s_add_u32 s70, s70, %3\n"
    "v_mfma_f32_16x16x32_bf16 v[220:223], a[8:11], a[20:23], v[220:223]\n"
    "buffer_load_dwordx4 v43, s[28:31], s92 offen offset:2048 lds\n"
    "v_mfma_f32_16x16x32_bf16 v[224:227], a[8:11], a[24:27], v[224:227]\n"
    "s_mov_b32 s74, 0x41000000\n"
    "s_mov_b32 s73, 0x41000000\n"
    "v_mfma_f32_16x16x32_bf16 v[228:231], a[8:11], a[28:31], v[228:231]\n"
    "s_mov_b32 s85, 0x41000000\n"
    "s_mov_b32 s77, 0\n"
    "v_mfma_f32_16x16x32_bf16 v[232:235], a[8:11], a[32:35], v[232:235]\n"
    "ds_read_b64_tr_b16 a[8:9], v49 offset:4096\n"
    "ds_read_b64_tr_b16 a[10:11], v49 offset:4608\n"
    "v_mfma_f32_16x16x32_bf16 v[236:239], a[12:15], a[16:19], v[236:239]\n"
    "buffer_load_dwordx4 v44, s[28:31], s92 offen offset:3072 lds\n"
    "v_mfma_f32_16x16x32_bf16 v[240:243], a[12:15], a[20:23], v[240:243]\n"
    "s_mov_b32 s75, 6\n"
    "s_mov_b32 s86, 0x3db8aa3b\n"
    "v_mfma_f32_16x16x32_bf16 v[244:247], a[12:15], a[24:27], v[244:247]\n"
    "s_mov_b32 s87, 0x3db8aa3b\n"
    "s_mov_b32 s79, 0x3f803f80\n"
    "v_mfma_f32_16x16x32_bf16 v[248:251], a[12:15], a[28:31], v[248:251]\n"
    "s_add_u32 m0, s84, 0\n"
    "s_add_u32 s92, s82, 1310720\n"
    "v_mfma_f32_16x16x32_bf16 v[252:255], a[12:15], a[32:35], v[252:255]\n"
    "buffer_load_dwordx4 v41, s[28:31], s92 offen lds\n"
    "s_waitcnt lgkmcnt(2)\n"
    "v_mfma_f32_16x16x32_bf16 v[96:99], a[0:3], a[36:39], v[96:99]\n"
    "v_accvgpr_write_b32 a244, s79\n"
    "v_accvgpr_write_b32 a245, s79\n"
    "v_mfma_f32_16x16x32_bf16 v[100:103], a[0:3], a[40:43], v[100:103]\n"
    "v_accvgpr_write_b32 a246, s79\n"
    "v_accvgpr_write_b32 a247, s79\n"
    "v_mfma_f32_16x16x32_bf16 v[104:107], a[0:3], a[44:47], v[104:107]\n"
    "s_waitcnt vmcnt(13) lgkmcnt(0)\n"
    "s_barrier\n"
    "ds_read_b64_tr_b16 a[12:13], v49 offset:4104\n"
    "v_mfma_f32_16x16x32_bf16 v[108:111], a[0:3], a[48:51], v[108:111]\n"
    "ds_read_b64_tr_b16 a[14:15], v49 offset:4616\n"
    "v_mfma_f32_16x16x32_bf16 v[112:115], a[0:3], a[52:55], v[112:115]\n"
    "buffer_load_dwordx4 v42, s[28:31], s92 offen offset:1024 lds\n"
    "v_mfma_f32_16x16x32_bf16 v[116:119], a[4:7], a[36:39], v[116:119]\n"
    "s_waitcnt vmcnt(10)\n"
    "ds_read_b64_tr_b16 a[0:1], v48 offset:8192\n"
    "v_mfma_f32_16x16x32_bf16 v[120:123], a[4:7], a[40:43], v[120:123]\n"
    "ds_read_b64_tr_b16 a[2:3], v48 offset:8704\n"
    "ds_read_b128 a[16:19], v50 offset:0\n"
    "v_mfma_f32_16x16x32_bf16 v[124:127], a[4:7], a[44:47], v[124:127]\n"
    "v_mfma_f32_16x16x32_bf16 v[128:131], a[4:7], a[48:51], v[128:131]\n"
    "buffer_load_dwordx4 v43, s[28:31], s92 offen offset:2048 lds\n"
    "v_mfma_f32_16x16x32_bf16 v[132:135], a[4:7], a[52:55], v[132:135]\n"
    "ds_read_b64_tr_b16 a[4:5], v48 offset:8200\n"
    "ds_read_b64_tr_b16 a[6:7], v48 offset:8712\n"
    "v_mfma_f32_16x16x32_bf16 v[136:139], a[8:11], a[36:39], v[136:139]\n"
    "ds_read_b128 a[20:23], v50 offset:2048\n"
    "v_readfirstlane_b32 s76, v56\n"
    "v_mfma_f32_16x16x32_bf16 v[140:143], a[8:11], a[40:43], v[140:143]\n"
    "v_mov_b32 v22, v55\n"
    "v_mov_b32 v23, v54\n"
    "v_mfma_f32_16x16x32_bf16 v[144:147], a[8:11], a[44:47], v[144:147]\n"
    "v_mfma_f32_16x16x32_bf16 v[148:151], a[8:11], a[48:51], v[148:151]\n"
    "buffer_load_dwordx4 v44, s[28:31], s92 offen offset:3072 lds\n"
    "v_mfma_f32_16x16x32_bf16 v[152:155], a[8:11], a[52:55], v[152:155]\n"
    "ds_read_b64_tr_b16 a[8:9], v49 offset:8192\n"
    "ds_read_b64_tr_b16 a[10:11], v49 offset:8704\n"
    "s_waitcnt lgkmcnt(7)\n"
    "v_mfma_f32_16x16x32_bf16 v[156:159], a[12:15], a[36:39], v[156:159]\n"
    "v_mfma_f32_16x16x32_bf16 v[160:163], a[12:15], a[40:43], v[160:163]\n"
    "s_add_u32 m0, s83, 0\n"
    "s_add_u32 s96, s81, 384\n"
    "v_mfma_f32_16x16x32_bf16 v[164:167], a[12:15], a[44:47], v[164:167]\n"
    "buffer_load_dwordx4 v40, s[24:27], s96 offen lds\n"
    "v_mfma_f32_16x16x32_bf16 v[168:171], a[12:15], a[48:51], v[168:171]\n"
    "ds_read_b128 a[24:27], v50 offset:4096\n"
    "v_mfma_f32_16x16x32_bf16 v[172:175], a[12:15], a[52:55], v[172:175]\n"
    "ds_read_b64_tr_b16 a[12:13], v49 offset:8200\n"
    "ds_read_b64_tr_b16 a[14:15], v49 offset:8712\n"
    "s_waitcnt lgkmcnt(5)\n"
    "v_mfma_f32_16x16x32_bf16 v[176:179], a[0:3], a[36:39], v[176:179]\n"
    "s_add_u32 m0, s83, 4096\n"
    "s_add_u32 s96, s81, 29056\n"
    "v_mfma_f32_16x16x32_bf16 v[180:183], a[0:3], a[40:43], v[180:183]\n"
    "buffer_load_dwordx4 v40, s[24:27], s96 offen lds\n"
    "v_mfma_f32_16x16x32_bf16 v[184:187], a[0:3], a[44:47], v[184:187]\n"
    "ds_read_b128 a[28:31], v50 offset:6144\n"
    "v_mfma_f32_16x16x32_bf16 v[188:191], a[0:3], a[48:51], v[188:191]\n"
    "v_mfma_f32_16x16x32_bf16 v[192:195], a[0:3], a[52:55], v[192:195]\n"
    "s_waitcnt vmcnt(10)\n"
    "ds_read_b64_tr_b16 a[0:1], v48 offset:12288\n"
    "v_mfma_f32_16x16x32_bf16 v[196:199], a[4:7], a[36:39], v[196:199]\n"
    "s_cmp_eq_u32 s60, 1\n"
    "s_cselect_b32 s62, 8192, s63\n"
    "s_add_u32 m0, s83, s62\n"
    "s_add_u32 s96, s81, 57728\n"
    "v_mfma_f32_16x16x32_bf16 v[200:203], a[4:7], a[40:43], v[200:203]\n"
    "buffer_load_dwordx4 v40, s[24:27], s96 offen lds\n"
    "v_mfma_f32_16x16x32_bf16 v[204:207], a[4:7], a[44:47], v[204:207]\n"
    "ds_read_b64_tr_b16 a[2:3], v48 offset:12800\n"
    "v_mfma_f32_16x16x32_bf16 v[208:211], a[4:7], a[48:51], v[208:211]\n"
    "ds_read_b128 a[32:35], v50 offset:16384\n"
    "v_mfma_f32_16x16x32_bf16 v[212:215], a[4:7], a[52:55], v[212:215]\n"
    "s_add_u32 m0, s84, 4096\n"
    "s_add_u32 s92, s82, 1310848\n"
    "s_waitcnt lgkmcnt(4)\n"
    "v_mfma_f32_16x16x32_bf16 v[216:219], a[8:11], a[36:39], v[216:219]\n"
    "buffer_load_dwordx4 v41, s[28:31], s92 offen lds\n"
    "v_mfma_f32_16x16x32_bf16 v[220:223], a[8:11], a[40:43], v[220:223]\n"
    "ds_read_b64_tr_b16 a[4:5], v48 offset:12296\n"
    "ds_read_b64_tr_b16 a[6:7], v48 offset:12808\n"
    "v_mfma_f32_16x16x32_bf16 v[224:227], a[8:11], a[44:47], v[224:227]\n"
    "v_mfma_f32_16x16x32_bf16 v[228:231], a[8:11], a[48:51], v[228:231]\n"
    "v_mfma_f32_16x16x32_bf16 v[232:235], a[8:11], a[52:55], v[232:235]\n"
    "ds_read_b64_tr_b16 a[8:9], v49 offset:12288\n"
    "ds_read_b64_tr_b16 a[10:11], v49 offset:12800\n"
    "v_mfma_f32_16x16x32_bf16 v[236:239], a[12:15], a[36:39], v[236:239]\n"
    "buffer_load_dwordx4 v42, s[28:31], s92 offen offset:1024 lds\n"
    "v_mfma_f32_16x16x32_bf16 v[240:243], a[12:15], a[40:43], v[240:243]\n"
    "v_mfma_f32_16x16x32_bf16 v[244:247], a[12:15], a[44:47], v[244:247]\n"
    "v_mfma_f32_16x16x32_bf16 v[248:251], a[12:15], a[48:51], v[248:251]\n"
    "v_mfma_f32_16x16x32_bf16 v[252:255], a[12:15], a[52:55], v[252:255]\n"
    "buffer_load_dwordx4 v43, s[28:31], s92 offen offset:2048 lds\n"
    "s_waitcnt lgkmcnt(4)\n"
    "v_mfma_f32_16x16x32_bf16 v[96:99], a[0:3], a[16:19], v[96:99]\n"
    "ds_read_b64_tr_b16 a[12:13], v49 offset:12296\n"
    "ds_read_b64_tr_b16 a[14:15], v49 offset:12808\n"
    "v_mfma_f32_16x16x32_bf16 v[100:103], a[0:3], a[20:23], v[100:103]\n"
    "v_mfma_f32_16x16x32_bf16 v[104:107], a[0:3], a[24:27], v[104:107]\n"
    "v_mfma_f32_16x16x32_bf16 v[108:111], a[0:3], a[28:31], v[108:111]\n"
    "buffer_load_dwordx4 v44, s[28:31], s92 offen offset:3072 lds\n"
    "v_mfma_f32_16x16x32_bf16 v[112:115], a[0:3], a[32:35], v[112:115]\n"
    "s_waitcnt vmcnt(11)\n"
    "ds_read_b64_tr_b16 a[0:1], v52 offset:0\n"
    "s_waitcnt lgkmcnt(3)\n"
    "v_mfma_f32_16x16x32_bf16 v[116:119], a[4:7], a[16:19], v[116:119]\n"
    "ds_read_b64_tr_b16 a[2:3], v52 offset:512\n"
    "ds_read_b128 a[36:39], v51 offset:0\n"
    "v_mfma_f32_16x16x32_bf16 v[120:123], a[4:7], a[20:23], v[120:123]\n"
    "v_mfma_f32_16x16x32_bf16 v[124:127], a[4:7], a[24:27], v[124:127]\n"
    "s_add_u32 m0, s84, 8192\n"
    "s_add_u32 s92, s82, 1572864\n"
    "v_mfma_f32_16x16x32_bf16 v[128:131], a[4:7], a[28:31], v[128:131]\n"
    "buffer_load_dwordx4 v41, s[28:31], s92 offen lds\n"
    "v_mfma_f32_16x16x32_bf16 v[132:135], a[4:7], a[32:35], v[132:135]\n"
    "ds_read_b64_tr_b16 a[4:5], v52 offset:8\n"
    "ds_read_b64_tr_b16 a[6:7], v52 offset:520\n"
    "v_mfma_f32_16x16x32_bf16 v[136:139], a[8:11], a[16:19], v[136:139]\n"
    "ds_read_b128 a[40:43], v51 offset:2048\n"
    "v_mfma_f32_16x16x32_bf16 v[140:143], a[8:11], a[20:23], v[140:143]\n"
    "v_mfma_f32_16x16x32_bf16 v[144:147], a[8:11], a[24:27], v[144:147]\n"
    "buffer_load_dwordx4 v42, s[28:31], s92 offen offset:1024 lds\n"
    "v_mfma_f32_16x16x32_bf16 v[148:151], a[8:11], a[28:31], v[148:151]\n"
    "v_mfma_f32_16x16x32_bf16 v[152:155], a[8:11], a[32:35], v[152:155]\n"
    "ds_read_b64_tr_b16 a[8:9], v53 offset:0\n"
    "ds_read_b64_tr_b16 a[10:11], v53 offset:512\n"
    "s_waitcnt lgkmcnt(5)\n"
    "v_mfma_f32_16x16x32_bf16 v[156:159], a[12:15], a[16:19], v[156:159]\n"
    "v_mfma_f32_16x16x32_bf16 v[160:163], a[12:15], a[20:23], v[160:163]\n"
    "ds_read_b128 a[44:47], v51 offset:4096\n"
    "v_mfma_f32_16x16x32_bf16 v[164:167], a[12:15], a[24:27], v[164:167]\n"
    "buffer_load_dwordx4 v43, s[28:31], s92 offen offset:2048 lds\n"
    "v_mfma_f32_16x16x32_bf16 v[168:171], a[12:15], a[28:31], v[168:171]\n"
    "v_mfma_f32_16x16x32_bf16 v[172:175], a[12:15], a[32:35], v[172:175]\n"
    "ds_read_b64_tr_b16 a[12:13], v53 offset:8\n"
    "ds_read_b64_tr_b16 a[14:15], v53 offset:520\n"
    "v_mfma_f32_16x16x32_bf16 v[176:179], a[0:3], a[16:19], v[176:179]\n"
    "v_mfma_f32_16x16x32_bf16 v[180:183], a[0:3], a[20:23], v[180:183]\n"
    "buffer_load_dwordx4 v44, s[28:31], s92 offen offset:3072 lds\n"
    "v_mfma_f32_16x16x32_bf16 v[184:187], a[0:3], a[24:27], v[184:187]\n"
    "ds_read_b128 a[48:51], v51 offset:6144\n"
    "v_mfma_f32_16x16x32_bf16 v[188:191], a[0:3], a[28:31], v[188:191]\n"
    "v_mfma_f32_16x16x32_bf16 v[192:195], a[0:3], a[32:35], v[192:195]\n"
    "s_add_u32 m0, s84, 12288\n"
    "s_add_u32 s92, s82, 1572992\n"
    "s_waitcnt lgkmcnt(4)\n"
    "v_mfma_f32_16x16x32_bf16 v[196:199], a[4:7], a[16:19], v[196:199]\n"
    "buffer_load_dwordx4 v41, s[28:31], s92 offen lds\n"
    "v_mfma_f32_16x16x32_bf16 v[200:203], a[4:7], a[20:23], v[200:203]\n"
    "s_waitcnt vmcnt(12)\n"
    "ds_read_b64_tr_b16 a[0:1], v48 offset:0\n"
    "v_mfma_f32_16x16x32_bf16 v[204:207], a[4:7], a[24:27], v[204:207]\n"
    "ds_read_b64_tr_b16 a[2:3], v48 offset:512\n"
    "v_mfma_f32_16x16x32_bf16 v[208:211], a[4:7], a[28:31], v[208:211]\n"
    "ds_read_b128 a[52:55], v51 offset:16384\n"
    "v_mfma_f32_16x16x32_bf16 v[212:215], a[4:7], a[32:35], v[212:215]\n"
    "ds_read_b64_tr_b16 a[4:5], v48 offset:8\n"
    "ds_read_b64_tr_b16 a[6:7], v48 offset:520\n"
    "v_mfma_f32_16x16x32_bf16 v[216:219], a[8:11], a[16:19], v[216:219]\n"
    "buffer_load_dwordx4 v42, s[28:31], s92 offen offset:1024 lds\n"
    "v_mfma_f32_16x16x32_bf16 v[220:223], a[8:11], a[20:23], v[220:223]\n"
    "v_mfma_f32_16x16x32_bf16 v[224:227], a[8:11], a[24:27], v[224:227]\n"
    "v_mfma_f32_16x16x32_bf16 v[228:231], a[8:11], a[28:31], v[228:231]\n"
    "v_mfma_f32_16x16x32_bf16 v[232:235], a[8:11], a[32:35], v[232:235]\n"
    "buffer_load_dwordx4 v43, s[28:31], s92 offen offset:2048 lds\n"
    "s_waitcnt lgkmcnt(5)\n"
    "v_mfma_f32_16x16x32_bf16 v[236:239], a[12:15], a[16:19], v[236:239]\n"
    "ds_read_b64_tr_b16 a[8:9], v49 offset:0\n"
    "ds_read_b64_tr_b16 a[10:11], v49 offset:512\n"
    "v_mfma_f32_16x16x32_bf16 v[240:243], a[12:15], a[20:23], v[240:243]\n"
    "v_mfma_f32_16x16x32_bf16 v[244:247], a[12:15], a[24:27], v[244:247]\n"
    "v_mfma_f32_16x16x32_bf16 v[248:251], a[12:15], a[28:31], v[248:251]\n"
    "v_mfma_f32_16x16x32_bf16 v[252:255], a[12:15], a[32:35], v[252:255]\n"
    "buffer_load_dwordx4 v44, s[28:31], s92 offen offset:3072 lds\n"
    "s_waitcnt lgkmcnt(2)\n"
    "v_mfma_f32_16x16x32_bf16 v[96:99], a[0:3], a[36:39], v[96:99]\n"
    "ds_read_b64_tr_b16 a[12:13], v49 offset:8\n"
    "ds_read_b64_tr_b16 a[14:15], v49 offset:520\n"
    "v_mfma_f32_16x16x32_bf16 v[100:103], a[0:3], a[40:43], v[100:103]\n"
    "v_mfma_f32_16x16x32_bf16 v[104:107], a[0:3], a[44:47], v[104:107]\n"
    "s_waitcnt vmcnt(12) lgkmcnt(0)\n"
    "s_barrier\n"
    "s_add_u32 m0, s61, 0\n"
    "s_add_u32 s92, s82, 1835008\n"
    "v_mfma_f32_16x16x32_bf16 v[108:111], a[0:3], a[48:51], v[108:111]\n"
    "buffer_load_dwordx4 v41, s[28:31], s92 offen lds\n"
    "v_mfma_f32_16x16x32_bf16 v[112:115], a[0:3], a[52:55], v[112:115]\n"
    "s_waitcnt vmcnt(9)\n"
    "ds_read_b64_tr_b16 a[0:1], v48 offset:4096\n"
    "v_mfma_f32_16x16x32_bf16 v[116:119], a[4:7], a[36:39], v[116:119]\n"
    "ds_read_b64_tr_b16 a[2:3], v48 offset:4608\n"
    "ds_read_b128 a[16:19], v46 offset:0\n"
    "v_mfma_f32_16x16x32_bf16 v[120:123], a[4:7], a[40:43], v[120:123]\n"
    "v_mfma_f32_16x16x32_bf16 v[124:127], a[4:7], a[44:47], v[124:127]\n"
    "buffer_load_dwordx4 v42, s[28:31], s92 offen offset:1024 lds\n"
    "v_mfma_f32_16x16x32_bf16 v[128:131], a[4:7], a[48:51], v[128:131]\n"
    "v_mfma_f32_16x16x32_bf16 v[132:135], a[4:7], a[52:55], v[132:135]\n"
    "ds_read_b64_tr_b16 a[4:5], v48 offset:4104\n"
    "ds_read_b64_tr_b16 a[6:7], v48 offset:4616\n"
    "v_mfma_f32_16x16x32_bf16 v[136:139], a[8:11], a[36:39], v[136:139]\n"
    "ds_read_b128 a[20:23], v46 offset:2048\n"
    "v_mfma_f32_16x16x32_bf16 v[140:143], a[8:11], a[40:43], v[140:143]\n"
    "v_mfma_f32_16x16x32_bf16 v[144:147], a[8:11], a[44:47], v[144:147]\n"
    "buffer_load_dwordx4 v43, s[28:31], s92 offen offset:2048 lds\n"
    "v_mfma_f32_16x16x32_bf16 v[148:151], a[8:11], a[48:51], v[148:151]\n"
    "v_mfma_f32_16x16x32_bf16 v[152:155], a[8:11], a[52:55], v[152:155]\n"
    "ds_read_b64_tr_b16 a[8:9], v49 offset:4096\n"
    "ds_read_b64_tr_b16 a[10:11], v49 offset:4608\n"
    "v_mfma_f32_16x16x32_bf16 v[156:159], a[12:15], a[36:39], v[156:159]\n"
    "v_mfma_f32_16x16x32_bf16 v[160:163], a[12:15], a[40:43], v[160:163]\n"
    "buffer_load_dwordx4 v44, s[28:31], s92 offen offset:3072 lds\n"
    "v_mfma_f32_16x16x32_bf16 v[164:167], a[12:15], a[44:47], v[164:167]\n"
    "ds_read_b128 a[24:27], v46 offset:4096\n"
    "v_mfma_f32_16x16x32_bf16 v[168:171], a[12:15], a[48:51], v[168:171]\n"
    "v_mfma_f32_16x16x32_bf16 v[172:175], a[12:15], a[52:55], v[172:175]\n"
    "ds_read_b64_tr_b16 a[12:13], v49 offset:4104\n"
    "ds_read_b64_tr_b16 a[14:15], v49 offset:4616\n"
    "s_waitcnt lgkmcnt(5)\n"
    "v_mfma_f32_16x16x32_bf16 v[176:179], a[0:3], a[36:39], v[176:179]\n"
    "s_add_u32 m0, s83, 32768\n"
    "s_add_u32 s96, s81, 512\n"
    "v_mfma_f32_16x16x32_bf16 v[180:183], a[0:3], a[40:43], v[180:183]\n"
    "buffer_load_dwordx4 v40, s[24:27], s96 offen lds\n"
    "v_mfma_f32_16x16x32_bf16 v[184:187], a[0:3], a[44:47], v[184:187]\n"
    "ds_read_b128 a[28:31], v46 offset:6144\n"
    "v_mfma_f32_16x16x32_bf16 v[188:191], a[0:3], a[48:51], v[188:191]\n"
    "v_mfma_f32_16x16x32_bf16 v[192:195], a[0:3], a[52:55], v[192:195]\n"
    "s_add_u32 m0, s83, 36864\n"
    "s_add_u32 s96, s81, 29184\n"
    "v_mfma_f32_16x16x32_bf16 v[196:199], a[4:7], a[36:39], v[196:199]\n"
    "buffer_load_dwordx4 v40, s[24:27], s96 offen lds\n"
    "v_mfma_f32_16x16x32_bf16 v[200:203], a[4:7], a[40:43], v[200:203]\n"
    "s_waitcnt vmcnt(10)\n"
    "ds_read_b64_tr_b16 a[0:1], v48 offset:8192\n"
    "v_mfma_f32_16x16x32_bf16 v[204:207], a[4:7], a[44:47], v[204:207]\n"
    "ds_read_b64_tr_b16 a[2:3], v48 offset:8704\n"
    "v_mfma_f32_16x16x32_bf16 v[208:211], a[4:7], a[48:51], v[208:211]\n"
    "s_cmp_eq_u32 s60, 1\n"
    "s_cselect_b32 s62, 40960, s63\n"
    "s_add_u32 m0, s83, s62\n"
    "s_add_u32 s96, s81, 57856\n"
    "v_mfma_f32_16x16x32_bf16 v[212:215], a[4:7], a[52:55], v[212:215]\n"
    "buffer_load_dwordx4 v40, s[24:27], s96 offen lds\n"
    "s_waitcnt lgkmcnt(3)\n"
    "v_mfma_f32_16x16x32_bf16 v[216:219], a[8:11], a[36:39], v[216:219]\n"
    "ds_read_b128 a[32:35], v46 offset:8192\n"
    "ds_read_b64_tr_b16 a[4:5], v48 offset:8200\n"
    "v_mfma_f32_16x16x32_bf16 v[220:223], a[8:11], a[40:43], v[220:223]\n"
    "ds_read_b64_tr_b16 a[6:7], v48 offset:8712\n"
    "v_mfma_f32_16x16x32_bf16 v[224:227], a[8:11], a[44:47], v[224:227]\n"
    "v_mfma_f32_16x16x32_bf16 v[228:231], a[8:11], a[48:51], v[228:231]\n"
    "s_add_u32 m0, s84, 0\n"
    "s_add_u32 s92, s82, 1835136\n"
    "v_mfma_f32_16x16x32_bf16 v[232:235], a[8:11], a[52:55], v[232:235]\n"
    "buffer_load_dwordx4 v41, s[28:31], s92 offen lds\n"
    "v_mfma_f32_16x16x32_bf16 v[236:239], a[12:15], a[36:39], v[236:239]\n"
    "ds_read_b64_tr_b16 a[8:9], v49 offset:8192\n"
    "ds_read_b64_tr_b16 a[10:11], v49 offset:8704\n"
    "v_mfma_f32_16x16x32_bf16 v[240:243], a[12:15], a[40:43], v[240:243]\n"
    "v_mfma_f32_16x16x32_bf16 v[244:247], a[12:15], a[44:47], v[244:247]\n"
    "v_mfma_f32_16x16x32_bf16 v[248:251], a[12:15], a[48:51], v[248:251]\n"
    "buffer_load_dwordx4 v42, s[28:31], s92 offen offset:1024 lds\n"
    "v_mfma_f32_16x16x32_bf16 v[252:255], a[12:15], a[52:55], v[252:255]\n"
    "ds_read_b64_tr_b16 a[12:13], v49 offset:8200\n"
    "ds_read_b64_tr_b16 a[14:15], v49 offset:8712\n"
    "s_waitcnt lgkmcnt(5)\n"
    "v_mfma_f32_16x16x32_bf16 v[96:99], a[0:3], a[16:19], v[96:99]\n"
    "v_mfma_f32_16x16x32_bf16 v[100:103], a[0:3], a[20:23], v[100:103]\n"
    "v_mfma_f32_16x16x32_bf16 v[104:107], a[0:3], a[24:27], v[104:107]\n"
    "v_mfma_f32_16x16x32_bf16 v[108:111], a[0:3], a[28:31], v[108:111]\n"
    "buffer_load_dwordx4 v43, s[28:31], s92 offen offset:2048 lds\n"
    "v_mfma_f32_16x16x32_bf16 v[112:115], a[0:3], a[32:35], v[112:115]\n"
    "s_waitcnt vmcnt(10)\n"
    "ds_read_b64_tr_b16 a[0:1], v48 offset:12288\n"
    "s_waitcnt lgkmcnt(3)\n"
    "v_mfma_f32_16x16x32_bf16 v[116:119], a[4:7], a[16:19], v[116:119]\n"
    "ds_read_b64_tr_b16 a[2:3], v48 offset:12800\n"
    "ds_read_b128 a[36:39], v47 offset:0\n"
    "v_mfma_f32_16x16x32_bf16 v[120:123], a[4:7], a[20:23], v[120:123]\n"
    "v_mfma_f32_16x16x32_bf16 v[124:127], a[4:7], a[24:27], v[124:127]\n"
    "buffer_load_dwordx4 v44, s[28:31], s92 offen offset:3072 lds\n"
    "v_mfma_f32_16x16x32_bf16 v[128:131], a[4:7], a[28:31], v[128:131]\n"
    "v_mfma_f32_16x16x32_bf16 v[132:135], a[4:7], a[32:35], v[132:135]\n"
    "ds_read_b64_tr_b16 a[4:5], v48 offset:12296\n"
    "ds_read_b64_tr_b16 a[6:7], v48 offset:12808\n"
    "v_mfma_f32_16x16x32_bf16 v[136:139], a[8:11], a[16:19], v[136:139]\n"
    "s_add_u32 m0, s84, 4096\n"
    "s_add_u32 s92, s82, 2097152\n"
    "v_mfma_f32_16x16x32_bf16 v[140:143], a[8:11], a[20:23], v[140:143]\n"
    "buffer_load_dwordx4 v41, s[28:31], s92 offen lds\n"
    "v_mfma_f32_16x16x32_bf16 v[144:147], a[8:11], a[24:27], v[144:147]\n"
    "ds_read_b128 a[40:43], v47 offset:2048\n"
    "v_mfma_f32_16x16x32_bf16 v[148:151], a[8:11], a[28:31], v[148:151]\n"
    "v_mfma_f32_16x16x32_bf16 v[152:155], a[8:11], a[32:35], v[152:155]\n"
    "ds_read_b64_tr_b16 a[8:9], v49 offset:12288\n"
    "ds_read_b64_tr_b16 a[10:11], v49 offset:12800\n"
    "s_waitcnt lgkmcnt(5)\n"
    "v_mfma_f32_16x16x32_bf16 v[156:159], a[12:15], a[16:19], v[156:159]\n"
    "v_mfma_f32_16x16x32_bf16 v[160:163], a[12:15], a[20:23], v[160:163]\n"
    "buffer_load_dwordx4 v42, s[28:31], s92 offen offset:1024 lds\n"
    "v_mfma_f32_16x16x32_bf16 v[164:167], a[12:15], a[24:27], v[164:167]\n"
    "ds_read_b128 a[44:47], v47 offset:4096\n"
    "v_mfma_f32_16x16x32_bf16 v[168:171], a[12:15], a[28:31], v[168:171]\n"
    "v_mfma_f32_16x16x32_bf16 v[172:175], a[12:15], a[32:35], v[172:175]\n"
    "ds_read_b64_tr_b16 a[12:13], v49 offset:12296\n"
    "ds_read_b64_tr_b16 a[14:15], v49 offset:12808\n"
    "v_mfma_f32_16x16x32_bf16 v[176:179], a[0:3], a[16:19], v[176:179]\n"
    "buffer_load_dwordx4 v43, s[28:31], s92 offen offset:2048 lds\n"
    "v_mfma_f32_16x16x32_bf16 v[180:183], a[0:3], a[20:23], v[180:183]\n"
    "v_mfma_f32_16x16x32_bf16 v[184:187], a[0:3], a[24:27], v[184:187]\n"
    "ds_read_b128 a[48:51], v47 offset:6144\n"
    "v_mfma_f32_16x16x32_bf16 v[188:191], a[0:3], a[28:31], v[188:191]\n"
    "v_mfma_f32_16x16x32_bf16 v[192:195], a[0:3], a[32:35], v[192:195]\n"
    "s_waitcnt vmcnt(10)\n"
    "ds_read_b64_tr_b16 a[0:1], v52 offset:0\n"
    "s_waitcnt lgkmcnt(5)\n"
    "v_mfma_f32_16x16x32_bf16 v[196:199], a[4:7], a[16:19], v[196:199]\n"
    "buffer_load_dwordx4 v44, s[28:31], s92 offen offset:3072 lds\n"
    "v_mfma_f32_16x16x32_bf16 v[200:203], a[4:7], a[20:23], v[200:203]\n"
    "ds_read_b64_tr_b16 a[2:3], v52 offset:512\n"
    "v_mfma_f32_16x16x32_bf16 v[204:207], a[4:7], a[24:27], v[204:207]\n"
    "v_mfma_f32_16x16x32_bf16 v[208:211], a[4:7], a[28:31], v[208:211]\n"
    "s_add_u32 m0, s84, 8192\n"
    "s_add_u32 s92, s82, 2097280\n"
    "v_mfma_f32_16x16x32_bf16 v[212:215], a[4:7], a[32:35], v[212:215]\n"
    "buffer_load_dwordx4 v41, s[28:31], s92 offen lds\n"
    "v_mfma_f32_16x16x32_bf16 v[216:219], a[8:11], a[16:19], v[216:219]\n"
    "ds_read_b128 a[52:55], v47 offset:8192\n"
    "ds_read_b64_tr_b16 a[4:5], v52 offset:8\n"
    "v_mfma_f32_16x16x32_bf16 v[220:223], a[8:11], a[20:23], v[220:223]\n"
    "ds_read_b64_tr_b16 a[6:7], v52 offset:520\n"
    "v_mfma_f32_16x16x32_bf16 v[224:227], a[8:11], a[24:27], v[224:227]\n"
    "v_mfma_f32_16x16x32_bf16 v[228:231], a[8:11], a[28:31], v[228:231]\n"
    "buffer_load_dwordx4 v42, s[28:31], s92 offen offset:1024 lds\n"
    "v_mfma_f32_16x16x32_bf16 v[232:235], a[8:11], a[32:35], v[232:235]\n"
    "ds_read_b64_tr_b16 a[8:9], v53 offset:0\n"
    "ds_read_b64_tr_b16 a[10:11], v53 offset:512\n"
    "s_waitcnt lgkmcnt(6)\n"
    "v_mfma_f32_16x16x32_bf16 v[236:239], a[12:15], a[16:19], v[236:239]\n"
    "v_mfma_f32_16x16x32_bf16 v[240:243], a[12:15], a[20:23], v[240:243]\n"
    "v_mfma_f32_16x16x32_bf16 v[244:247], a[12:15], a[24:27], v[244:247]\n"
    "v_mfma_f32_16x16x32_bf16 v[248:251], a[12:15], a[28:31], v[248:251]\n"
    "buffer_load_dwordx4 v43, s[28:31], s92 offen offset:2048 lds\n"
    "v_mfma_f32_16x16x32_bf16 v[252:255], a[12:15], a[32:35], v[252:255]\n"
    "ds_read_b64_tr_b16 a[12:13], v53 offset:8\n"
    "ds_read_b64_tr_b16 a[14:15], v53 offset:520\n"
    "s_waitcnt lgkmcnt(4)\n"
    "v_mfma_f32_16x16x32_bf16 v[96:99], a[0:3], a[36:39], v[96:99]\n"
    "v_mfma_f32_16x16x32_bf16 v[100:103], a[0:3], a[40:43], v[100:103]\n"
    "v_mfma_f32_16x16x32_bf16 v[104:107], a[0:3], a[44:47], v[104:107]\n"
    "s_waitcnt vmcnt(11) lgkmcnt(0)\n"
    "s_barrier\n"
    "buffer_load_dwordx4 v44, s[28:31], s92 offen offset:3072 lds\n"
    "v_mfma_f32_16x16x32_bf16 v[108:111], a[0:3], a[48:51], v[108:111]\n"
    "v_mfma_f32_16x16x32_bf16 v[112:115], a[0:3], a[52:55], v[112:115]\n"
    "s_waitcnt vmcnt(8)\n"
    "ds_read_b64_tr_b16 a[0:1], v48 offset:0\n"
    "v_mfma_f32_16x16x32_bf16 v[116:119], a[4:7], a[36:39], v[116:119]\n"
    "ds_read_b64_tr_b16 a[2:3], v48 offset:512\n"
    "ds_read_b128 a[16:19], v46 offset:32768\n"
    "v_mfma_f32_16x16x32_bf16 v[120:123], a[4:7], a[40:43], v[120:123]\n"
    "s_add_u32 m0, s84, 12288\n"
    "s_add_u32 s92, s82, 2359296\n"
    "v_mfma_f32_16x16x32_bf16 v[124:127], a[4:7], a[44:47], v[124:127]\n"
    "buffer_load_dwordx4 v41, s[28:31], s92 offen lds\n"
    "v_mfma_f32_16x16x32_bf16 v[128:131], a[4:7], a[48:51], v[128:131]\n"
    "v_mfma_f32_16x16x32_bf16 v[132:135], a[4:7], a[52:55], v[132:135]\n"
    "ds_read_b64_tr_b16 a[4:5], v48 offset:8\n"
    "ds_read_b64_tr_b16 a[6:7], v48 offset:520\n"
    "v_mfma_f32_16x16x32_bf16 v[136:139], a[8:11], a[36:39], v[136:139]\n"
    "ds_read_b128 a[20:23], v46 offset:34816\n"
    "v_mfma_f32_16x16x32_bf16 v[140:143], a[8:11], a[40:43], v[140:143]\n"
    "buffer_load_dwordx4 v42, s[28:31], s92 offen offset:1024 lds\n"
    "v_mfma_f32_16x16x32_bf16 v[144:147], a[8:11], a[44:47], v[144:147]\n"
    "v_mfma_f32_16x16x32_bf16 v[148:151], a[8:11], a[48:51], v[148:151]\n"
    "v_mfma_f32_16x16x32_bf16 v[152:155], a[8:11], a[52:55], v[152:155]\n"
    "ds_read_b64_tr_b16 a[8:9], v49 offset:0\n"
    "ds_read_b64_tr_b16 a[10:11], v49 offset:512\n"
    "v_mfma_f32_16x16x32_bf16 v[156:159], a[12:15], a[36:39], v[156:159]\n"
    "buffer_load_dwordx4 v43, s[28:31], s92 offen offset:2048 lds\n"
    "v_mfma_f32_16x16x32_bf16 v[160:163], a[12:15], a[40:43], v[160:163]\n"
    "ds_read_b128 a[24:27], v46 offset:36864\n"
    "v_mfma_f32_16x16x32_bf16 v[164:167], a[12:15], a[44:47], v[164:167]\n"
    "v_mfma_f32_16x16x32_bf16 v[168:171], a[12:15], a[48:51], v[168:171]\n"
    "v_mfma_f32_16x16x32_bf16 v[172:175], a[12:15], a[52:55], v[172:175]\n"
    "ds_read_b64_tr_b16 a[12:13], v49 offset:8\n"
    "ds_read_b64_tr_b16 a[14:15], v49 offset:520\n"
    "s_waitcnt lgkmcnt(5)\n"
    "v_mfma_f32_16x16x32_bf16 v[176:179], a[0:3], a[36:39], v[176:179]\n"
    "buffer_load_dwordx4 v44, s[28:31], s92 offen offset:3072 lds\n"
    "v_mfma_f32_16x16x32_bf16 v[180:183], a[0:3], a[40:43], v[180:183]\n"
    "v_mfma_f32_16x16x32_bf16 v[184:187], a[0:3], a[44:47], v[184:187]\n"
    "ds_read_b128 a[28:31], v46 offset:38912\n"
    "v_mfma_f32_16x16x32_bf16 v[188:191], a[0:3], a[48:51], v[188:191]\n"
    "s_add_u32 m0, s83, 16384\n"
    "s_add_u32 s96, s81, 640\n"
    "v_mfma_f32_16x16x32_bf16 v[192:195], a[0:3], a[52:55], v[192:195]\n"
    "buffer_load_dwordx4 v40, s[24:27], s96 offen lds\n"
    "v_mfma_f32_16x16x32_bf16 v[196:199], a[4:7], a[36:39], v[196:199]\n"
    "s_waitcnt vmcnt(9)\n"
    "ds_read_b64_tr_b16 a[0:1], v48 offset:4096\n"
    "v_mfma_f32_16x16x32_bf16 v[200:203], a[4:7], a[40:43], v[200:203]\n"
    "ds_read_b64_tr_b16 a[2:3], v48 offset:4608\n"
    "v_mfma_f32_16x16x32_bf16 v[204:207], a[4:7], a[44:47], v[204:207]\n"
    "s_add_u32 m0, s83, 20480\n"
    "s_add_u32 s96, s81, 29312\n"
    "v_mfma_f32_16x16x32_bf16 v[208:211], a[4:7], a[48:51], v[208:211]\n"
    "buffer_load_dwordx4 v40, s[24:27], s96 offen lds\n"
    "v_mfma_f32_16x16x32_bf16 v[212:215], a[4:7], a[52:55], v[212:215]\n"
    "ds_read_b128 a[32:35], v46 offset:40960\n"
    "ds_read_b64_tr_b16 a[4:5], v48 offset:4104\n"
    "s_waitcnt lgkmcnt(5)\n"
    "v_mfma_f32_16x16x32_bf16 v[216:219], a[8:11], a[36:39], v[216:219]\n"
    "ds_read_b64_tr_b16 a[6:7], v48 offset:4616\n"
    "v_mfma_f32_16x16x32_bf16 v[220:223], a[8:11], a[40:43], v[220:223]\n"
    "v_mfma_f32_16x16x32_bf16 v[224:227], a[8:11], a[44:47], v[224:227]\n"
    "s_cmp_eq_u32 s60, 1\n"
    "s_cselect_b32 s62, 24576, s63\n"
    "s_add_u32 m0, s83, s62\n"
    "s_add_u32 s96, s81, 57984\n"
    "v_mfma_f32_16x16x32_bf16 v[228:231], a[8:11], a[48:51], v[228:231]\n"
    "buffer_load_dwordx4 v40, s[24:27], s96 offen lds\n"
    "v_mfma_f32_16x16x32_bf16 v[232:235], a[8:11], a[52:55], v[232:235]\n"
    "ds_read_b64_tr_b16 a[8:9], v49 offset:4096\n"
    "ds_read_b64_tr_b16 a[10:11], v49 offset:4608\n"
    "v_mfma_f32_16x16x32_bf16 v[236:239], a[12:15], a[36:39], v[236:239]\n"
    "v_mfma_f32_16x16x32_bf16 v[240:243], a[12:15], a[40:43], v[240:243]\n"
    "s_add_u32 m0, s83, 49152\n"
    "s_add_u32 s96, s81, 768\n"
    "v_mfma_f32_16x16x32_bf16 v[244:247], a[12:15], a[44:47], v[244:247]\n"
    "buffer_load_dwordx4 v40, s[24:27], s96 offen lds\n"
    "v_mfma_f32_16x16x32_bf16 v[248:251], a[12:15], a[48:51], v[248:251]\n"
    "v_mfma_f32_16x16x32_bf16 v[252:255], a[12:15], a[52:55], v[252:255]\n"
    "ds_read_b64_tr_b16 a[12:13], v49 offset:4104\n"
    "ds_read_b64_tr_b16 a[14:15], v49 offset:4616\n"
    "s_waitcnt lgkmcnt(4)\n"
    "v_mfma_f32_16x16x32_bf16 v[96:99], a[0:3], a[16:19], v[96:99]\n"
    "v_mfma_f32_16x16x32_bf16 v[100:103], a[0:3], a[20:23], v[100:103]\n"
    "s_add_u32 m0, s83, 53248\n"
    "s_add_u32 s96, s81, 29440\n"
    "v_mfma_f32_16x16x32_bf16 v[104:107], a[0:3], a[24:27], v[104:107]\n"
    "buffer_load_dwordx4 v40, s[24:27], s96 offen lds\n"
    "v_mfma_f32_16x16x32_bf16 v[108:111], a[0:3], a[28:31], v[108:111]\n"
    "v_mfma_f32_16x16x32_bf16 v[112:115], a[0:3], a[32:35], v[112:115]\n"
    "s_waitcnt vmcnt(9)\n"
    "ds_read_b64_tr_b16 a[0:1], v48 offset:8192\n"
    "v_mfma_f32_16x16x32_bf16 v[116:119], a[4:7], a[16:19], v[116:119]\n"
    "s_cmp_eq_u32 s60, 1\n"
    "s_cselect_b32 s62, 57344, s63\n"
    "s_add_u32 m0, s83, s62\n"
    "s_add_u32 s96, s81, 58112\n"
    "v_mfma_f32_16x16x32_bf16 v[120:123], a[4:7], a[20:23], v[120:123]\n"
    "buffer_load_dwordx4 v40, s[24:27], s96 offen lds\n"
    "v_mfma_f32_16x16x32_bf16 v[124:127], a[4:7], a[24:27], v[124:127]\n"
    "ds_read_b64_tr_b16 a[2:3], v48 offset:8704\n"
    "ds_read_b128 a[36:39], v47 offset:32768\n"
    "v_mfma_f32_16x16x32_bf16 v[128:131], a[4:7], a[28:31], v[128:131]\n"
    "v_mfma_f32_16x16x32_bf16 v[132:135], a[4:7], a[32:35], v[132:135]\n"
    "s_add_u32 m0, s61, 0\n"
    "s_add_u32 s92, s82, 2359424\n"
    "s_waitcnt lgkmcnt(3)\n"
    "v_mfma_f32_16x16x32_bf16 v[136:139], a[8:11], a[16:19], v[136:139]\n"
    "buffer_load_dwordx4 v41, s[28:31], s92 offen lds\n"
    "v_mfma_f32_16x16x32_bf16 v[140:143], a[8:11], a[20:23], v[140:143]\n"
    "ds_read_b64_tr_b16 a[4:5], v48 offset:8200\n"
    "ds_read_b64_tr_b16 a[6:7], v48 offset:8712\n"
    "v_mfma_f32_16x16x32_bf16 v[144:147], a[8:11], a[24:27], v[144:147]\n"
    "ds_read_b128 a[40:43], v47 offset:34816\n"
    "v_mfma_f32_16x16x32_bf16 v[148:151], a[8:11], a[28:31], v[148:151]\n"
    "v_mfma_f32_16x16x32_bf16 v[152:155], a[8:11], a[32:35], v[152:155]\n"
    "ds_read_b64_tr_b16 a[8:9], v49 offset:8192\n"
    "ds_read_b64_tr_b16 a[10:11], v49 offset:8704\n"
    "v_mfma_f32_16x16x32_bf16 v[156:159], a[12:15], a[16:19], v[156:159]\n"
    "buffer_load_dwordx4 v42, s[28:31], s92 offen offset:1024 lds\n"
    "v_mfma_f32_16x16x32_bf16 v[160:163], a[12:15], a[20:23], v[160:163]\n"
    "ds_read_b128 a[44:47], v47 offset:36864\n"
    "v_mfma_f32_16x16x32_bf16 v[164:167], a[12:15], a[24:27], v[164:167]\n"
    "v_mfma_f32_16x16x32_bf16 v[168:171], a[12:15], a[28:31], v[168:171]\n"
    "v_mfma_f32_16x16x32_bf16 v[172:175], a[12:15], a[32:35], v[172:175]\n"
    "buffer_load_dwordx4 v43, s[28:31], s92 offen offset:2048 lds\n"
    "s_waitcnt lgkmcnt(6)\n"
    "v_mfma_f32_16x16x32_bf16 v[176:179], a[0:3], a[16:19], v[176:179]\n"
    "ds_read_b64_tr_b16 a[12:13], v49 offset:8200\n"
    "ds_read_b64_tr_b16 a[14:15], v49 offset:8712\n"
    "v_mfma_f32_16x16x32_bf16 v[180:183], a[0:3], a[20:23], v[180:183]\n"
    "v_mfma_f32_16x16x32_bf16 v[184:187], a[0:3], a[24:27], v[184:187]\n"
    "ds_read_b128 a[48:51], v47 offset:38912\n"
    "v_mfma_f32_16x16x32_bf16 v[188:191], a[0:3], a[28:31], v[188:191]\n"
    "v_mfma_f32_16x16x32_bf16 v[192:195], a[0:3], a[32:35], v[192:195]\n"
    "buffer_load_dwordx4 v44, s[28:31], s92 offen offset:3072 lds\n"
    "s_waitcnt lgkmcnt(3)\n"
    "v_mfma_f32_16x16x32_bf16 v[196:199], a[4:7], a[16:19], v[196:199]\n"
    "s_waitcnt vmcnt(10)\n"
    "ds_read_b64_tr_b16 a[0:1], v48 offset:12288\n"
    "v_mfma_f32_16x16x32_bf16 v[200:203], a[4:7], a[20:23], v[200:203]\n"
    "ds_read_b64_tr_b16 a[2:3], v48 offset:12800\n"
    "v_mfma_f32_16x16x32_bf16 v[204:207], a[4:7], a[24:27], v[204:207]\n"
    "s_add_u32 m0, s84, 0\n"
    "s_add_u32 s92, s82, 2621440\n"
    "v_mfma_f32_16x16x32_bf16 v[208:211], a[4:7], a[28:31], v[208:211]\n"
    "buffer_load_dwordx4 v41, s[28:31], s92 offen lds\n"
    "v_mfma_f32_16x16x32_bf16 v[212:215], a[4:7], a[32:35], v[212:215]\n"
    "ds_read_b128 a[52:55], v47 offset:40960\n"
    "ds_read_b64_tr_b16 a[4:5], v48 offset:12296\n"
    "v_mfma_f32_16x16x32_bf16 v[216:219], a[8:11], a[16:19], v[216:219]\n"
    "ds_read_b64_tr_b16 a[6:7], v48 offset:12808\n"
    "v_mfma_f32_16x16x32_bf16 v[220:223], a[8:11], a[20:23], v[220:223]\n"
    "v_mfma_f32_16x16x32_bf16 v[224:227], a[8:11], a[24:27], v[224:227]\n"
    "buffer_load_dwordx4 v42, s[28:31], s92 offen offset:1024 lds\n"
    "v_mfma_f32_16x16x32_bf16 v[228:231], a[8:11], a[28:31], v[228:231]\n"
    "v_mfma_f32_16x16x32_bf16 v[232:235], a[8:11], a[32:35], v[232:235]\n"
    "ds_read_b64_tr_b16 a[8:9], v49 offset:12288\n"
    "ds_read_b64_tr_b16 a[10:11], v49 offset:12800\n"
    "s_waitcnt lgkmcnt(6)\n"
    "v_mfma_f32_16x16x32_bf16 v[236:239], a[12:15], a[16:19], v[236:239]\n"
    "v_mfma_f32_16x16x32_bf16 v[240:243], a[12:15], a[20:23], v[240:243]\n"
    "v_mfma_f32_16x16x32_bf16 v[244:247], a[12:15], a[24:27], v[244:247]\n"
    "buffer_load_dwordx4 v43, s[28:31], s92 offen offset:2048 lds\n"
    "v_mfma_f32_16x16x32_bf16 v[248:251], a[12:15], a[28:31], v[248:251]\n"
    "v_mfma_f32_16x16x32_bf16 v[252:255], a[12:15], a[32:35], v[252:255]\n"
    "ds_read_b64_tr_b16 a[12:13], v49 offset:12296\n"
    "ds_read_b64_tr_b16 a[14:15], v49 offset:12808\n"
    "s_waitcnt lgkmcnt(4)\n"
    "v_mfma_f32_16x16x32_bf16 v[96:99], a[0:3], a[36:39], v[96:99]\n"
    "v_mfma_f32_16x16x32_bf16 v[100:103], a[0:3], a[40:43], v[100:103]\n"
    "buffer_load_dwordx4 v44, s[28:31], s92 offen offset:3072 lds\n"
    "v_mfma_f32_16x16x32_bf16 v[104:107], a[0:3], a[44:47], v[104:107]\n"
    "s_waitcnt vmcnt(8) lgkmcnt(0)\n"
    "s_barrier\n"
    "v_mfma_f32_16x16x32_bf16 v[108:111], a[0:3], a[48:51], v[108:111]\n"
    "v_mfma_f32_16x16x32_bf16 v[112:115], a[0:3], a[52:55], v[112:115]\n"
    "s_waitcnt vmcnt(4)\n"
    "ds_read_b64_tr_b16 a[0:1], v52 offset:0\n"
    "v_mfma_f32_16x16x32_bf16 v[116:119], a[4:7], a[36:39], v[116:119]\n"
    "s_add_u32 m0, s84, 4096\n"
    "s_add_u32 s92, s82, 2621568\n"
    "v_mfma_f32_16x16x32_bf16 v[120:123], a[4:7], a[40:43], v[120:123]\n"
    "buffer_load_dwordx4 v41, s[28:31], s92 offen lds\n"
    "v_mfma_f32_16x16x32_bf16 v[124:127], a[4:7], a[44:47], v[124:127]\n"
    "ds_read_b64_tr_b16 a[2:3], v52 offset:512\n"
    "ds_read_b128 a[16:19], v46 offset:16384\n"
    "v_mfma_f32_16x16x32_bf16 v[128:131], a[4:7], a[48:51], v[128:131]\n"
    "v_mfma_f32_16x16x32_bf16 v[132:135], a[4:7], a[52:55], v[132:135]\n"
    "ds_read_b64_tr_b16 a[4:5], v52 offset:8\n"
    "ds_read_b64_tr_b16 a[6:7], v52 offset:520\n"
    "v_mfma_f32_16x16x32_bf16 v[136:139], a[8:11], a[36:39], v[136:139]\n"
    "buffer_load_dwordx4 v42, s[28:31], s92 offen offset:1024 lds\n"
    "v_mfma_f32_16x16x32_bf16 v[140:143], a[8:11], a[40:43], v[140:143]\n"
    "ds_read_b128 a[20:23], v46 offset:18432\n"
    "v_mfma_f32_16x16x32_bf16 v[144:147], a[8:11], a[44:47], v[144:147]\n"
    "v_mfma_f32_16x16x32_bf16 v[148:151], a[8:11], a[48:51], v[148:151]\n"
    "v_mfma_f32_16x16x32_bf16 v[152:155], a[8:11], a[52:55], v[152:155]\n"
    "buffer_load_dwordx4 v43, s[28:31], s92 offen offset:2048 lds\n"
    "v_mfma_f32_16x16x32_bf16 v[156:159], a[12:15], a[36:39], v[156:159]\n"
    "ds_read_b64_tr_b16 a[8:9], v53 offset:0\n"
    "ds_read_b64_tr_b16 a[10:11], v53 offset:512\n"
    "v_mfma_f32_16x16x32_bf16 v[160:163], a[12:15], a[40:43], v[160:163]\n"
    "ds_read_b128 a[24:27], v46 offset:20480\n"
    "v_mfma_f32_16x16x32_bf16 v[164:167], a[12:15], a[44:47], v[164:167]\n"
    "v_mfma_f32_16x16x32_bf16 v[168:171], a[12:15], a[48:51], v[168:171]\n"
    "v_mfma_f32_16x16x32_bf16 v[172:175], a[12:15], a[52:55], v[172:175]\n"
    "buffer_load_dwordx4 v44, s[28:31], s92 offen offset:3072 lds\n"
    "s_waitcnt lgkmcnt(4)\n"
    "v_mfma_f32_16x16x32_bf16 v[176:179], a[0:3], a[36:39], v[176:179]\n"
    "ds_read_b64_tr_b16 a[12:13], v53 offset:8\n"
    "ds_read_b64_tr_b16 a[14:15], v53 offset:520\n"
    "v_mfma_f32_16x16x32_bf16 v[180:183], a[0:3], a[40:43], v[180:183]\n"
    "v_mfma_f32_16x16x32_bf16 v[184:187], a[0:3], a[44:47], v[184:187]\n"
    "s_add_u32 m0, s84, 8192\n"
    "s_add_u32 s92, s82, 2883584\n"
    "v_mfma_f32_16x16x32_bf16 v[188:191], a[0:3], a[48:51], v[188:191]\n"
    "buffer_load_dwordx4 v41, s[28:31], s92 offen lds\n"
    "v_mfma_f32_16x16x32_bf16 v[192:195], a[0:3], a[52:55], v[192:195]\n"
    "s_waitcnt vmcnt(5)\n"
    "ds_read_b64_tr_b16 a[0:1], v48 offset:0\n"
    "v_mfma_f32_16x16x32_bf16 v[196:199], a[4:7], a[36:39], v[196:199]\n"
    "ds_read_b64_tr_b16 a[2:3], v48 offset:512\n"
    "ds_read_b128 a[28:31], v46 offset:22528\n"
    "v_mfma_f32_16x16x32_bf16 v[200:203], a[4:7], a[40:43], v[200:203]\n"
    "v_mfma_f32_16x16x32_bf16 v[204:207], a[4:7], a[44:47], v[204:207]\n"
    "v_mfma_f32_16x16x32_bf16 v[208:211], a[4:7], a[48:51], v[208:211]\n"
    "buffer_load_dwordx4 v42, s[28:31], s92 offen offset:1024 lds\n"
    "v_mfma_f32_16x16x32_bf16 v[212:215], a[4:7], a[52:55], v[212:215]\n"
    "ds_read_b128 a[32:35], v46 offset:24576\n"
    "ds_read_b64_tr_b16 a[4:5], v48 offset:8\n"
    "s_waitcnt lgkmcnt(5)\n"
    "v_mfma_f32_16x16x32_bf16 v[216:219], a[8:11], a[36:39], v[216:219]\n"
    "ds_read_b64_tr_b16 a[6:7], v48 offset:520\n"
    "v_mfma_f32_16x16x32_bf16 v[220:223], a[8:11], a[40:43], v[220:223]\n"
    "v_mfma_f32_16x16x32_bf16 v[224:227], a[8:11], a[44:47], v[224:227]\n"
    "buffer_load_dwordx4 v43, s[28:31], s92 offen offset:2048 lds\n"
    "v_mfma_f32_16x16x32_bf16 v[228:231], a[8:11], a[48:51], v[228:231]\n"
    "v_mfma_f32_16x16x32_bf16 v[232:235], a[8:11], a[52:55], v[232:235]\n"
    "ds_read_b64_tr_b16 a[8:9], v49 offset:0\n"
    "ds_read_b64_tr_b16 a[10:11], v49 offset:512\n"
    "v_mfma_f32_16x16x32_bf16 v[236:239], a[12:15], a[36:39], v[236:239]\n"
    "v_mfma_f32_16x16x32_bf16 v[240:243], a[12:15], a[40:43], v[240:243]\n"
    "buffer_load_dwordx4 v44, s[28:31], s92 offen offset:3072 lds\n"
    "v_mfma_f32_16x16x32_bf16 v[244:247], a[12:15], a[44:47], v[244:247]\n"
    "v_mfma_f32_16x16x32_bf16 v[248:251], a[12:15], a[48:51], v[248:251]\n"
    "v_mfma_f32_16x16x32_bf16 v[252:255], a[12:15], a[52:55], v[252:255]\n"
    "ds_read_b64_tr_b16 a[12:13], v49 offset:8\n"
    "ds_read_b64_tr_b16 a[14:15], v49 offset:520\n"
    "s_waitcnt lgkmcnt(4)\n"
    "v_mfma_f32_16x16x32_bf16 v[96:99], a[0:3], a[16:19], v[96:99]\n"
    "s_add_u32 m0, s84, 12288\n"
    "s_add_u32 s92, s82, 2883712\n"
    "v_mfma_f32_16x16x32_bf16 v[100:103], a[0:3], a[20:23], v[100:103]\n"
    "buffer_load_dwordx4 v41, s[28:31], s92 offen lds\n"
    "v_mfma_f32_16x16x32_bf16 v[104:107], a[0:3], a[24:27], v[104:107]\n"
    "v_mfma_f32_16x16x32_bf16 v[108:111], a[0:3], a[28:31], v[108:111]\n"
    "v_mfma_f32_16x16x32_bf16 v[112:115], a[0:3], a[32:35], v[112:115]\n"
    "s_waitcnt vmcnt(5)\n"
    "ds_read_b64_tr_b16 a[0:1], v48 offset:4096\n"
    "v_mfma_f32_16x16x32_bf16 v[116:119], a[4:7], a[16:19], v[116:119]\n"
    "buffer_load_dwordx4 v42, s[28:31], s92 offen offset:1024 lds\n"
    "v_mfma_f32_16x16x32_bf16 v[120:123], a[4:7], a[20:23], v[120:123]\n"
    "ds_read_b64_tr_b16 a[2:3], v48 offset:4608\n"
    "ds_read_b128 a[36:39], v47 offset:16384\n"
    "v_mfma_f32_16x16x32_bf16 v[124:127], a[4:7], a[24:27], v[124:127]\n"
    "v_mfma_f32_16x16x32_bf16 v[128:131], a[4:7], a[28:31], v[128:131]\n"
    "v_mfma_f32_16x16x32_bf16 v[132:135], a[4:7], a[32:35], v[132:135]\n"
    "ds_read_b64_tr_b16 a[4:5], v48 offset:4104\n"
    "ds_read_b64_tr_b16 a[6:7], v48 offset:4616\n"
    "s_waitcnt lgkmcnt(5)\n"
    "v_mfma_f32_16x16x32_bf16 v[136:139], a[8:11], a[16:19], v[136:139]\n"
    "buffer_load_dwordx4 v43, s[28:31], s92 offen offset:2048 lds\n"
    "v_mfma_f32_16x16x32_bf16 v[140:143], a[8:11], a[20:23], v[140:143]\n"
    "ds_read_b128 a[40:43], v47 offset:18432\n"
    "v_mfma_f32_16x16x32_bf16 v[144:147], a[8:11], a[24:27], v[144:147]\n"
    "v_mfma_f32_16x16x32_bf16 v[148:151], a[8:11], a[28:31], v[148:151]\n"
    "v_mfma_f32_16x16x32_bf16 v[152:155], a[8:11], a[32:35], v[152:155]\n"
    "buffer_load_dwordx4 v44, s[28:31], s92 offen offset:3072 lds\n"
    "v_mfma_f32_16x16x32_bf16 v[156:159], a[12:15], a[16:19], v[156:159]\n"
    "ds_read_b64_tr_b16 a[8:9], v49 offset:4096\n"
    "ds_read_b64_tr_b16 a[10:11], v49 offset:4608\n"
    "v_mfma_f32_16x16x32_bf16 v[160:163], a[12:15], a[20:23], v[160:163]\n"
    "ds_read_b128 a[44:47], v47 offset:20480\n"
    "v_mfma_f32_16x16x32_bf16 v[164:167], a[12:15], a[24:27], v[164:167]\n"
    "s_add_u32 m0, s61, 0\n"
    "s_add_u32 s92, s82, 3145728\n"
    "v_mfma_f32_16x16x32_bf16 v[168:171], a[12:15], a[28:31], v[168:171]\n"
    "buffer_load_dwordx4 v41, s[28:31], s92 offen lds\n"
    "v_mfma_f32_16x16x32_bf16 v[172:175], a[12:15], a[32:35], v[172:175]\n"
    "ds_read_b64_tr_b16 a[12:13], v49 offset:4104\n"
    "ds_read_b64_tr_b16 a[14:15], v49 offset:4616\n"
    "s_waitcnt lgkmcnt(6)\n"
    "v_mfma_f32_16x16x32_bf16 v[176:179], a[0:3], a[16:19], v[176:179]\n"
    "v_mfma_f32_16x16x32_bf16 v[180:183], a[0:3], a[20:23], v[180:183]\n"
    "v_mfma_f32_16x16x32_bf16 v[184:187], a[0:3], a[24:27], v[184:187]\n"
    "ds_read_b128 a[48:51], v47 offset:22528\n"
    "v_mfma_f32_16x16x32_bf16 v[188:191], a[0:3], a[28:31], v[188:191]\n"
    "buffer_load_dwordx4 v42, s[28:31], s92 offen offset:1024 lds\n"
    "v_mfma_f32_16x16x32_bf16 v[192:195], a[0:3], a[32:35], v[192:195]\n"
    "s_waitcnt vmcnt(6)\n"
    "ds_read_b64_tr_b16 a[0:1], v48 offset:8192\n"
    "v_mfma_f32_16x16x32_bf16 v[196:199], a[4:7], a[16:19], v[196:199]\n"
    "ds_read_b64_tr_b16 a[2:3], v48 offset:8704\n"
    "v_mfma_f32_16x16x32_bf16 v[200:203], a[4:7], a[20:23], v[200:203]\n"
    "v_mfma_f32_16x16x32_bf16 v[204:207], a[4:7], a[24:27], v[204:207]\n"
    "buffer_load_dwordx4 v43, s[28:31], s92 offen offset:2048 lds\n"
    "v_mfma_f32_16x16x32_bf16 v[208:211], a[4:7], a[28:31], v[208:211]\n"
    "ds_read_b128 a[52:55], v47 offset:24576\n"
    "v_mfma_f32_16x16x32_bf16 v[212:215], a[4:7], a[32:35], v[212:215]\n"
    "ds_read_b64_tr_b16 a[4:5], v48 offset:8200\n"
    "ds_read_b64_tr_b16 a[6:7], v48 offset:8712\n"
    "s_waitcnt lgkmcnt(6)\n"
    "v_mfma_f32_16x16x32_bf16 v[216:219], a[8:11], a[16:19], v[216:219]\n"
    "v_mfma_f32_16x16x32_bf16 v[220:223], a[8:11], a[20:23], v[220:223]\n"
    "v_mfma_f32_16x16x32_bf16 v[224:227], a[8:11], a[24:27], v[224:227]\n"
    "buffer_load_dwordx4 v44, s[28:31], s92 offen offset:3072 lds\n"
    "v_mfma_f32_16x16x32_bf16 v[228:231], a[8:11], a[28:31], v[228:231]\n"
    "v_mfma_f32_16x16x32_bf16 v[232:235], a[8:11], a[32:35], v[232:235]\n"
    "ds_read_b64_tr_b16 a[8:9], v49 offset:8192\n"
    "ds_read_b64_tr_b16 a[10:11], v49 offset:8704\n"
    "v_mfma_f32_16x16x32_bf16 v[236:239], a[12:15], a[16:19], v[236:239]\n"
    "s_add_u32 m0, s84, 0\n"
    "s_add_u32 s92, s82, 3145856\n"
    "v_mfma_f32_16x16x32_bf16 v[240:243], a[12:15], a[20:23], v[240:243]\n"
    "buffer_load_dwordx4 v41, s[28:31], s92 offen lds\n"
    "v_mfma_f32_16x16x32_bf16 v[244:247], a[12:15], a[24:27], v[244:247]\n"
    "v_mfma_f32_16x16x32_bf16 v[248:251], a[12:15], a[28:31], v[248:251]\n"
    "v_mfma_f32_16x16x32_bf16 v[252:255], a[12:15], a[32:35], v[252:255]\n"
    "ds_read_b64_tr_b16 a[12:13], v49 offset:8200\n"
    "ds_read_b64_tr_b16 a[14:15], v49 offset:8712\n"
    "s_waitcnt lgkmcnt(4)\n"
    "v_mfma_f32_16x16x32_bf16 v[96:99], a[0:3], a[36:39], v[96:99]\n"
    "buffer_load_dwordx4 v42, s[28:31], s92 offen offset:1024 lds\n"
    "v_mfma_f32_16x16x32_bf16 v[100:103], a[0:3], a[40:43], v[100:103]\n"
    "v_mfma_f32_16x16x32_bf16 v[104:107], a[0:3], a[44:47], v[104:107]\n"
    "v_mfma_f32_16x16x32_bf16 v[108:111], a[0:3], a[48:51], v[108:111]\n"
    "v_mfma_f32_16x16x32_bf16 v[112:115], a[0:3], a[52:55], v[112:115]\n"
    "s_waitcnt vmcnt(6)\n"
    "ds_read_b64_tr_b16 a[0:1], v48 offset:12288\n"
    "v_mfma_f32_16x16x32_bf16 v[116:119], a[4:7], a[36:39], v[116:119]\n"
    "buffer_load_dwordx4 v43, s[28:31], s92 offen offset:2048 lds\n"
    "v_mfma_f32_16x16x32_bf16 v[120:123], a[4:7], a[40:43], v[120:123]\n"
    "ds_read_b64_tr_b16 a[2:3], v48 offset:12800\n"
    "ds_read_b128 a[16:19], v46 offset:49152\n"
    "v_mfma_f32_16x16x32_bf16 v[124:127], a[4:7], a[44:47], v[124:127]\n"
    "v_mfma_f32_16x16x32_bf16 v[128:131], a[4:7], a[48:51], v[128:131]\n"
    "v_mfma_f32_16x16x32_bf16 v[132:135], a[4:7], a[52:55], v[132:135]\n"
    "buffer_load_dwordx4 v44, s[28:31], s92 offen offset:3072 lds\n"
    "s_waitcnt lgkmcnt(3)\n"
    "v_mfma_f32_16x16x32_bf16 v[136:139], a[8:11], a[36:39], v[136:139]\n"
    "ds_read_b64_tr_b16 a[4:5], v48 offset:12296\n"
    "ds_read_b64_tr_b16 a[6:7], v48 offset:12808\n"
    "v_mfma_f32_16x16x32_bf16 v[140:143], a[8:11], a[40:43], v[140:143]\n"
    "ds_read_b128 a[20:23], v46 offset:51200\n"
    "v_mfma_f32_16x16x32_bf16 v[144:147], a[8:11], a[44:47], v[144:147]\n"
    "v_mfma_f32_16x16x32_bf16 v[148:151], a[8:11], a[48:51], v[148:151]\n"
    "s_add_u32 m0, s84, 4096\n"
    "s_add_u32 s92, s82, 3407872\n"
    "v_mfma_f32_16x16x32_bf16 v[152:155], a[8:11], a[52:55], v[152:155]\n"
    "buffer_load_dwordx4 v41, s[28:31], s92 offen lds\n"
    "v_mfma_f32_16x16x32_bf16 v[156:159], a[12:15], a[36:39], v[156:159]\n"
    "ds_read_b64_tr_b16 a[8:9], v49 offset:12288\n"
    "ds_read_b64_tr_b16 a[10:11], v49 offset:12800\n"
    "v_mfma_f32_16x16x32_bf16 v[160:163], a[12:15], a[40:43], v[160:163]\n"
    "ds_read_b128 a[24:27], v46 offset:53248\n"
    "v_mfma_f32_16x16x32_bf16 v[164:167], a[12:15], a[44:47], v[164:167]\n"
    "v_mfma_f32_16x16x32_bf16 v[168:171], a[12:15], a[48:51], v[168:171]\n"
    "buffer_load_dwordx4 v42, s[28:31], s92 offen offset:1024 lds\n"
    "v_mfma_f32_16x16x32_bf16 v[172:175], a[12:15], a[52:55], v[172:175]\n"
    "ds_read_b64_tr_b16 a[12:13], v49 offset:12296\n"
    "ds_read_b64_tr_b16 a[14:15], v49 offset:12808\n"
    "s_waitcnt lgkmcnt(6)\n"
    "v_mfma_f32_16x16x32_bf16 v[176:179], a[0:3], a[36:39], v[176:179]\n"
    "v_mfma_f32_16x16x32_bf16 v[180:183], a[0:3], a[40:43], v[180:183]\n"
    "v_mfma_f32_16x16x32_bf16 v[184:187], a[0:3], a[44:47], v[184:187]\n"
    "buffer_load_dwordx4 v43, s[28:31], s92 offen offset:2048 lds\n"
    "v_mfma_f32_16x16x32_bf16 v[188:191], a[0:3], a[48:51], v[188:191]\n"
    "ds_read_b128 a[28:31], v46 offset:55296\n"
    "v_mfma_f32_16x16x32_bf16 v[192:195], a[0:3], a[52:55], v[192:195]\n"
    "s_waitcnt vmcnt(7)\n"
    "ds_read_b64_tr_b16 a[0:1], v52 offset:0\n"
    "v_mfma_f32_16x16x32_bf16 v[196:199], a[4:7], a[36:39], v[196:199]\n"
    "ds_read_b64_tr_b16 a[2:3], v52 offset:512\n"
    "v_mfma_f32_16x16x32_bf16 v[200:203], a[4:7], a[40:43], v[200:203]\n"
    "v_mfma_f32_16x16x32_bf16 v[204:207], a[4:7], a[44:47], v[204:207]\n"
    "buffer_load_dwordx4 v44, s[28:31], s92 offen offset:3072 lds\n"
    "v_mfma_f32_16x16x32_bf16 v[208:211], a[4:7], a[48:51], v[208:211]\n"
    "ds_read_b128 a[32:35], v46 offset:57344\n"
    "v_mfma_f32_16x16x32_bf16 v[212:215], a[4:7], a[52:55], v[212:215]\n"
    "ds_read_b64_tr_b16 a[4:5], v52 offset:8\n"
    "ds_read_b64_tr_b16 a[6:7], v52 offset:520\n"
    "s_waitcnt lgkmcnt(6)\n"
    "v_mfma_f32_16x16x32_bf16 v[216:219], a[8:11], a[36:39], v[216:219]\n"
    "s_add_u32 m0, s84, 8192\n"
    "s_add_u32 s92, s82, 3408000\n"
    "v_mfma_f32_16x16x32_bf16 v[220:223], a[8:11], a[40:43], v[220:223]\n"
    "buffer_load_dwordx4 v41, s[28:31], s92 offen lds\n"
    "v_mfma_f32_16x16x32_bf16 v[224:227], a[8:11], a[44:47], v[224:227]\n"
    "v_mfma_f32_16x16x32_bf16 v[228:231], a[8:11], a[48:51], v[228:231]\n"
    "v_mfma_f32_16x16x32_bf16 v[232:235], a[8:11], a[52:55], v[232:235]\n"
    "ds_read_b64_tr_b16 a[8:9], v53 offset:0\n"
    "ds_read_b64_tr_b16 a[10:11], v53 offset:512\n"
    "v_mfma_f32_16x16x32_bf16 v[236:239], a[12:15], a[36:39], v[236:239]\n"
    "v_mfma_f32_16x16x32_bf16 v[240:243], a[12:15], a[40:43], v[240:243]\n"
    "buffer_load_dwordx4 v42, s[28:31], s92 offen offset:1024 lds\n"
    "v_mfma_f32_16x16x32_bf16 v[244:247], a[12:15], a[44:47], v[244:247]\n"
    "ds_read_b128 a[36:39], v47 offset:49152\n"
    "v_mfma_f32_16x16x32_bf16 v[248:251], a[12:15], a[48:51], v[248:251]\n"
    "ds_read_b128 a[40:43], v47 offset:51200\n"
    "v_mfma_f32_16x16x32_bf16 v[252:255], a[12:15], a[52:55], v[252:255]\n"
    "ds_read_b64_tr_b16 a[12:13], v53 offset:8\n"
    "ds_read_b64_tr_b16 a[14:15], v53 offset:520\n"
    "s_waitcnt lgkmcnt(6)\n"
    "v_mfma_f32_16x16x32_bf16 v[96:99], a[0:3], a[16:19], v[96:99]\n"
    "buffer_load_dwordx4 v43, s[28:31], s92 offen offset:2048 lds\n"
    "v_mfma_f32_16x16x32_bf16 v[100:103], a[0:3], a[20:23], v[100:103]\n"
    "ds_read_b128 a[44:47], v47 offset:53248\n"
    "v_mfma_f32_16x16x32_bf16 v[104:107], a[0:3], a[24:27], v[104:107]\n"
    "ds_read_b128 a[48:51], v47 offset:55296\n"
    "v_mfma_f32_16x16x32_bf16 v[108:111], a[0:3], a[28:31], v[108:111]\n"
    "v_mfma_f32_16x16x32_bf16 v[112:115], a[0:3], a[32:35], v[112:115]\n"
    "buffer_load_dwordx4 v44, s[28:31], s92 offen offset:3072 lds\n"
    "v_mfma_f32_16x16x32_bf16 v[116:119], a[4:7], a[16:19], v[116:119]\n"
    "ds_read_b128 a[52:55], v47 offset:57344\n"
    "s_waitcnt vmcnt(4)\n"
    "v_mfma_f32_16x16x32_bf16 v[120:123], a[4:7], a[20:23], v[120:123]\n"
    "ds_read_b64_tr_b16 a[0:1], v48 offset:4096\n"
    "ds_read_b64_tr_b16 a[2:3], v48 offset:4608\n"
    "v_mfma_f32_16x16x32_bf16 v[124:127], a[4:7], a[24:27], v[124:127]\n"
    "v_mfma_f32_16x16x32_bf16 v[128:131], a[4:7], a[28:31], v[128:131]\n"
    "s_add_u32 m0, s71, 0\n"
    "v_mfma_f32_16x16x32_bf16 v[132:135], a[4:7], a[32:35], v[132:135]\n"
    "buffer_load_dwordx4 v1, s[40:43], s68 offen nt lds\n"
    "s_waitcnt lgkmcnt(5)\n"
    "v_mfma_f32_16x16x32_bf16 v[136:139], a[8:11], a[16:19], v[136:139]\n"
    "ds_read_b64_tr_b16 a[4:5], v48 offset:4104\n"
    "ds_read_b64_tr_b16 a[6:7], v48 offset:4616\n"
    "v_mfma_f32_16x16x32_bf16 v[140:143], a[8:11], a[20:23], v[140:143]\n"
    "v_mfma_f32_16x16x32_bf16 v[144:147], a[8:11], a[24:27], v[144:147]\n"
    "v_mfma_f32_16x16x32_bf16 v[148:151], a[8:11], a[28:31], v[148:151]\n"
    "buffer_load_dwordx4 v1, s[40:43], s68 offen offset:1024 nt lds\n"
    "v_mfma_f32_16x16x32_bf16 v[152:155], a[8:11], a[32:35], v[152:155]\n"
    "ds_read_b64_tr_b16 a[8:9], v49 offset:4096\n"
    "ds_read_b64_tr_b16 a[10:11], v49 offset:4608\n"
    "v_mfma_f32_16x16x32_bf16 v[156:159], a[12:15], a[16:19], v[156:159]\n"
    "v_mfma_f32_16x16x32_bf16 v[160:163], a[12:15], a[20:23], v[160:163]\n"
    "v_mfma_f32_16x16x32_bf16 v[164:167], a[12:15], a[24:27], v[164:167]\n"
    "v_mfma_f32_16x16x32_bf16 v[168:171], a[12:15], a[28:31], v[168:171]\n"
    "buffer_load_dwordx4 v1, s[40:43], s68 offen offset:2048 nt lds\n"
    "v_mfma_f32_16x16x32_bf16 v[172:175], a[12:15], a[32:35], v[172:175]\n"
    "ds_read_b64_tr_b16 a[12:13], v49 offset:4104\n"
    "ds_read_b64_tr_b16 a[14:15], v49 offset:4616\n"
    "s_waitcnt lgkmcnt(4)\n"
    "v_mfma_f32_16x16x32_bf16 v[96:99], a[0:3], a[36:39], v[96:99]\n"
    "v_mfma_f32_16x16x32_bf16 v[100:103], a[0:3], a[40:43], v[100:103]\n"
    "v_mfma_f32_16x16x32_bf16 v[104:107], a[0:3], a[44:47], v[104:107]\n"
    "buffer_load_dwordx4 v1, s[40:43], s68 offen offset:3072 nt lds\n"
    "s_add_u32 s68, s68, 32768\n"
    "v_mfma_f32_16x16x32_bf16 v[108:111], a[0:3], a[48:51], v[108:111]\n"
    "v_mfma_f32_16x16x32_bf16 v[112:115], a[0:3], a[52:55], v[112:115]\n"
    "ds_read_b64_tr_b16 a[0:1], v48 offset:0\n"
    "v_mfma_f32_16x16x32_bf16 v[116:119], a[4:7], a[36:39], v[116:119]\n"
    "s_add_u32 m0, s70, 0\n"
    "ds_read_b64_tr_b16 a[2:3], v48 offset:512\n"
    "v_mfma_f32_16x16x32_bf16 v[120:123], a[4:7], a[40:43], v[120:123]\n"
    "buffer_load_dwordx4 v1, s[36:39], s66 offen nt lds\n"
    "v_mfma_f32_16x16x32_bf16 v[124:127], a[4:7], a[44:47], v[124:127]\n"
    "v_mfma_f32_16x16x32_bf16 v[128:131], a[4:7], a[48:51], v[128:131]\n"
    "v_mfma_f32_16x16x32_bf16 v[132:135], a[4:7], a[52:55], v[132:135]\n"
    "ds_read_b64_tr_b16 a[4:5], v48 offset:8\n"
    "ds_read_b64_tr_b16 a[6:7], v48 offset:520\n"
    "s_waitcnt lgkmcnt(4)\n"
    "v_mfma_f32_16x16x32_bf16 v[136:139], a[8:11], a[36:39], v[136:139]\n"
    "v_pk_mul_f32 v[96:97], v[96:97], s[86:87]\n"
    "v_pk_mul_f32 v[98:99], v[98:99], s[86:87]\n"
    "v_mfma_f32_16x16x32_bf16 v[140:143], a[8:11], a[40:43], v[140:143]\n"
    "buffer_load_dwordx4 v1, s[36:39], s66 offen offset:1024 nt lds\n"
    "v_mfma_f32_16x16x32_bf16 v[144:147], a[8:11], a[44:47], v[144:147]\n"
    "v_pk_mul_f32 v[116:117], v[116:117], s[86:87]\n"
    "v_pk_mul_f32 v[118:119], v[118:119], s[86:87]\n"
    "v_mfma_f32_16x16x32_bf16 v[148:151], a[8:11], a[48:51], v[148:151]\n"
    "v_cvt_pk_bf16_f32 v96, v96, v97\n"
    "v_cvt_pk_bf16_f32 v97, v98, v99\n"
    "v_mfma_f32_16x16x32_bf16 v[152:155], a[8:11], a[52:55], v[152:155]\n"
    "s_waitcnt vmcnt(6)\n"
    "ds_read_b64_tr_b16 a[8:9], v48 offset:8192\n"
    "v_mfma_f32_16x16x32_bf16 v[156:159], a[12:15], a[36:39], v[156:159]\n"
    "buffer_load_dwordx4 v1, s[36:39], s66 offen offset:2048 nt lds\n"
    "v_mfma_f32_16x16x32_bf16 v[160:163], a[12:15], a[40:43], v[160:163]\n"
    "ds_read_b64_tr_b16 a[10:11], v48 offset:8704\n"
    "v_cvt_pk_bf16_f32 v98, v116, v117\n"
    "v_mfma_f32_16x16x32_bf16 v[164:167], a[12:15], a[44:47], v[164:167]\n"
    "v_cvt_pk_bf16_f32 v99, v118, v119\n"
    "v_mfma_f32_16x16x32_bf16 v[168:171], a[12:15], a[48:51], v[168:171]\n"
    "ds_write_b128 v94, v[96:99] offset:12288\n"
    "v_mfma_f32_16x16x32_bf16 v[172:175], a[12:15], a[52:55], v[172:175]\n"
    "ds_read_b64_tr_b16 a[12:13], v48 offset:8200\n"
    "ds_read_b64_tr_b16 a[14:15], v48 offset:8712\n"
    "s_waitcnt lgkmcnt(5)\n"
    "v_mfma_f32_16x16x32_bf16 v[176:179], a[0:3], a[16:19], v[176:179]\n"
    "buffer_load_dwordx4 v1, s[36:39], s66 offen offset:3072 nt lds\n"
    "s_add_u32 s66, s66, 32768\n"
    "v_mfma_f32_16x16x32_bf16 v[180:183], a[0:3], a[20:23], v[180:183]\n"
    "v_pk_mul_f32 v[100:101], v[100:101], s[86:87]\n"
    "v_pk_mul_f32 v[102:103], v[102:103], s[86:87]\n"
    "v_mfma_f32_16x16x32_bf16 v[184:187], a[0:3], a[24:27], v[184:187]\n"
    "v_pk_mul_f32 v[120:121], v[120:121], s[86:87]\n"
    "v_pk_mul_f32 v[122:123], v[122:123], s[86:87]\n"
    "v_mfma_f32_16x16x32_bf16 v[188:191], a[0:3], a[28:31], v[188:191]\n"
    "s_add_u32 m0, s70, 4096\n"
    "v_cvt_pk_bf16_f32 v100, v100, v101\n"
    "v_mfma_f32_16x16x32_bf16 v[192:195], a[0:3], a[32:35], v[192:195]\n"
    "buffer_load_dwordx4 v1, s[36:39], s67 offen nt lds\n"
    "v_mfma_f32_16x16x32_bf16 v[196:199], a[4:7], a[16:19], v[196:199]\n"
    "ds_read_b64_tr_b16 a[0:1], v49 offset:0\n"
    "ds_read_b64_tr_b16 a[2:3], v49 offset:512\n"
    "v_mfma_f32_16x16x32_bf16 v[200:203], a[4:7], a[20:23], v[200:203]\n"
    "v_cvt_pk_bf16_f32 v101, v102, v103\n"
    "v_cvt_pk_bf16_f32 v102, v120, v121\n"
    "v_mfma_f32_16x16x32_bf16 v[204:207], a[4:7], a[24:27], v[204:207]\n"
    "v_cvt_pk_bf16_f32 v103, v122, v123\n"
    "v_mfma_f32_16x16x32_bf16 v[208:211], a[4:7], a[28:31], v[208:211]\n"
    "buffer_load_dwordx4 v1, s[36:39], s67 offen offset:1024 nt lds\n"
    "v_mfma_f32_16x16x32_bf16 v[212:215], a[4:7], a[32:35], v[212:215]\n"
    "ds_read_b64_tr_b16 a[4:5], v49 offset:8\n"
    "ds_read_b64_tr_b16 a[6:7], v49 offset:520\n"
    "s_waitcnt lgkmcnt(4)\n"
    "v_mfma_f32_16x16x32_bf16 v[176:179], a[8:11], a[36:39], v[176:179]\n"
    "ds_write_b128 v94, v[100:103] offset:13312\n"
    "v_mfma_f32_16x16x32_bf16 v[180:183], a[8:11], a[40:43], v[180:183]\n"
    "v_pk_mul_f32 v[104:105], v[104:105], s[86:87]\n"
    "v_pk_mul_f32 v[106:107], v[106:107], s[86:87]\n"
    "v_mfma_f32_16x16x32_bf16 v[184:187], a[8:11], a[44:47], v[184:187]\n"
    "v_pk_mul_f32 v[124:125], v[124:125], s[86:87]\n"
    "v_pk_mul_f32 v[126:127], v[126:127], s[86:87]\n"
    "v_mfma_f32_16x16x32_bf16 v[188:191], a[8:11], a[48:51], v[188:191]\n"
    "buffer_load_dwordx4 v1, s[36:39], s67 offen offset:2048 nt lds\n"
    "v_mfma_f32_16x16x32_bf16 v[192:195], a[8:11], a[52:55], v[192:195]\n"
    "ds_read_b64_tr_b16 a[8:9], v49 offset:8192\n"
    "ds_read_b64_tr_b16 a[10:11], v49 offset:8704\n"
    "v_mfma_f32_16x16x32_bf16 v[196:199], a[12:15], a[36:39], v[196:199]\n"
    "v_cvt_pk_bf16_f32 v104, v104, v105\n"
    "v_cvt_pk_bf16_f32 v105, v106, v107\n"
    "v_mfma_f32_16x16x32_bf16 v[200:203], a[12:15], a[40:43], v[200:203]\n"
    "v_cvt_pk_bf16_f32 v106, v124, v125\n"
    "v_cvt_pk_bf16_f32 v107, v126, v127\n"
    "v_mfma_f32_16x16x32_bf16 v[204:207], a[12:15], a[44:47], v[204:207]\n"
    "buffer_load_dwordx4 v1, s[36:39], s67 offen offset:3072 nt lds\n"
    "s_add_u32 s67, s67, 32768\n"
    "v_mfma_f32_16x16x32_bf16 v[208:211], a[12:15], a[48:51], v[208:211]\n"
    "ds_write_b128 v94, v[104:107] offset:14336\n"
    "v_mfma_f32_16x16x32_bf16 v[212:215], a[12:15], a[52:55], v[212:215]\n"
    "ds_read_b64_tr_b16 a[12:13], v49 offset:8200\n"
    "ds_read_b64_tr_b16 a[14:15], v49 offset:8712\n"
    "s_waitcnt lgkmcnt(5)\n"
    "v_mfma_f32_16x16x32_bf16 v[216:219], a[0:3], a[16:19], v[216:219]\n"
    "v_pk_mul_f32 v[108:109], v[108:109], s[86:87]\n"
    "v_pk_mul_f32 v[110:111], v[110:111], s[86:87]\n"
    "v_mfma_f32_16x16x32_bf16 v[220:223], a[0:3], a[20:23], v[220:223]\n"
    "v_pk_mul_f32 v[128:129], v[128:129], s[86:87]\n"
    "v_pk_mul_f32 v[130:131], v[130:131], s[86:87]\n"
    "v_mfma_f32_16x16x32_bf16 v[224:227], a[0:3], a[24:27], v[224:227]\n"
    "v_cvt_pk_bf16_f32 v108, v108, v109\n"
    "v_cvt_pk_bf16_f32 v109, v110, v111\n"
    "v_mfma_f32_16x16x32_bf16 v[228:231], a[0:3], a[28:31], v[228:231]\n"
    "v_cvt_pk_bf16_f32 v110, v128, v129\n"
    "v_cvt_pk_bf16_f32 v111, v130, v131\n"
    "v_mfma_f32_16x16x32_bf16 v[232:235], a[0:3], a[32:35], v[232:235]\n"
    "ds_write_b128 v94, v[108:111] offset:15360\n"
    "v_mfma_f32_16x16x32_bf16 v[236:239], a[4:7], a[16:19], v[236:239]\n"
    "v_pk_mul_f32 v[112:113], v[112:113], s[86:87]\n"
    "v_pk_mul_f32 v[114:115], v[114:115], s[86:87]\n"
    "v_mfma_f32_16x16x32_bf16 v[240:243], a[4:7], a[20:23], v[240:243]\n"
    "v_pk_mul_f32 v[132:133], v[132:133], s[86:87]\n"
    "v_pk_mul_f32 v[134:135], v[134:135], s[86:87]\n"
    "v_mfma_f32_16x16x32_bf16 v[244:247], a[4:7], a[24:27], v[244:247]\n"
    "v_cvt_pk_bf16_f32 v112, v112, v113\n"
    "v_cvt_pk_bf16_f32 v113, v114, v115\n"
    "v_mfma_f32_16x16x32_bf16 v[248:251], a[4:7], a[28:31], v[248:251]\n"
    "v_cvt_pk_bf16_f32 v114, v132, v133\n"
    "v_cvt_pk_bf16_f32 v115, v134, v135\n"
    "v_mfma_f32_16x16x32_bf16 v[252:255], a[4:7], a[32:35], v[252:255]\n"
    "ds_write_b128 v95, v[112:115] offset:0\n"
    "s_waitcnt lgkmcnt(2)\n"
    "v_mfma_f32_16x16x32_bf16 v[216:219], a[8:11], a[36:39], v[216:219]\n"
    "v_pk_mul_f32 v[136:137], v[136:137], s[86:87]\n"
    "v_pk_mul_f32 v[138:139], v[138:139], s[86:87]\n"
    "v_mfma_f32_16x16x32_bf16 v[220:223], a[8:11], a[40:43], v[220:223]\n"
    "v_pk_mul_f32 v[156:157], v[156:157], s[86:87]\n"
    "v_pk_mul_f32 v[158:159], v[158:159], s[86:87]\n"
    "v_mfma_f32_16x16x32_bf16 v[224:227], a[8:11], a[44:47], v[224:227]\n"
    "v_cvt_pk_bf16_f32 v136, v136, v137\n"
    "v_cvt_pk_bf16_f32 v137, v138, v139\n"
    "v_mfma_f32_16x16x32_bf16 v[228:231], a[8:11], a[48:51], v[228:231]\n"
    "v_cvt_pk_bf16_f32 v138, v156, v157\n"
    "v_cvt_pk_bf16_f32 v139, v158, v159\n"
    "v_mfma_f32_16x16x32_bf16 v[232:235], a[8:11], a[52:55], v[232:235]\n"
    "ds_write_b128 v95, v[136:139] offset:1024\n"
    "v_mfma_f32_16x16x32_bf16 v[236:239], a[12:15], a[36:39], v[236:239]\n"
    "v_pk_mul_f32 v[140:141], v[140:141], s[86:87]\n"
    "v_pk_mul_f32 v[142:143], v[142:143], s[86:87]\n"
    "v_mfma_f32_16x16x32_bf16 v[240:243], a[12:15], a[40:43], v[240:243]\n"
    "v_pk_mul_f32 v[160:161], v[160:161], s[86:87]\n"
    "v_pk_mul_f32 v[162:163], v[162:163], s[86:87]\n"
    "v_mfma_f32_16x16x32_bf16 v[244:247], a[12:15], a[44:47], v[244:247]\n"
    "v_cvt_pk_bf16_f32 v140, v140, v141\n"
    "v_cvt_pk_bf16_f32 v141, v142, v143\n"
    "v_mfma_f32_16x16x32_bf16 v[248:251], a[12:15], a[48:51], v[248:251]\n"
    "v_cvt_pk_bf16_f32 v142, v160, v161\n"
    "v_cvt_pk_bf16_f32 v143, v162, v163\n"
    "v_mfma_f32_16x16x32_bf16 v[252:255], a[12:15], a[52:55], v[252:255]\n"
    "ds_write_b128 v95, v[140:143] offset:2048\n"
    "v_pk_mul_f32 v[144:145], v[144:145], s[86:87]\n"
    "v_pk_mul_f32 v[146:147], v[146:147], s[86:87]\n"
    "v_pk_mul_f32 v[164:165], v[164:165], s[86:87]\n"
    "v_pk_mul_f32 v[166:167], v[166:167], s[86:87]\n"
    "v_cvt_pk_bf16_f32 v144, v144, v145\n"
    "v_cvt_pk_bf16_f32 v145, v146, v147\n"
    "v_cvt_pk_bf16_f32 v146, v164, v165\n"
    "v_cvt_pk_bf16_f32 v147, v166, v167\n"
    "ds_write_b128 v95, v[144:147] offset:3072\n"
    "v_pk_mul_f32 v[148:149], v[148:149], s[86:87]\n"
    "v_pk_mul_f32 v[150:151], v[150:151], s[86:87]\n"
    "v_pk_mul_f32 v[168:169], v[168:169], s[86:87]\n"
    "v_pk_mul_f32 v[170:171], v[170:171], s[86:87]\n"
    "v_cvt_pk_bf16_f32 v148, v148, v149\n"
    "v_cvt_pk_bf16_f32 v149, v150, v151\n"
    "v_cvt_pk_bf16_f32 v150, v168, v169\n"
    "v_cvt_pk_bf16_f32 v151, v170, v171\n"
    "ds_write_b128 v94, v[148:151] offset:4096\n"
    "v_pk_mul_f32 v[152:153], v[152:153], s[86:87]\n"
    "v_pk_mul_f32 v[154:155], v[154:155], s[86:87]\n"
    "v_pk_mul_f32 v[172:173], v[172:173], s[86:87]\n"
    "v_pk_mul_f32 v[174:175], v[174:175], s[86:87]\n"
    "v_cvt_pk_bf16_f32 v152, v152, v153\n"
    "v_cvt_pk_bf16_f32 v153, v154, v155\n"
    "v_cvt_pk_bf16_f32 v154, v172, v173\n"
    "v_cvt_pk_bf16_f32 v155, v174, v175\n"
    "ds_write_b128 v94, v[152:155] offset:5120\n"
    "v_pk_mul_f32 v[176:177], v[176:177], s[86:87]\n"
    "v_pk_mul_f32 v[178:179], v[178:179], s[86:87]\n"
    "v_pk_mul_f32 v[196:197], v[196:197], s[86:87]\n"
    "v_pk_mul_f32 v[198:199], v[198:199], s[86:87]\n"
    "v_cvt_pk_bf16_f32 v176, v176, v177\n"
    "v_cvt_pk_bf16_f32 v177, v178, v179\n"
    "v_cvt_pk_bf16_f32 v178, v196, v197\n"
    "v_cvt_pk_bf16_f32 v179, v198, v199\n"
    "ds_write_b128 v94, v[176:179] offset:6144\n"
    "v_pk_mul_f32 v[180:181], v[180:181], s[86:87]\n"
    "v_pk_mul_f32 v[182:183], v[182:183], s[86:87]\n"
    "v_pk_mul_f32 v[200:201], v[200:201], s[86:87]\n"
    "v_pk_mul_f32 v[202:203], v[202:203], s[86:87]\n"
    "v_cvt_pk_bf16_f32 v180, v180, v181\n"
    "v_cvt_pk_bf16_f32 v181, v182, v183\n"
    "v_cvt_pk_bf16_f32 v182, v200, v201\n"
    "v_cvt_pk_bf16_f32 v183, v202, v203\n"
    "ds_write_b128 v94, v[180:183] offset:7168\n"
    "v_pk_mul_f32 v[184:185], v[184:185], s[86:87]\n"
    "v_pk_mul_f32 v[186:187], v[186:187], s[86:87]\n"
    "v_pk_mul_f32 v[204:205], v[204:205], s[86:87]\n"
    "v_pk_mul_f32 v[206:207], v[206:207], s[86:87]\n"
    "v_cvt_pk_bf16_f32 v184, v184, v185\n"
    "v_cvt_pk_bf16_f32 v185, v186, v187\n"
    "v_cvt_pk_bf16_f32 v186, v204, v205\n"
    "v_cvt_pk_bf16_f32 v187, v206, v207\n"
    "v_pk_mul_f32 v[188:189], v[188:189], s[86:87]\n"
    "v_pk_mul_f32 v[190:191], v[190:191], s[86:87]\n"
    "v_pk_mul_f32 v[208:209], v[208:209], s[86:87]\n"
    "v_pk_mul_f32 v[210:211], v[210:211], s[86:87]\n"
    "v_cvt_pk_bf16_f32 v188, v188, v189\n"
    "v_cvt_pk_bf16_f32 v189, v190, v191\n"
    "v_cvt_pk_bf16_f32 v190, v208, v209\n"
    "v_cvt_pk_bf16_f32 v191, v210, v211\n"
    "v_pk_mul_f32 v[192:193], v[192:193], s[86:87]\n"
    "v_pk_mul_f32 v[194:195], v[194:195], s[86:87]\n"
    "v_pk_mul_f32 v[212:213], v[212:213], s[86:87]\n"
    "v_pk_mul_f32 v[214:215], v[214:215], s[86:87]\n"
    "v_cvt_pk_bf16_f32 v192, v192, v193\n"
    "v_cvt_pk_bf16_f32 v193, v194, v195\n"
    "v_cvt_pk_bf16_f32 v194, v212, v213\n"
    "v_cvt_pk_bf16_f32 v195, v214, v215\n"
    "v_pk_mul_f32 v[216:217], v[216:217], s[86:87]\n"
    "v_pk_mul_f32 v[218:219], v[218:219], s[86:87]\n"
    "v_pk_mul_f32 v[236:237], v[236:237], s[86:87]\n"
    "v_pk_mul_f32 v[238:239], v[238:239], s[86:87]\n"
    "v_cvt_pk_bf16_f32 v216, v216, v217\n"
    "v_cvt_pk_bf16_f32 v217, v218, v219\n"
    "v_cvt_pk_bf16_f32 v218, v236, v237\n"
    "v_cvt_pk_bf16_f32 v219, v238, v239\n"
    "ds_write_b128 v94, v[184:187] offset:0\n"
    "ds_write_b128 v94, v[188:191] offset:1024\n"
    "ds_write_b128 v94, v[192:195] offset:2048\n"
    "ds_write_b128 v94, v[216:219] offset:3072\n"
    "v_pk_mul_f32 v[220:221], v[220:221], s[86:87]\n"
    "v_pk_mul_f32 v[222:223], v[222:223], s[86:87]\n"
    "v_pk_mul_f32 v[240:241], v[240:241], s[86:87]\n"
    "v_pk_mul_f32 v[242:243], v[242:243], s[86:87]\n"
    "v_cvt_pk_bf16_f32 v220, v220, v221\n"
    "v_cvt_pk_bf16_f32 v221, v222, v223\n"
    "v_cvt_pk_bf16_f32 v222, v240, v241\n"
    "v_cvt_pk_bf16_f32 v223, v242, v243\n"
    "ds_write_b128 v94, v[220:223] offset:8192\n"
    "v_pk_mul_f32 v[224:225], v[224:225], s[86:87]\n"
    "v_pk_mul_f32 v[226:227], v[226:227], s[86:87]\n"
    "v_pk_mul_f32 v[244:245], v[244:245], s[86:87]\n"
    "v_pk_mul_f32 v[246:247], v[246:247], s[86:87]\n"
    "v_cvt_pk_bf16_f32 v224, v224, v225\n"
    "v_cvt_pk_bf16_f32 v225, v226, v227\n"
    "v_cvt_pk_bf16_f32 v226, v244, v245\n"
    "v_cvt_pk_bf16_f32 v227, v246, v247\n"
    "ds_write_b128 v94, v[224:227] offset:9216\n"
    "v_pk_mul_f32 v[228:229], v[228:229], s[86:87]\n"
    "v_pk_mul_f32 v[230:231], v[230:231], s[86:87]\n"
    "v_pk_mul_f32 v[248:249], v[248:249], s[86:87]\n"
    "v_pk_mul_f32 v[250:251], v[250:251], s[86:87]\n"
    "v_cvt_pk_bf16_f32 v228, v228, v229\n"
    "v_cvt_pk_bf16_f32 v229, v230, v231\n"
    "v_cvt_pk_bf16_f32 v230, v248, v249\n"
    "v_cvt_pk_bf16_f32 v231, v250, v251\n"
    "ds_write_b128 v94, v[228:231] offset:10240\n"
    "v_pk_mul_f32 v[232:233], v[232:233], s[86:87]\n"
    "v_pk_mul_f32 v[234:235], v[234:235], s[86:87]\n"
    "v_pk_mul_f32 v[252:253], v[252:253], s[86:87]\n"
    "v_pk_mul_f32 v[254:255], v[254:255], s[86:87]\n"
    "v_cvt_pk_bf16_f32 v232, v232, v233\n"
    "v_cvt_pk_bf16_f32 v233, v234, v235\n"
    "v_cvt_pk_bf16_f32 v234, v252, v253\n"
    "v_cvt_pk_bf16_f32 v235, v254, v255\n"
    "ds_write_b128 v94, v[232:235] offset:11264\n"
    "s_waitcnt vmcnt(0) lgkmcnt(0)\n"
    "s_barrier\n"
    "ds_read_b128 a[180:183], v2 offset:0\n"
    "ds_read_b128 a[184:187], v3 offset:0\n"
    "ds_read_b128 a[188:191], v2 offset:4096\n"
    "ds_read_b128 a[192:195], v3 offset:4096\n"
    "ds_read_b128 a[0:3], v90 offset:12288\n"
    "ds_read_b128 a[4:7], v90 offset:13312\n"
    "ds_read_b128 a[8:11], v90 offset:14336\n"
    "ds_read_b128 a[12:15], v90 offset:15360\n"
    "ds_read_b128 a[16:19], v91 offset:0\n"
    "ds_read_b128 a[20:23], v91 offset:1024\n"
    "ds_read_b128 a[24:27], v91 offset:2048\n"
    "ds_read_b128 a[28:31], v91 offset:3072\n"
    "ds_read_b128 a[32:35], v90 offset:4096\n"
    "s_waitcnt lgkmcnt(8)\n"
    "v_mfma_f32_16x16x32_bf16 v[40:43], a[180:183], a[0:3], 0\n"
    "ds_read_b128 a[36:39], v90 offset:5120\n"
    "ds_read_b128 a[40:43], v90 offset:6144\n"
    "s_waitcnt lgkmcnt(5)\n"
    "v_mfma_f32_16x16x32_bf16 v[40:43], a[184:187], a[20:23], v[40:43]\n"
    "ds_read_b128 a[60:63], v90 offset:3072\n"
    "ds_read_b128 a[44:47], v90 offset:7168\n"
    "v_mfma_f32_16x16x32_bf16 v[44:47], a[180:183], a[4:7], 0\n"
    "ds_read_b128 a[64:67], v90 offset:8192\n"
    "ds_read_b128 a[48:51], v90 offset:0\n"
    "s_waitcnt lgkmcnt(8)\n"
    "v_mfma_f32_16x16x32_bf16 v[44:47], a[184:187], a[24:27], v[44:47]\n"
    "ds_read_b128 a[68:71], v90 offset:9216\n"
    "ds_read_b128 a[52:55], v90 offset:1024\n"
    "v_mfma_f32_16x16x32_bf16 v[48:51], a[180:183], a[8:11], 0\n"
    "ds_read_b128 a[72:75], v90 offset:10240\n"
    "ds_read_b128 a[56:59], v90 offset:2048\n"
    "s_waitcnt lgkmcnt(11)\n"
    "v_mfma_f32_16x16x32_bf16 v[48:51], a[184:187], a[28:31], v[48:51]\n"
    "ds_read_b128 a[76:79], v90 offset:11264\n"
    "v_mfma_f32_16x16x32_bf16 v[52:55], a[180:183], a[12:15], 0\n"
    "ds_read_b128 a[80:83], v92 offset:12288\n"
    "s_waitcnt lgkmcnt(12)\n"
    "v_mfma_f32_16x16x32_bf16 v[52:55], a[184:187], a[32:35], v[52:55]\n"
    "ds_read_b128 a[100:103], v93 offset:1024\n"
    "v_mfma_f32_16x16x32_bf16 v[56:59], a[180:183], a[16:19], 0\n"
    "ds_read_b128 a[180:183], v2 offset:8192\n"
    "ds_read_b128 a[84:87], v92 offset:13312\n"
    "s_waitcnt lgkmcnt(13)\n"
    "v_mfma_f32_16x16x32_bf16 v[56:59], a[184:187], a[36:39], v[56:59]\n"
    "ds_read_b128 a[184:187], v3 offset:8192\n"
    "ds_read_b128 a[104:107], v93 offset:2048\n"
    "v_mfma_f32_16x16x32_bf16 v[40:43], a[188:191], a[40:43], v[40:43]\n"
    "ds_read_b128 a[88:91], v92 offset:14336\n"
    "s_waitcnt lgkmcnt(12)\n"
    "v_mfma_f32_16x16x32_bf16 v[40:43], a[192:195], a[60:63], v[40:43]\n"
    "ds_read_b128 a[108:111], v93 offset:3072\n"
    "v_mfma_f32_16x16x32_bf16 v[44:47], a[188:191], a[44:47], v[44:47]\n"
    "ds_read_b128 a[92:95], v92 offset:15360\n"
    "v_mfma_f32_16x16x32_bf16 v[44:47], a[192:195], a[64:67], v[44:47]\n"
    "ds_read_b128 a[112:115], v92 offset:4096\n"
    "v_mfma_f32_16x16x32_bf16 v[48:51], a[188:191], a[48:51], v[48:51]\n"
    "ds_read_b128 a[96:99], v93 offset:0\n"
    "s_waitcnt lgkmcnt(11)\n"
    "v_mfma_f32_16x16x32_bf16 v[48:51], a[192:195], a[68:71], v[48:51]\n"
    "ds_read_b128 a[116:119], v92 offset:5120\n"
    "v_mfma_f32_16x16x32_bf16 v[52:55], a[188:191], a[52:55], v[52:55]\n"
    "ds_read_b128 a[120:123], v92 offset:6144\n"
    "v_mfma_f32_16x16x32_bf16 v[52:55], a[192:195], a[72:75], v[52:55]\n"
    "ds_read_b128 a[140:143], v92 offset:3072\n"
    "v_mfma_f32_16x16x32_bf16 v[56:59], a[188:191], a[56:59], v[56:59]\n"
    "ds_read_b128 a[188:191], v2 offset:12288\n"
    "ds_read_b128 a[124:127], v92 offset:7168\n"
    "v_mfma_f32_16x16x32_bf16 v[56:59], a[192:195], a[76:79], v[56:59]\n"
    "ds_read_b128 a[192:195], v3 offset:12288\n"
    "ds_read_b128 a[144:147], v92 offset:8192\n"
    "s_waitcnt lgkmcnt(11)\n"
    "v_mfma_f32_16x16x32_bf16 v[40:43], a[180:183], a[80:83], v[40:43]\n"
    "ds_read_b128 a[128:131], v92 offset:0\n"
    "v_mfma_f32_16x16x32_bf16 v[40:43], a[184:187], a[100:103], v[40:43]\n"
    "ds_read_b128 a[148:151], v92 offset:9216\n"
    "v_mfma_f32_16x16x32_bf16 v[44:47], a[180:183], a[84:87], v[44:47]\n"
    "s_cmp_lg_u32 %2, 0\n"
    "s_cbranch_scc1 NON_%=\n"
    "v_mov_b32 v16, s76\n"
    "v_mov_b32 v17, s76\n"
    "v_mov_b32 v18, s76\n"
    "v_mov_b32 v19, s76\n"
    "v_lshlrev_b32 v15, 4, v0\n"
    "s_lshl_b32 s79, s22, 11\n"
    "buffer_store_dwordx4 v[16:19], v15, s[44:47], s79 offen sc1\n"
    "buffer_store_dwordx4 v[16:19], v15, s[44:47], s79 offen offset:1024 sc1\n"
    "v_add_u32 v22, 1, v22\n"
    "v_add_u32 v15, %3, v23\n"
    "v_add_u32 v15, 163584, v15\n"
    "ds_write_b32 v15, v22 offset:64\n"
    "v_lshlrev_b32 v23, 4, v23\n"
    "s_lshl_b32 s79, s57, 2\n"
    "s_add_u32 s79, s79, 1179648\n"
    "buffer_store_dword v22, v23, s[52:55], s79 offen sc1\n"
    "NON_%=:\n"
    "s_add_u32 s76, s76, 1\n"
    "v_mfma_f32_16x16x32_bf16 v[44:47], a[184:187], a[104:107], v[44:47]\n"
    "ds_read_b128 a[132:135], v92 offset:1024\n"
    "ds_read_b128 a[152:155], v92 offset:10240\n"
    "v_mfma_f32_16x16x32_bf16 v[48:51], a[180:183], a[88:91], v[48:51]\n"
    "ds_read_b128 a[136:139], v92 offset:2048\n"
    "s_waitcnt lgkmcnt(5)\n"
    "v_mfma_f32_16x16x32_bf16 v[48:51], a[184:187], a[108:111], v[48:51]\n"
    "ds_read_b128 a[156:159], v92 offset:11264\n"
    "v_mfma_f32_16x16x32_bf16 v[52:55], a[180:183], a[92:95], v[52:55]\n"
    "v_mfma_f32_16x16x32_bf16 v[52:55], a[184:187], a[112:115], v[52:55]\n"
    "v_mfma_f32_16x16x32_bf16 v[56:59], a[180:183], a[96:99], v[56:59]\n"
    "ds_read_b128 a[180:183], v2 offset:2048\n"
    "v_mfma_f32_16x16x32_bf16 v[56:59], a[184:187], a[116:119], v[56:59]\n"
    "ds_read_b128 a[184:187], v3 offset:2048\n"
    "v_mfma_f32_16x16x32_bf16 v[40:43], a[188:191], a[120:123], v[40:43]\n"
    "v_mfma_f32_16x16x32_bf16 v[40:43], a[192:195], a[140:143], v[40:43]\n"
    "v_mfma_f32_16x16x32_bf16 v[44:47], a[188:191], a[124:127], v[44:47]\n"
    "v_mfma_f32_16x16x32_bf16 v[44:47], a[192:195], a[144:147], v[44:47]\n"
    "s_waitcnt lgkmcnt(0)\n"
    "s_barrier\n"
    "v_mfma_f32_16x16x32_bf16 v[48:51], a[188:191], a[128:131], v[48:51]\n"
    "s_add_u32 m0, s70, 16384\n"
    "v_mfma_f32_16x16x32_bf16 v[48:51], a[192:195], a[148:151], v[48:51]\n"
    "buffer_load_dwordx4 v1, s[36:39], s66 offen nt lds\n"
    "v_mfma_f32_16x16x32_bf16 v[52:55], a[188:191], a[132:135], v[52:55]\n"
    "v_mfma_f32_16x16x32_bf16 v[52:55], a[192:195], a[152:155], v[52:55]\n"
    "v_mfma_f32_16x16x32_bf16 v[56:59], a[188:191], a[136:139], v[56:59]\n"
    "buffer_load_dwordx4 v1, s[36:39], s66 offen offset:1024 nt lds\n"
    "v_mfma_f32_16x16x32_bf16 v[56:59], a[192:195], a[156:159], v[56:59]\n"
    "ds_read_b128 a[188:191], v2 offset:6144\n"
    "ds_read_b128 a[192:195], v3 offset:6144\n"
    "v_mfma_f32_16x16x32_bf16 v[60:63], a[180:183], a[0:3], 0\n"
    "v_max3_f32 v84, v40, v41, v42\n"
    "v_mfma_f32_16x16x32_bf16 v[60:63], a[184:187], a[20:23], v[60:63]\n"
    "buffer_load_dwordx4 v1, s[36:39], s66 offen offset:2048 nt lds\n"
    "v_mfma_f32_16x16x32_bf16 v[64:67], a[180:183], a[4:7], 0\n"
    "v_max3_f32 v85, v44, v45, v46\n"
    "v_max3_f32 v86, v48, v49, v50\n"
    "v_mfma_f32_16x16x32_bf16 v[64:67], a[184:187], a[24:27], v[64:67]\n"
    "v_max3_f32 v87, v52, v53, v54\n"
    "v_mfma_f32_16x16x32_bf16 v[68:71], a[180:183], a[8:11], 0\n"
    "buffer_load_dwordx4 v1, s[36:39], s66 offen offset:3072 nt lds\n"
    "v_mfma_f32_16x16x32_bf16 v[68:71], a[184:187], a[28:31], v[68:71]\n"
    "s_add_u32 s66, s66, 32768\n"
    "v_max3_f32 v88, v56, v57, v58\n"
    "v_mfma_f32_16x16x32_bf16 v[72:75], a[180:183], a[12:15], 0\n"
    "s_add_u32 m0, s70, 20480\n"
    "v_max_f32 v84, v84, v43\n"
    "v_mfma_f32_16x16x32_bf16 v[72:75], a[184:187], a[32:35], v[72:75]\n"
    "buffer_load_dwordx4 v1, s[36:39], s67 offen nt lds\n"
    "v_mfma_f32_16x16x32_bf16 v[76:79], a[180:183], a[16:19], 0\n"
    "ds_read_b128 a[180:183], v2 offset:10240\n"
    "v_max_f32 v85, v85, v47\n"
    "v_mfma_f32_16x16x32_bf16 v[76:79], a[184:187], a[36:39], v[76:79]\n"
    "ds_read_b128 a[184:187], v3 offset:10240\n"
    "v_max_f32 v86, v86, v51\n"
    "s_waitcnt lgkmcnt(2)\n"
    "v_mfma_f32_16x16x32_bf16 v[60:63], a[188:191], a[40:43], v[60:63]\n"
    "buffer_load_dwordx4 v1, s[36:39], s67 offen offset:1024 nt lds\n"
    "v_mfma_f32_16x16x32_bf16 v[60:63], a[192:195], a[60:63], v[60:63]\n"
    "v_max_f32 v87, v87, v55\n"
    "v_max_f32 v88, v88, v59\n"
    "v_mfma_f32_16x16x32_bf16 v[64:67], a[188:191], a[44:47], v[64:67]\n"
    "v_mov_b32 v89, v84\n"
    "v_mov_b32 v90, v85\n"
    "v_mfma_f32_16x16x32_bf16 v[64:67], a[192:195], a[64:67], v[64:67]\n"
    "buffer_load_dwordx4 v1, s[36:39], s67 offen offset:2048 nt lds\n"
    "v_mfma_f32_16x16x32_bf16 v[68:71], a[188:191], a[48:51], v[68:71]\n"
    "v_mov_b32 v91, v86\n"
    "v_mov_b32 v92, v87\n"
    "v_mfma_f32_16x16x32_bf16 v[68:71], a[192:195], a[68:71], v[68:71]\n"
    "v_mov_b32 v93, v88\n"
    "v_permlane16_swap_b32 v84, v89\n"
    "v_mfma_f32_16x16x32_bf16 v[72:75], a[188:191], a[52:55], v[72:75]\n"
    "v_permlane16_swap_b32 v85, v90\n"
    "v_permlane16_swap_b32 v86, v91\n"
    "v_permlane16_swap_b32 v87, v92\n"
    "v_permlane16_swap_b32 v88, v93\n"
    "v_max_f32 v84, v84, v89\n"
    "v_max_f32 v85, v85, v90\n"
    "v_max_f32 v86, v86, v91\n"
    "v_max_f32 v87, v87, v92\n"
    "v_max_f32 v88, v88, v93\n"
    "v_mov_b32 v89, v84\n"
    "v_mov_b32 v90, v85\n"
    "v_mov_b32 v91, v86\n"
    "v_mov_b32 v92, v87\n"
    "v_mov_b32 v93, v88\n"
    "v_permlane32_swap_b32 v84, v89\n"
    "v_permlane32_swap_b32 v85, v90\n"
    "v_permlane32_swap_b32 v86, v91\n"
    "v_permlane32_swap_b32 v87, v92\n"
    "v_permlane32_swap_b32 v88, v93\n"
    "v_max_f32 v84, v84, v89\n"
    "v_max_f32 v85, v85, v90\n"
    "v_max_f32 v86, v86, v91\n"
    "v_max_f32 v87, v87, v92\n"
    "v_max_f32 v88, v88, v93\n"
    "buffer_load_dwordx4 v1, s[36:39], s67 offen offset:3072 nt lds\n"
    "v_mfma_f32_16x16x32_bf16 v[72:75], a[192:195], a[72:75], v[72:75]\n"
    "ds_write_b32 v12, v84 offset:0\n"
    "ds_write_b32 v12, v85 offset:256\n"
    "ds_write_b32 v12, v86 offset:512\n"
    "ds_write_b32 v12, v87 offset:768\n"
    "ds_write_b32 v12, v88 offset:1024\n"
    "s_add_u32 s67, s67, 32768\n"
    "v_mfma_f32_16x16x32_bf16 v[76:79], a[188:191], a[56:59], v[76:79]\n"
    "s_waitcnt lgkmcnt(0)\n"
    "s_barrier\n"
    "ds_read_b128 a[188:191], v2 offset:14336\n"
    "v_mfma_f32_16x16x32_bf16 v[76:79], a[192:195], a[76:79], v[76:79]\n"
    "s_add_u32 m0, s72, 0\n"
    "ds_read_b128 a[192:195], v3 offset:14336\n"
    "v_mfma_f32_16x16x32_bf16 v[60:63], a[180:183], a[80:83], v[60:63]\n"
    "buffer_load_dwordx4 v1, s[40:43], s69 offen nt lds\n"
    "v_mfma_f32_16x16x32_bf16 v[60:63], a[184:187], a[100:103], v[60:63]\n"
    "ds_read_b32 v89, v13 offset:0\n"
    "ds_read_b32 v90, v13 offset:256\n"
    "v_mfma_f32_16x16x32_bf16 v[64:67], a[180:183], a[84:87], v[64:67]\n"
    "ds_read_b32 v91, v13 offset:512\n"
    "ds_read_b32 v92, v13 offset:768\n"
    "ds_read_b32 v93, v13 offset:1024\n"
    "v_mfma_f32_16x16x32_bf16 v[64:67], a[184:187], a[104:107], v[64:67]\n"
    "s_waitcnt lgkmcnt(4)\n"
    "v_max_f32 v84, v84, v89\n"
    "v_sub_f32 v40, v40, v84\n"
    "v_mfma_f32_16x16x32_bf16 v[68:71], a[180:183], a[88:91], v[68:71]\n"
    "buffer_load_dwordx4 v1, s[40:43], s69 offen offset:1024 nt lds\n"
    "v_mfma_f32_16x16x32_bf16 v[68:71], a[184:187], a[108:111], v[68:71]\n"
    "v_sub_f32 v41, v41, v84\n"
    "v_sub_f32 v42, v42, v84\n"
    "v_mfma_f32_16x16x32_bf16 v[72:75], a[180:183], a[92:95], v[72:75]\n"
    "v_sub_f32 v43, v43, v84\n"
    "v_mfma_f32_16x16x32_bf16 v[72:75], a[184:187], a[112:115], v[72:75]\n"
    "v_mfma_f32_16x16x32_bf16 v[76:79], a[180:183], a[96:99], v[76:79]\n"
    "buffer_load_dwordx4 v1, s[40:43], s69 offen offset:2048 nt lds\n"
    "v_mfma_f32_16x16x32_bf16 v[76:79], a[184:187], a[116:119], v[76:79]\n"
    "v_mfma_f32_16x16x32_bf16 v[60:63], a[188:191], a[120:123], v[60:63]\n"
    "s_nop 7\n"
    "v_sub_f32 v60, v60, v84\n"
    "v_sub_f32 v61, v61, v84\n"
    "v_sub_f32 v62, v62, v84\n"
    "v_sub_f32 v63, v63, v84\n"
    "v_sub_f32 v20, 0, v84\n"
    "v_sub_f32 v21, 0, v84\n"
    "v_sub_f32 v22, 0, v84\n"
    "v_sub_f32 v23, 0, v84\n"
    "s_waitcnt lgkmcnt(0)\n"
    "v_max_f32 v85, v85, v90\n"
    "v_sub_f32 v44, v44, v85\n"
    "v_sub_f32 v45, v45, v85\n"
    "v_sub_f32 v46, v46, v85\n"
    "v_sub_f32 v47, v47, v85\n"
    "v_sub_f32 v64, v64, v85\n"
    "v_sub_f32 v65, v65, v85\n"
    "v_sub_f32 v66, v66, v85\n"
    "v_sub_f32 v67, v67, v85\n"
    "v_sub_f32 v24, 0, v85\n"
    "v_sub_f32 v25, 0, v85\n"
    "v_sub_f32 v26, 0, v85\n"
    "v_sub_f32 v27, 0, v85\n"
    "v_max_f32 v86, v86, v91\n"
    "v_sub_f32 v48, v48, v86\n"
    "v_sub_f32 v49, v49, v86\n"
    "v_sub_f32 v50, v50, v86\n"
    "v_sub_f32 v51, v51, v86\n"
    "v_sub_f32 v68, v68, v86\n"
    "v_sub_f32 v69, v69, v86\n"
    "v_sub_f32 v70, v70, v86\n"
    "v_sub_f32 v71, v71, v86\n"
    "v_sub_f32 v28, 0, v86\n"
    "v_sub_f32 v29, 0, v86\n"
    "v_sub_f32 v30, 0, v86\n"
    "v_sub_f32 v31, 0, v86\n"
    "v_max_f32 v87, v87, v92\n"
    "v_sub_f32 v52, v52, v87\n"
    "v_sub_f32 v53, v53, v87\n"
    "v_sub_f32 v54, v54, v87\n"
    "v_sub_f32 v55, v55, v87\n"
    "v_sub_f32 v72, v72, v87\n"
    "v_sub_f32 v73, v73, v87\n"
    "v_sub_f32 v74, v74, v87\n"
    "v_sub_f32 v75, v75, v87\n"
    "v_sub_f32 v32, 0, v87\n"
    "v_sub_f32 v33, 0, v87\n"
    "v_sub_f32 v34, 0, v87\n"
    "v_sub_f32 v35, 0, v87\n"
    "v_max_f32 v88, v88, v93\n"
    "v_sub_f32 v56, v56, v88\n"
    "v_sub_f32 v57, v57, v88\n"
    "v_sub_f32 v58, v58, v88\n"
    "v_sub_f32 v59, v59, v88\n"
    "v_sub_f32 v76, v76, v88\n"
    "v_sub_f32 v77, v77, v88\n"
    "v_sub_f32 v78, v78, v88\n"
    "v_sub_f32 v79, v79, v88\n"
    "v_sub_f32 v36, 0, v88\n"
    "v_sub_f32 v37, 0, v88\n"
    "v_sub_f32 v38, 0, v88\n"
    "v_sub_f32 v39, 0, v88\n"
    "v_mfma_f32_16x16x32_bf16 v[60:63], a[192:195], a[140:143], v[60:63]\n"
    "s_barrier\n"
    "v_mfma_f32_16x16x32_bf16 v[64:67], a[188:191], a[124:127], v[64:67]\n"
    "buffer_load_dwordx4 v1, s[40:43], s69 offen offset:3072 nt lds\n"
    "v_mfma_f32_16x16x32_bf16 v[64:67], a[192:195], a[144:147], v[64:67]\n"
    "s_add_u32 s69, s69, 32768\n"
    "ds_write_b32 v12, v20 offset:0\n"
    "v_mfma_f32_16x16x32_bf16 v[68:71], a[188:191], a[128:131], v[68:71]\n"
    "v_exp_f32 v40, v40\n"
    "v_mfma_f32_16x16x32_bf16 v[68:71], a[192:195], a[148:151], v[68:71]\n"
    "s_add_u32 m0, s71, 8192\n"
    "ds_write_b32 v12, v24 offset:256\n"
    "v_mfma_f32_16x16x32_bf16 v[72:75], a[188:191], a[132:135], v[72:75]\n"
    "buffer_load_dwordx4 v1, s[40:43], s68 offen nt lds\n"
    "v_mfma_f32_16x16x32_bf16 v[72:75], a[192:195], a[152:155], v[72:75]\n"
    "v_exp_f32 v41, v41\n"
    "v_mfma_f32_16x16x32_bf16 v[76:79], a[188:191], a[136:139], v[76:79]\n"
    "v_exp_f32 v42, v42\n"
    "v_mfma_f32_16x16x32_bf16 v[76:79], a[192:195], a[156:159], v[76:79]\n"
    "v_exp_f32 v43, v43\n"
    "buffer_load_dwordx4 v1, s[40:43], s68 offen offset:1024 nt lds\n"
    "ds_write_b32 v12, v28 offset:512\n"
    "ds_write_b32 v12, v32 offset:768\n"
    "ds_write_b32 v12, v36 offset:1024\n"
    "v_mov_b32 v91, 0\n"
    "ds_write_b32 v12, v91 offset:1280\n"
    "v_cvt_pk_bf16_f32 v40, v40, v41\n"
    "v_cvt_pk_bf16_f32 v41, v42, v43\n"
    "v_exp_f32 v44, v44\n"
    "v_exp_f32 v45, v45\n"
    "v_exp_f32 v46, v46\n"
    "v_exp_f32 v47, v47\n"
    "v_cvt_pk_bf16_f32 v44, v44, v45\n"
    "v_cvt_pk_bf16_f32 v45, v46, v47\n"
    "v_exp_f32 v48, v48\n"
    "v_exp_f32 v49, v49\n"
    "v_exp_f32 v50, v50\n"
    "v_exp_f32 v51, v51\n"
    "v_cvt_pk_bf16_f32 v48, v48, v49\n"
    "v_cvt_pk_bf16_f32 v49, v50, v51\n"
    "v_exp_f32 v52, v52\n"
    "v_exp_f32 v53, v53\n"
    "v_exp_f32 v54, v54\n"
    "v_exp_f32 v55, v55\n"
    "v_cvt_pk_bf16_f32 v52, v52, v53\n"
    "v_cvt_pk_bf16_f32 v53, v54, v55\n"
    "v_exp_f32 v56, v56\n"
    "v_exp_f32 v57, v57\n"
    "v_exp_f32 v58, v58\n"
    "v_exp_f32 v59, v59\n"
    "v_cvt_pk_bf16_f32 v56, v56, v57\n"
    "v_cvt_pk_bf16_f32 v57, v58, v59\n"
    "buffer_load_dwordx4 v1, s[40:43], s68 offen offset:2048 nt lds\n"
    "v_max3_f32 v10, v60, v61, v61\n"
    "v_max3_f32 v10, v10, v62, v63\n"
    "v_max3_f32 v10, v10, v64, v65\n"
    "v_max3_f32 v10, v10, v66, v67\n"
    "v_max3_f32 v10, v10, v68, v69\n"
    "v_max3_f32 v10, v10, v70, v71\n"
    "v_max3_f32 v10, v10, v72, v73\n"
    "v_max3_f32 v10, v10, v74, v75\n"
    "v_max3_f32 v10, v10, v76, v77\n"
    "v_max3_f32 v10, v10, v78, v79\n"
    "v_cmp_lt_f32 vcc, s85, v10\n"
    "s_cbranch_vccnz R1_f_%=\n"
    "R1R_f_%=:\n"
    "buffer_load_dwordx4 v1, s[40:43], s68 offen offset:3072 nt lds\n"
    "s_add_u32 s68, s68, 32768\n"
    "v_exp_f32 v60, v60\n"
    "v_exp_f32 v61, v61\n"
    "v_exp_f32 v62, v62\n"
    "v_exp_f32 v63, v63\n"
    "v_cvt_pk_bf16_f32 v42, v60, v61\n"
    "v_cvt_pk_bf16_f32 v43, v62, v63\n"
    "v_exp_f32 v64, v64\n"
    "v_exp_f32 v65, v65\n"
    "v_exp_f32 v66, v66\n"
    "v_exp_f32 v67, v67\n"
    "v_cvt_pk_bf16_f32 v46, v64, v65\n"
    "v_cvt_pk_bf16_f32 v47, v66, v67\n"
    "ds_read_b64_tr_b16 a[196:197], v4 offset:0\n"
    "ds_read_b64_tr_b16 a[198:199], v4 offset:2048\n"
    "v_exp_f32 v68, v68\n"
    "v_exp_f32 v69, v69\n"
    "s_add_u32 m0, s70, 0\n"
    "s_nop 0\n"
    "buffer_load_dwordx4 v1, s[36:39], s66 offen nt lds\n"
    "ds_read_b64_tr_b16 a[200:201], v5 offset:0\n"
    "ds_read_b64_tr_b16 a[202:203], v5 offset:2048\n"
    "v_exp_f32 v70, v70\n"
    "v_exp_f32 v71, v71\n"
    "v_cvt_pk_bf16_f32 v50, v68, v69\n"
    "v_cvt_pk_bf16_f32 v51, v70, v71\n"
    "v_exp_f32 v72, v72\n"
    "v_exp_f32 v73, v73\n"
    "v_exp_f32 v74, v74\n"
    "buffer_load_dwordx4 v1, s[36:39], s66 offen offset:1024 nt lds\n"
    "v_exp_f32 v75, v75\n"
    "v_cvt_pk_bf16_f32 v54, v72, v73\n"
    "v_cvt_pk_bf16_f32 v55, v74, v75\n"
    "v_exp_f32 v76, v76\n"
    "v_exp_f32 v77, v77\n"
    "v_exp_f32 v78, v78\n"
    "v_exp_f32 v79, v79\n"
    "buffer_load_dwordx4 v1, s[36:39], s66 offen offset:2048 nt lds\n"
    "s_waitcnt lgkmcnt(2)\n"
    "v_mfma_f32_16x16x32_bf16 v[96:99], a[196:199], v[40:43], 0\n"
    "ds_read_b64_tr_b16 a[204:205], v6 offset:0\n"
    "ds_read_b64_tr_b16 a[206:207], v6 offset:2048\n"
    "v_mfma_f32_16x16x32_bf16 v[100:103], a[196:199], v[44:47], 0\n"
    "v_cvt_pk_bf16_f32 v58, v76, v77\n"
    "v_cvt_pk_bf16_f32 v59, v78, v79\n"
    "ds_write_b128 v8, v[40:43] offset:0\n"
    "v_mfma_f32_16x16x32_bf16 v[104:107], a[196:199], v[48:51], 0\n"
    "ds_read_b64_tr_b16 a[208:209], v7 offset:0\n"
    "ds_read_b64_tr_b16 a[210:211], v7 offset:2048\n"
    "v_mfma_f32_16x16x32_bf16 v[108:111], a[196:199], v[52:55], 0\n"
    "ds_read_b64_tr_b16 a[212:213], v4 offset:4096\n"
    "ds_read_b64_tr_b16 a[214:215], v4 offset:6144\n"
    "v_mfma_f32_16x16x32_bf16 v[112:115], a[196:199], v[56:59], 0\n"
    "ds_read_b64_tr_b16 a[216:217], v5 offset:4096\n"
    "ds_read_b64_tr_b16 a[218:219], v5 offset:6144\n"
    "s_waitcnt lgkmcnt(9)\n"
    "v_mfma_f32_16x16x32_bf16 v[116:119], a[200:203], v[40:43], 0\n"
    "ds_read_b64_tr_b16 a[220:221], v6 offset:4096\n"
    "ds_read_b64_tr_b16 a[222:223], v6 offset:6144\n"
    "v_mfma_f32_16x16x32_bf16 v[120:123], a[200:203], v[44:47], 0\n"
    "ds_write_b128 v8, v[44:47] offset:1024\n"
    "v_mfma_f32_16x16x32_bf16 v[124:127], a[200:203], v[48:51], 0\n"
    "ds_read_b64_tr_b16 a[224:225], v7 offset:4096\n"
    "ds_read_b64_tr_b16 a[226:227], v7 offset:6144\n"
    "v_mfma_f32_16x16x32_bf16 v[128:131], a[200:203], v[52:55], 0\n"
    "buffer_load_dwordx4 v1, s[36:39], s66 offen offset:3072 nt lds\n"
    "v_mfma_f32_16x16x32_bf16 v[132:135], a[200:203], v[56:59], 0\n"
    "s_add_u32 s66, s66, 32768\n"
    "s_waitcnt lgkmcnt(12)\n"
    "v_mfma_f32_16x16x32_bf16 v[136:139], a[204:207], v[40:43], 0\n"
    "v_mfma_f32_16x16x32_bf16 v[140:143], a[204:207], v[44:47], 0\n"
    "ds_write_b128 v8, v[48:51] offset:2048\n"
    "v_mfma_f32_16x16x32_bf16 v[144:147], a[204:207], v[48:51], 0\n"
    "v_mfma_f32_16x16x32_bf16 v[148:151], a[204:207], v[52:55], 0\n"
    "v_mfma_f32_16x16x32_bf16 v[152:155], a[204:207], v[56:59], 0\n"
    "s_waitcnt lgkmcnt(4)\n"
    "v_mfma_f32_16x16x32_bf16 v[156:159], a[208:211], v[40:43], 0\n"
    "v_mfma_f32_16x16x32_bf16 v[160:163], a[208:211], v[44:47], 0\n"
    "ds_write_b128 v8, v[52:55] offset:3072\n"
    "v_mfma_f32_16x16x32_bf16 v[164:167], a[208:211], v[48:51], 0\n"
    "s_add_u32 m0, s72, 8192\n"
    "v_mfma_f32_16x16x32_bf16 v[168:171], a[208:211], v[52:55], 0\n"
    "buffer_load_dwordx4 v1, s[40:43], s69 offen nt lds\n"
    "v_mfma_f32_16x16x32_bf16 v[172:175], a[208:211], v[56:59], 0\n"
    "v_mfma_f32_16x16x32_bf16 v[176:179], a[212:215], v[40:43], 0\n"
    "ds_write_b128 v8, v[56:59] offset:4096\n"
    "v_mfma_f32_16x16x32_bf16 v[180:183], a[212:215], v[44:47], 0\n"
    "v_mfma_f32_16x16x32_bf16 v[184:187], a[212:215], v[48:51], 0\n"
    "v_mfma_f32_16x16x32_bf16 v[188:191], a[212:215], v[52:55], 0\n"
    "v_mfma_f32_16x16x32_bf16 v[192:195], a[212:215], v[56:59], 0\n"
    "v_mfma_f32_16x16x32_bf16 v[196:199], a[216:219], v[40:43], 0\n"
    "v_mfma_f32_16x16x32_bf16 v[200:203], a[216:219], v[44:47], 0\n"
    "s_waitcnt vmcnt(9) lgkmcnt(0)\n"
    "s_barrier\n"
    "v_mfma_f32_16x16x32_bf16 v[204:207], a[216:219], v[48:51], 0\n"
    "v_mfma_f32_16x16x32_bf16 v[208:211], a[216:219], v[52:55], 0\n"
    "buffer_load_dwordx4 v1, s[40:43], s69 offen offset:1024 nt lds\n"
    "v_mfma_f32_16x16x32_bf16 v[212:215], a[216:219], v[56:59], 0\n"
    "s_cmp_lg_u32 %2, 0\n"
    "s_cbranch_scc1 NON2_%=\n"
    "v_mov_b32 v16, s76\n"
    "buffer_store_dword v16, off, s[44:47], s97 sc1\n"
    "NON2_%=:\n"
    "v_mfma_f32_16x16x32_bf16 v[216:219], a[220:223], v[40:43], 0\n"
    "ds_read_b128 a[180:183], v2 offset:16384\n"
    "v_mfma_f32_16x16x32_bf16 v[220:223], a[220:223], v[44:47], 0\n"
    "v_mfma_f32_16x16x32_bf16 v[224:227], a[220:223], v[48:51], 0\n"
    "v_mfma_f32_16x16x32_bf16 v[228:231], a[220:223], v[52:55], 0\n"
    "ds_read_b128 a[184:187], v3 offset:16384\n"
    "v_mfma_f32_16x16x32_bf16 v[232:235], a[220:223], v[56:59], 0\n"
    "v_mfma_f32_16x16x32_bf16 v[236:239], a[224:227], v[40:43], 0\n"
    "buffer_load_dwordx4 v1, s[40:43], s69 offen offset:2048 nt lds\n"
    "v_mfma_f32_16x16x32_bf16 v[240:243], a[224:227], v[44:47], 0\n"
    "v_mfma_f32_16x16x32_bf16 v[244:247], a[224:227], v[48:51], 0\n"
    "ds_read_b128 a[188:191], v2 offset:20480\n"
    "v_mfma_f32_16x16x32_bf16 v[248:251], a[224:227], v[52:55], 0\n"
    "v_mfma_f32_16x16x32_bf16 v[252:255], a[224:227], v[56:59], 0\n"
    "v_mfma_f32_16x16x32_bf16 a[160:163], a[244:247], v[40:43], 0\n"
    "v_mfma_f32_16x16x32_bf16 a[164:167], a[244:247], v[44:47], 0\n"
    "ds_read_b128 a[192:195], v3 offset:20480\n"
    "v_mfma_f32_16x16x32_bf16 a[168:171], a[244:247], v[48:51], 0\n"
    "buffer_load_dwordx4 v1, s[40:43], s69 offen offset:3072 nt lds\n"
    "v_mfma_f32_16x16x32_bf16 a[172:175], a[244:247], v[52:55], 0\n"
    "s_add_u32 s69, s69, 32768\n"
    "v_mfma_f32_16x16x32_bf16 a[176:179], a[244:247], v[56:59], 0\n"
    "LOOP_%=:\n"
    "ds_read_b128 v[80:83], v9 offset:0\n"
    "s_waitcnt lgkmcnt(3)\n"
    "v_mfma_f32_16x16x32_bf16 v[40:43], a[180:183], a[0:3], v[20:23]\n"
    "ds_read_b128 a[228:231], v9 offset:1024\n"
    "v_mfma_f32_16x16x32_bf16 v[40:43], a[184:187], a[20:23], v[40:43]\n"
    "ds_read_b128 a[232:235], v9 offset:2048\n"
    "v_mfma_f32_16x16x32_bf16 v[44:47], a[180:183], a[4:7], v[24:27]\n"
    "ds_read_b128 a[236:239], v9 offset:3072\n"
    "v_mfma_f32_16x16x32_bf16 v[44:47], a[184:187], a[24:27], v[44:47]\n"
    "ds_read_b128 a[240:243], v9 offset:4096\n"
    "ds_read_b32 v91, v13 offset:1280\n"
    "v_mfma_f32_16x16x32_bf16 v[48:51], a[180:183], a[8:11], v[28:31]\n"
    "ds_read_b64_tr_b16 a[196:197], v4 offset:32768\n"
    "ds_read_b64_tr_b16 a[198:199], v4 offset:34816\n"
    "v_mfma_f32_16x16x32_bf16 v[48:51], a[184:187], a[28:31], v[48:51]\n"
    "v_mfma_f32_16x16x32_bf16 v[52:55], a[180:183], a[12:15], v[32:35]\n"
    "v_mfma_f32_16x16x32_bf16 v[52:55], a[184:187], a[32:35], v[52:55]\n"
    "s_add_u32 m0, s70, 4096\n"
    "ds_read_b64_tr_b16 a[200:201], v5 offset:32768\n"
    "v_mfma_f32_16x16x32_bf16 v[56:59], a[180:183], a[16:19], v[36:39]\n"
    "buffer_load_dwordx4 v1, s[36:39], s67 offen nt lds\n"
    "v_mfma_f32_16x16x32_bf16 v[56:59], a[184:187], a[36:39], v[56:59]\n"
    "ds_read_b128 a[180:183], v2 offset:24576\n"
    "ds_read_b128 a[184:187], v3 offset:24576\n"
    "s_waitcnt lgkmcnt(9)\n"
    "v_mfma_f32_16x16x32_bf16 v[40:43], a[188:191], a[40:43], v[40:43]\n"
    "ds_read_b64_tr_b16 a[202:203], v5 offset:34816\n"
    "ds_read_b64_tr_b16 a[204:205], v6 offset:32768\n"
    "v_mfma_f32_16x16x32_bf16 v[40:43], a[192:195], a[60:63], v[40:43]\n"
    "ds_read_b64_tr_b16 a[206:207], v6 offset:34816\n"
    "v_mfma_f32_16x16x32_bf16 v[44:47], a[188:191], a[44:47], v[44:47]\n"
    "v_mfma_f32_16x16x32_bf16 v[44:47], a[192:195], a[64:67], v[44:47]\n"
    "ds_read_b64_tr_b16 a[208:209], v7 offset:32768\n"
    "ds_read_b64_tr_b16 a[210:211], v7 offset:34816\n"
    "v_mfma_f32_16x16x32_bf16 v[48:51], a[188:191], a[48:51], v[48:51]\n"
    "v_mfma_f32_16x16x32_bf16 v[48:51], a[192:195], a[68:71], v[48:51]\n"
    "v_mfma_f32_16x16x32_bf16 v[52:55], a[188:191], a[52:55], v[52:55]\n"
    "buffer_load_dwordx4 v1, s[36:39], s67 offen offset:1024 nt lds\n"
    "v_mfma_f32_16x16x32_bf16 v[52:55], a[192:195], a[72:75], v[52:55]\n"
    "ds_read_b64_tr_b16 a[212:213], v4 offset:36864\n"
    "ds_read_b64_tr_b16 a[214:215], v4 offset:38912\n"
    "v_mfma_f32_16x16x32_bf16 v[56:59], a[188:191], a[56:59], v[56:59]\n"
    "ds_read_b128 a[188:191], v2 offset:28672\n"
    "v_mfma_f32_16x16x32_bf16 v[56:59], a[192:195], a[76:79], v[56:59]\n"
    "ds_read_b128 a[192:195], v3 offset:28672\n"
    "ds_read_b64_tr_b16 a[216:217], v5 offset:36864\n"
    "s_waitcnt lgkmcnt(8)\n"
    "v_mfma_f32_16x16x32_bf16 v[40:43], a[180:183], a[80:83], v[40:43]\n"
    "ds_read_b64_tr_b16 a[218:219], v5 offset:38912\n"
    "v_mfma_f32_16x16x32_bf16 v[40:43], a[184:187], a[100:103], v[40:43]\n"
    "v_mfma_f32_16x16x32_bf16 v[44:47], a[180:183], a[84:87], v[44:47]\n"
    "ds_read_b64_tr_b16 a[220:221], v6 offset:36864\n"
    "ds_read_b64_tr_b16 a[222:223], v6 offset:38912\n"
    "v_mfma_f32_16x16x32_bf16 v[44:47], a[184:187], a[104:107], v[44:47]\n"
    "v_mfma_f32_16x16x32_bf16 v[48:51], a[180:183], a[88:91], v[48:51]\n"
    "buffer_load_dwordx4 v1, s[36:39], s67 offen offset:2048 nt lds\n"
    "v_mfma_f32_16x16x32_bf16 v[48:51], a[184:187], a[108:111], v[48:51]\n"
    "ds_read_b64_tr_b16 a[224:225], v7 offset:36864\n"
    "ds_read_b64_tr_b16 a[226:227], v7 offset:38912\n"
    "v_mfma_f32_16x16x32_bf16 v[52:55], a[180:183], a[92:95], v[52:55]\n"
    "v_mfma_f32_16x16x32_bf16 v[52:55], a[184:187], a[112:115], v[52:55]\n"
    "v_mfma_f32_16x16x32_bf16 v[56:59], a[180:183], a[96:99], v[56:59]\n"
    "ds_read_b128 a[180:183], v2 offset:18432\n"
    "v_mfma_f32_16x16x32_bf16 v[56:59], a[184:187], a[116:119], v[56:59]\n"
    "ds_read_b128 a[184:187], v3 offset:18432\n"
    "s_waitcnt lgkmcnt(6)\n"
    "v_mfma_f32_16x16x32_bf16 v[40:43], a[188:191], a[120:123], v[40:43]\n"
    "v_mfma_f32_16x16x32_bf16 v[40:43], a[192:195], a[140:143], v[40:43]\n"
    "v_mfma_f32_16x16x32_bf16 v[44:47], a[188:191], a[124:127], v[44:47]\n"
    "buffer_load_dwordx4 v1, s[36:39], s67 offen offset:3072 nt lds\n"
    "v_mfma_f32_16x16x32_bf16 v[44:47], a[192:195], a[144:147], v[44:47]\n"
    "s_add_u32 s67, s67, 32768\n"
    "v_mfma_f32_16x16x32_bf16 v[48:51], a[188:191], a[128:131], v[48:51]\n"
    "v_mfma_f32_16x16x32_bf16 v[48:51], a[192:195], a[148:151], v[48:51]\n"
    "v_mfma_f32_16x16x32_bf16 v[52:55], a[188:191], a[132:135], v[52:55]\n"
    "v_mfma_f32_16x16x32_bf16 v[52:55], a[192:195], a[152:155], v[52:55]\n"
    "v_mfma_f32_16x16x32_bf16 v[56:59], a[188:191], a[136:139], v[56:59]\n"
    "s_add_u32 m0, s71, 0\n"
    "ds_read_b128 a[188:191], v2 offset:22528\n"
    "v_mfma_f32_16x16x32_bf16 v[56:59], a[192:195], a[156:159], v[56:59]\n"
    "buffer_load_dwordx4 v1, s[40:43], s68 offen nt lds\n"
    "s_waitcnt lgkmcnt(1)\n"
    "v_mfma_f32_16x16x32_bf16 v[60:63], a[180:183], a[0:3], v[20:23]\n"
    "ds_read_b128 a[192:195], v3 offset:22528\n"
    "v_max3_f32 v10, v40, v41, v91\n"
    "v_mfma_f32_16x16x32_bf16 v[60:63], a[184:187], a[20:23], v[60:63]\n"
    "v_max3_f32 v10, v10, v42, v43\n"
    "v_max3_f32 v10, v10, v44, v45\n"
    "v_mfma_f32_16x16x32_bf16 v[64:67], a[180:183], a[4:7], v[24:27]\n"
    "v_max3_f32 v10, v10, v46, v47\n"
    "v_max3_f32 v10, v10, v48, v49\n"
    "v_mfma_f32_16x16x32_bf16 v[64:67], a[184:187], a[24:27], v[64:67]\n"
    "v_max3_f32 v10, v10, v50, v51\n"
    "v_max3_f32 v10, v10, v52, v53\n"
    "v_mfma_f32_16x16x32_bf16 v[68:71], a[180:183], a[8:11], v[28:31]\n"
    "v_max3_f32 v10, v10, v54, v55\n"
    "v_max3_f32 v10, v10, v56, v57\n"
    "v_mfma_f32_16x16x32_bf16 v[68:71], a[184:187], a[28:31], v[68:71]\n"
    "v_max3_f32 v10, v10, v58, v59\n"
    "v_cmp_lt_f32 vcc, s73, v10\n"
    "v_mfma_f32_16x16x32_bf16 v[72:75], a[180:183], a[12:15], v[32:35]\n"
    "v_mfma_f32_16x16x32_bf16 v[72:75], a[184:187], a[32:35], v[72:75]\n"
    "v_mfma_f32_16x16x32_bf16 v[76:79], a[180:183], a[16:19], v[36:39]\n"
    "ds_read_b128 a[180:183], v2 offset:26624\n"
    "v_mfma_f32_16x16x32_bf16 v[76:79], a[184:187], a[36:39], v[76:79]\n"
    "s_cbranch_vccnz R0_o_%=\n"
    "R0R_o_%=:\n"
    "ds_read_b128 a[184:187], v3 offset:26624\n"
    "s_waitcnt lgkmcnt(2)\n"
    "v_mfma_f32_16x16x32_bf16 v[60:63], a[188:191], a[40:43], v[60:63]\n"
    "v_exp_f32 v40, v40\n"
    "v_mfma_f32_16x16x32_bf16 v[60:63], a[192:195], a[60:63], v[60:63]\n"
    "v_exp_f32 v41, v41\n"
    "v_mfma_f32_16x16x32_bf16 v[64:67], a[188:191], a[44:47], v[64:67]\n"
    "v_exp_f32 v42, v42\n"
    "v_mfma_f32_16x16x32_bf16 v[64:67], a[192:195], a[64:67], v[64:67]\n"
    "v_exp_f32 v43, v43\n"
    "v_mfma_f32_16x16x32_bf16 v[68:71], a[188:191], a[48:51], v[68:71]\n"
    "buffer_load_dwordx4 v1, s[40:43], s68 offen offset:1024 nt lds\n"
    "v_mfma_f32_16x16x32_bf16 v[68:71], a[192:195], a[68:71], v[68:71]\n"
    "v_cvt_pk_bf16_f32 v40, v40, v41\n"
    "v_cvt_pk_bf16_f32 v41, v42, v43\n"
    "v_mfma_f32_16x16x32_bf16 v[72:75], a[188:191], a[52:55], v[72:75]\n"
    "v_exp_f32 v44, v44\n"
    "v_mfma_f32_16x16x32_bf16 v[72:75], a[192:195], a[72:75], v[72:75]\n"
    "v_exp_f32 v45, v45\n"
    "v_mfma_f32_16x16x32_bf16 v[76:79], a[188:191], a[56:59], v[76:79]\n"
    "ds_read_b128 a[188:191], v2 offset:30720\n"
    "v_mfma_f32_16x16x32_bf16 v[76:79], a[192:195], a[76:79], v[76:79]\n"
    "ds_read_b128 a[192:195], v3 offset:30720\n"
    "s_waitcnt lgkmcnt(2)\n"
    "v_mfma_f32_16x16x32_bf16 v[60:63], a[180:183], a[80:83], v[60:63]\n"
    "v_exp_f32 v46, v46\n"
    "v_mfma_f32_16x16x32_bf16 v[60:63], a[184:187], a[100:103], v[60:63]\n"
    "v_exp_f32 v47, v47\n"
    "v_mfma_f32_16x16x32_bf16 v[64:67], a[180:183], a[84:87], v[64:67]\n"
    "buffer_load_dwordx4 v1, s[40:43], s68 offen offset:2048 nt lds\n"
    "v_mfma_f32_16x16x32_bf16 v[64:67], a[184:187], a[104:107], v[64:67]\n"
    "v_cvt_pk_bf16_f32 v44, v44, v45\n"
    "v_cvt_pk_bf16_f32 v45, v46, v47\n"
    "v_mfma_f32_16x16x32_bf16 v[68:71], a[180:183], a[88:91], v[68:71]\n"
    "v_exp_f32 v48, v48\n"
    "v_mfma_f32_16x16x32_bf16 v[68:71], a[184:187], a[108:111], v[68:71]\n"
    "v_exp_f32 v49, v49\n"
    "v_mfma_f32_16x16x32_bf16 v[72:75], a[180:183], a[92:95], v[72:75]\n"
    "v_exp_f32 v50, v50\n"
    "v_mfma_f32_16x16x32_bf16 v[72:75], a[184:187], a[112:115], v[72:75]\n"
    "v_exp_f32 v51, v51\n"
    "v_mfma_f32_16x16x32_bf16 v[76:79], a[180:183], a[96:99], v[76:79]\n"
    "v_cvt_pk_bf16_f32 v48, v48, v49\n"
    "v_cvt_pk_bf16_f32 v49, v50, v51\n"
    "v_mfma_f32_16x16x32_bf16 v[76:79], a[184:187], a[116:119], v[76:79]\n"
    "buffer_load_dwordx4 v1, s[40:43], s68 offen offset:3072 nt lds\n"
    "s_waitcnt lgkmcnt(0)\n"
    "v_mfma_f32_16x16x32_bf16 v[60:63], a[188:191], a[120:123], v[60:63]\n"
    "s_add_u32 s68, s68, 32768\n"
    "v_mfma_f32_16x16x32_bf16 v[60:63], a[192:195], a[140:143], v[60:63]\n"
    "s_waitcnt vmcnt(16)\n"
    "s_barrier\n"
    "v_mfma_f32_16x16x32_bf16 v[64:67], a[188:191], a[124:127], v[64:67]\n"
    "v_exp_f32 v52, v52\n"
    "v_mfma_f32_16x16x32_bf16 v[64:67], a[192:195], a[144:147], v[64:67]\n"
    "v_exp_f32 v53, v53\n"
    "v_mfma_f32_16x16x32_bf16 v[68:71], a[188:191], a[128:131], v[68:71]\n"
    "v_exp_f32 v54, v54\n"
    "v_mfma_f32_16x16x32_bf16 v[68:71], a[192:195], a[148:151], v[68:71]\n"
    "v_exp_f32 v55, v55\n"
    "v_mfma_f32_16x16x32_bf16 v[72:75], a[188:191], a[132:135], v[72:75]\n"
    "v_cvt_pk_bf16_f32 v52, v52, v53\n"
    "v_cvt_pk_bf16_f32 v53, v54, v55\n"
    "v_mfma_f32_16x16x32_bf16 v[72:75], a[192:195], a[152:155], v[72:75]\n"
    "s_add_u32 m0, s70, 16384\n"
    "v_mfma_f32_16x16x32_bf16 v[76:79], a[188:191], a[136:139], v[76:79]\n"
    "buffer_load_dwordx4 v1, s[36:39], s66 offen nt lds\n"
    "v_mfma_f32_16x16x32_bf16 v[76:79], a[192:195], a[156:159], v[76:79]\n"
    "v_exp_f32 v56, v56\n"
    "v_exp_f32 v57, v57\n"
    "v_exp_f32 v58, v58\n"
    "v_exp_f32 v59, v59\n"
    "v_mfma_f32_16x16x32_bf16 v[96:99], a[196:199], v[80:83], v[96:99]\n"
    "v_cvt_pk_bf16_f32 v56, v56, v57\n"
    "v_cvt_pk_bf16_f32 v57, v58, v59\n"
    "v_mfma_f32_16x16x32_bf16 v[100:103], a[196:199], a[228:231], v[100:103]\n"
    "v_max3_f32 v10, v60, v61, v61\n"
    "v_max3_f32 v10, v10, v62, v63\n"
    "v_mfma_f32_16x16x32_bf16 v[104:107], a[196:199], a[232:235], v[104:107]\n"
    "v_max3_f32 v10, v10, v64, v65\n"
    "v_max3_f32 v10, v10, v66, v67\n"
    "v_mfma_f32_16x16x32_bf16 v[108:111], a[196:199], a[236:239], v[108:111]\n"
    "v_max3_f32 v10, v10, v68, v69\n"
    "v_max3_f32 v10, v10, v70, v71\n"
    "v_mfma_f32_16x16x32_bf16 v[112:115], a[196:199], a[240:243], v[112:115]\n"
    "v_max3_f32 v10, v10, v72, v73\n"
    "v_max3_f32 v10, v10, v74, v75\n"
    "v_mfma_f32_16x16x32_bf16 v[116:119], a[200:203], v[80:83], v[116:119]\n"
    "v_max3_f32 v10, v10, v76, v77\n"
    "v_max3_f32 v10, v10, v78, v79\n"
    "v_mfma_f32_16x16x32_bf16 v[120:123], a[200:203], a[228:231], v[120:123]\n"
    "v_cmp_lt_f32 vcc, s85, v10\n"
    "v_mfma_f32_16x16x32_bf16 v[124:127], a[200:203], a[232:235], v[124:127]\n"
    "s_cbranch_vccnz R1_o_%=\n"
    "R1R_o_%=:\n"
    "v_mfma_f32_16x16x32_bf16 v[128:131], a[200:203], a[236:239], v[128:131]\n"
    "v_exp_f32 v60, v60\n"
    "v_mfma_f32_16x16x32_bf16 v[132:135], a[200:203], a[240:243], v[132:135]\n"
    "v_exp_f32 v61, v61\n"
    "v_mfma_f32_16x16x32_bf16 v[136:139], a[204:207], v[80:83], v[136:139]\n"
    "v_exp_f32 v62, v62\n"
    "v_mfma_f32_16x16x32_bf16 v[140:143], a[204:207], a[228:231], v[140:143]\n"
    "v_exp_f32 v63, v63\n"
    "v_mfma_f32_16x16x32_bf16 v[144:147], a[204:207], a[232:235], v[144:147]\n"
    "v_cvt_pk_bf16_f32 v42, v60, v61\n"
    "v_cvt_pk_bf16_f32 v43, v62, v63\n"
    "v_mfma_f32_16x16x32_bf16 v[148:151], a[204:207], a[236:239], v[148:151]\n"
    "v_exp_f32 v64, v64\n"
    "v_mfma_f32_16x16x32_bf16 v[152:155], a[204:207], a[240:243], v[152:155]\n"
    "v_exp_f32 v65, v65\n"
    "v_mfma_f32_16x16x32_bf16 v[156:159], a[208:211], v[80:83], v[156:159]\n"
    "v_exp_f32 v66, v66\n"
    "v_mfma_f32_16x16x32_bf16 v[160:163], a[208:211], a[228:231], v[160:163]\n"
    "v_exp_f32 v67, v67\n"
    "v_mfma_f32_16x16x32_bf16 v[164:167], a[208:211], a[232:235], v[164:167]\n"
    "v_cvt_pk_bf16_f32 v46, v64, v65\n"
    "v_cvt_pk_bf16_f32 v47, v66, v67\n"
    "v_mfma_f32_16x16x32_bf16 v[168:171], a[208:211], a[236:239], v[168:171]\n"
    "v_exp_f32 v68, v68\n"
    "v_mfma_f32_16x16x32_bf16 v[172:175], a[208:211], a[240:243], v[172:175]\n"
    "v_exp_f32 v69, v69\n"
    "v_mfma_f32_16x16x32_bf16 v[176:179], a[212:215], v[80:83], v[176:179]\n"
    "buffer_load_dwordx4 v1, s[36:39], s66 offen offset:1024 nt lds\n"
    "v_mfma_f32_16x16x32_bf16 v[180:183], a[212:215], a[228:231], v[180:183]\n"
    "ds_read_b64_tr_b16 a[196:197], v4 offset:8192\n"
    "ds_read_b64_tr_b16 a[198:199], v4 offset:10240\n"
    "v_mfma_f32_16x16x32_bf16 v[184:187], a[212:215], a[232:235], v[184:187]\n"
    "v_exp_f32 v70, v70\n"
    "v_mfma_f32_16x16x32_bf16 v[188:191], a[212:215], a[236:239], v[188:191]\n"
    "v_exp_f32 v71, v71\n"
    "v_mfma_f32_16x16x32_bf16 v[192:195], a[212:215], a[240:243], v[192:195]\n"
    "v_cvt_pk_bf16_f32 v50, v68, v69\n"
    "v_cvt_pk_bf16_f32 v51, v70, v71\n"
    "v_mfma_f32_16x16x32_bf16 v[196:199], a[216:219], v[80:83], v[196:199]\n"
    "ds_read_b64_tr_b16 a[200:201], v5 offset:8192\n"
    "ds_read_b64_tr_b16 a[202:203], v5 offset:10240\n"
    "v_mfma_f32_16x16x32_bf16 v[200:203], a[216:219], a[228:231], v[200:203]\n"
    "v_exp_f32 v72, v72\n"
    "v_mfma_f32_16x16x32_bf16 v[204:207], a[216:219], a[232:235], v[204:207]\n"
    "v_exp_f32 v73, v73\n"
    "v_mfma_f32_16x16x32_bf16 v[208:211], a[216:219], a[236:239], v[208:211]\n"
    "v_exp_f32 v74, v74\n"
    "v_mfma_f32_16x16x32_bf16 v[212:215], a[216:219], a[240:243], v[212:215]\n"
    "v_exp_f32 v75, v75\n"
    "v_mfma_f32_16x16x32_bf16 v[216:219], a[220:223], v[80:83], v[216:219]\n"
    "v_cvt_pk_bf16_f32 v54, v72, v73\n"
    "v_cvt_pk_bf16_f32 v55, v74, v75\n"
    "v_mfma_f32_16x16x32_bf16 v[220:223], a[220:223], a[228:231], v[220:223]\n"
    "v_exp_f32 v76, v76\n"
    "v_mfma_f32_16x16x32_bf16 v[224:227], a[220:223], a[232:235], v[224:227]\n"
    "v_exp_f32 v77, v77\n"
    "v_mfma_f32_16x16x32_bf16 v[228:231], a[220:223], a[236:239], v[228:231]\n"
    "v_exp_f32 v78, v78\n"
    "v_mfma_f32_16x16x32_bf16 v[232:235], a[220:223], a[240:243], v[232:235]\n"
    "v_exp_f32 v79, v79\n"
    "v_mfma_f32_16x16x32_bf16 v[236:239], a[224:227], v[80:83], v[236:239]\n"
    "v_cvt_pk_bf16_f32 v58, v76, v77\n"
    "v_cvt_pk_bf16_f32 v59, v78, v79\n"
    "v_mfma_f32_16x16x32_bf16 v[240:243], a[224:227], a[228:231], v[240:243]\n"
    "v_mfma_f32_16x16x32_bf16 v[244:247], a[224:227], a[232:235], v[244:247]\n"
    "v_mfma_f32_16x16x32_bf16 v[248:251], a[224:227], a[236:239], v[248:251]\n"
    "buffer_load_dwordx4 v1, s[36:39], s66 offen offset:2048 nt lds\n"
    "v_mfma_f32_16x16x32_bf16 v[252:255], a[224:227], a[240:243], v[252:255]\n"
    "ds_read_b64_tr_b16 a[204:205], v6 offset:8192\n"
    "ds_read_b64_tr_b16 a[206:207], v6 offset:10240\n"
    "s_waitcnt lgkmcnt(2)\n"
    "v_mfma_f32_16x16x32_bf16 v[96:99], a[196:199], v[40:43], v[96:99]\n"
    "ds_read_b64_tr_b16 a[208:209], v7 offset:8192\n"
    "ds_read_b64_tr_b16 a[210:211], v7 offset:10240\n"
    "v_mfma_f32_16x16x32_bf16 v[100:103], a[196:199], v[44:47], v[100:103]\n"
    "ds_write_b128 v8, v[40:43] offset:0\n"
    "v_mfma_f32_16x16x32_bf16 v[104:107], a[196:199], v[48:51], v[104:107]\n"
    "ds_read_b64_tr_b16 a[212:213], v4 offset:12288\n"
    "ds_read_b64_tr_b16 a[214:215], v4 offset:14336\n"
    "v_mfma_f32_16x16x32_bf16 v[108:111], a[196:199], v[52:55], v[108:111]\n"
    "ds_read_b64_tr_b16 a[216:217], v5 offset:12288\n"
    "ds_read_b64_tr_b16 a[218:219], v5 offset:14336\n"
    "v_mfma_f32_16x16x32_bf16 v[112:115], a[196:199], v[56:59], v[112:115]\n"
    "ds_read_b64_tr_b16 a[220:221], v6 offset:12288\n"
    "ds_read_b64_tr_b16 a[222:223], v6 offset:14336\n"
    "v_mfma_f32_16x16x32_bf16 v[116:119], a[200:203], v[40:43], v[116:119]\n"
    "ds_read_b64_tr_b16 a[224:225], v7 offset:12288\n"
    "ds_read_b64_tr_b16 a[226:227], v7 offset:14336\n"
    "v_mfma_f32_16x16x32_bf16 v[120:123], a[200:203], v[44:47], v[120:123]\n"
    "ds_write_b128 v8, v[44:47] offset:1024\n"
    "v_mfma_f32_16x16x32_bf16 v[124:127], a[200:203], v[48:51], v[124:127]\n"
    "v_mfma_f32_16x16x32_bf16 v[128:131], a[200:203], v[52:55], v[128:131]\n"
    "buffer_load_dwordx4 v1, s[36:39], s66 offen offset:3072 nt lds\n"
    "v_mfma_f32_16x16x32_bf16 v[132:135], a[200:203], v[56:59], v[132:135]\n"
    "s_add_u32 s66, s66, 32768\n"
    "s_waitcnt lgkmcnt(10)\n"
    "v_mfma_f32_16x16x32_bf16 v[136:139], a[204:207], v[40:43], v[136:139]\n"
    "v_mfma_f32_16x16x32_bf16 v[140:143], a[204:207], v[44:47], v[140:143]\n"
    "ds_write_b128 v8, v[48:51] offset:2048\n"
    "v_mfma_f32_16x16x32_bf16 v[144:147], a[204:207], v[48:51], v[144:147]\n"
    "v_mfma_f32_16x16x32_bf16 v[148:151], a[204:207], v[52:55], v[148:151]\n"
    "v_mfma_f32_16x16x32_bf16 v[152:155], a[204:207], v[56:59], v[152:155]\n"
    "v_mfma_f32_16x16x32_bf16 v[156:159], a[208:211], v[40:43], v[156:159]\n"
    "v_mfma_f32_16x16x32_bf16 v[160:163], a[208:211], v[44:47], v[160:163]\n"
    "ds_write_b128 v8, v[52:55] offset:3072\n"
    "v_mfma_f32_16x16x32_bf16 v[164:167], a[208:211], v[48:51], v[164:167]\n"
    "s_add_u32 m0, s72, 0\n"
    "v_mfma_f32_16x16x32_bf16 v[168:171], a[208:211], v[52:55], v[168:171]\n"
    "buffer_load_dwordx4 v1, s[40:43], s69 offen nt lds\n"
    "v_mfma_f32_16x16x32_bf16 v[172:175], a[208:211], v[56:59], v[172:175]\n"
    "s_waitcnt lgkmcnt(2)\n"
    "v_mfma_f32_16x16x32_bf16 v[176:179], a[212:215], v[40:43], v[176:179]\n"
    "ds_write_b128 v8, v[56:59] offset:4096\n"
    "v_mfma_f32_16x16x32_bf16 v[180:183], a[212:215], v[44:47], v[180:183]\n"
    "v_mfma_f32_16x16x32_bf16 v[184:187], a[212:215], v[48:51], v[184:187]\n"
    "v_mfma_f32_16x16x32_bf16 v[188:191], a[212:215], v[52:55], v[188:191]\n"
    "v_mfma_f32_16x16x32_bf16 v[192:195], a[212:215], v[56:59], v[192:195]\n"
    "v_mfma_f32_16x16x32_bf16 v[196:199], a[216:219], v[40:43], v[196:199]\n"
    "v_mfma_f32_16x16x32_bf16 v[200:203], a[216:219], v[44:47], v[200:203]\n"
    "s_waitcnt vmcnt(9) lgkmcnt(0)\n"
    "s_barrier\n"
    "v_mfma_f32_16x16x32_bf16 v[204:207], a[216:219], v[48:51], v[204:207]\n"
    "v_mfma_f32_16x16x32_bf16 v[208:211], a[216:219], v[52:55], v[208:211]\n"
    "buffer_load_dwordx4 v1, s[40:43], s69 offen offset:1024 nt lds\n"
    "v_mfma_f32_16x16x32_bf16 v[212:215], a[216:219], v[56:59], v[212:215]\n"
    "ds_read_b128 a[180:183], v2 offset:0\n"
    "v_mfma_f32_16x16x32_bf16 v[216:219], a[220:223], v[40:43], v[216:219]\n"
    "v_mfma_f32_16x16x32_bf16 v[220:223], a[220:223], v[44:47], v[220:223]\n"
    "v_mfma_f32_16x16x32_bf16 v[224:227], a[220:223], v[48:51], v[224:227]\n"
    "v_mfma_f32_16x16x32_bf16 v[228:231], a[220:223], v[52:55], v[228:231]\n"
    "ds_read_b128 a[184:187], v3 offset:0\n"
    "v_mfma_f32_16x16x32_bf16 v[232:235], a[220:223], v[56:59], v[232:235]\n"
    "v_mfma_f32_16x16x32_bf16 v[236:239], a[224:227], v[40:43], v[236:239]\n"
    "buffer_load_dwordx4 v1, s[40:43], s69 offen offset:2048 nt lds\n"
    "v_mfma_f32_16x16x32_bf16 v[240:243], a[224:227], v[44:47], v[240:243]\n"
    "v_mfma_f32_16x16x32_bf16 v[244:247], a[224:227], v[48:51], v[244:247]\n"
    "ds_read_b128 a[188:191], v2 offset:4096\n"
    "v_mfma_f32_16x16x32_bf16 v[248:251], a[224:227], v[52:55], v[248:251]\n"
    "v_mfma_f32_16x16x32_bf16 v[252:255], a[224:227], v[56:59], v[252:255]\n"
    "v_mfma_f32_16x16x32_bf16 a[160:163], a[244:247], v[40:43], a[160:163]\n"
    "v_mfma_f32_16x16x32_bf16 a[164:167], a[244:247], v[44:47], a[164:167]\n"
    "ds_read_b128 a[192:195], v3 offset:4096\n"
    "v_mfma_f32_16x16x32_bf16 a[168:171], a[244:247], v[48:51], a[168:171]\n"
    "buffer_load_dwordx4 v1, s[40:43], s69 offen offset:3072 nt lds\n"
    "v_mfma_f32_16x16x32_bf16 a[172:175], a[244:247], v[52:55], a[172:175]\n"
    "s_add_u32 s69, s69, 32768\n"
    "v_mfma_f32_16x16x32_bf16 a[176:179], a[244:247], v[56:59], a[176:179]\n"
    "s_cmp_eq_u32 s75, 0\n"
    "s_cbranch_scc1 P14_%=\n"
    "ds_read_b128 v[80:83], v9 offset:0\n"
    "s_waitcnt lgkmcnt(3)\n"
    "v_mfma_f32_16x16x32_bf16 v[40:43], a[180:183], a[0:3], v[20:23]\n"
    "ds_read_b128 a[228:231], v9 offset:1024\n"
    "v_mfma_f32_16x16x32_bf16 v[40:43], a[184:187], a[20:23], v[40:43]\n"
    "ds_read_b128 a[232:235], v9 offset:2048\n"
    "v_mfma_f32_16x16x32_bf16 v[44:47], a[180:183], a[4:7], v[24:27]\n"
    "ds_read_b128 a[236:239], v9 offset:3072\n"
    "v_mfma_f32_16x16x32_bf16 v[44:47], a[184:187], a[24:27], v[44:47]\n"
    "ds_read_b128 a[240:243], v9 offset:4096\n"
    "ds_read_b32 v91, v13 offset:1280\n"
    "v_mfma_f32_16x16x32_bf16 v[48:51], a[180:183], a[8:11], v[28:31]\n"
    "ds_read_b64_tr_b16 a[196:197], v4 offset:40960\n"
    "ds_read_b64_tr_b16 a[198:199], v4 offset:43008\n"
    "v_mfma_f32_16x16x32_bf16 v[48:51], a[184:187], a[28:31], v[48:51]\n"
    "v_mfma_f32_16x16x32_bf16 v[52:55], a[180:183], a[12:15], v[32:35]\n"
    "v_mfma_f32_16x16x32_bf16 v[52:55], a[184:187], a[32:35], v[52:55]\n"
    "s_add_u32 m0, s70, 20480\n"
    "ds_read_b64_tr_b16 a[200:201], v5 offset:40960\n"
    "v_mfma_f32_16x16x32_bf16 v[56:59], a[180:183], a[16:19], v[36:39]\n"
    "buffer_load_dwordx4 v1, s[36:39], s67 offen nt lds\n"
    "v_mfma_f32_16x16x32_bf16 v[56:59], a[184:187], a[36:39], v[56:59]\n"
    "ds_read_b128 a[180:183], v2 offset:8192\n"
    "ds_read_b128 a[184:187], v3 offset:8192\n"
    "s_waitcnt lgkmcnt(9)\n"
    "v_mfma_f32_16x16x32_bf16 v[40:43], a[188:191], a[40:43], v[40:43]\n"
    "ds_read_b64_tr_b16 a[202:203], v5 offset:43008\n"
    "ds_read_b64_tr_b16 a[204:205], v6 offset:40960\n"
    "v_mfma_f32_16x16x32_bf16 v[40:43], a[192:195], a[60:63], v[40:43]\n"
    "ds_read_b64_tr_b16 a[206:207], v6 offset:43008\n"
    "v_mfma_f32_16x16x32_bf16 v[44:47], a[188:191], a[44:47], v[44:47]\n"
    "v_mfma_f32_16x16x32_bf16 v[44:47], a[192:195], a[64:67], v[44:47]\n"
    "ds_read_b64_tr_b16 a[208:209], v7 offset:40960\n"
    "ds_read_b64_tr_b16 a[210:211], v7 offset:43008\n"
    "v_mfma_f32_16x16x32_bf16 v[48:51], a[188:191], a[48:51], v[48:51]\n"
    "v_mfma_f32_16x16x32_bf16 v[48:51], a[192:195], a[68:71], v[48:51]\n"
    "v_mfma_f32_16x16x32_bf16 v[52:55], a[188:191], a[52:55], v[52:55]\n"
    "buffer_load_dwordx4 v1, s[36:39], s67 offen offset:1024 nt lds\n"
    "v_mfma_f32_16x16x32_bf16 v[52:55], a[192:195], a[72:75], v[52:55]\n"
    "ds_read_b64_tr_b16 a[212:213], v4 offset:45056\n"
    "ds_read_b64_tr_b16 a[214:215], v4 offset:47104\n"
    "v_mfma_f32_16x16x32_bf16 v[56:59], a[188:191], a[56:59], v[56:59]\n"
    "ds_read_b128 a[188:191], v2 offset:12288\n"
    "v_mfma_f32_16x16x32_bf16 v[56:59], a[192:195], a[76:79], v[56:59]\n"
    "ds_read_b128 a[192:195], v3 offset:12288\n"
    "ds_read_b64_tr_b16 a[216:217], v5 offset:45056\n"
    "s_waitcnt lgkmcnt(8)\n"
    "v_mfma_f32_16x16x32_bf16 v[40:43], a[180:183], a[80:83], v[40:43]\n"
    "ds_read_b64_tr_b16 a[218:219], v5 offset:47104\n"
    "v_mfma_f32_16x16x32_bf16 v[40:43], a[184:187], a[100:103], v[40:43]\n"
    "v_mfma_f32_16x16x32_bf16 v[44:47], a[180:183], a[84:87], v[44:47]\n"
    "ds_read_b64_tr_b16 a[220:221], v6 offset:45056\n"
    "ds_read_b64_tr_b16 a[222:223], v6 offset:47104\n"
    "v_mfma_f32_16x16x32_bf16 v[44:47], a[184:187], a[104:107], v[44:47]\n"
    "v_mfma_f32_16x16x32_bf16 v[48:51], a[180:183], a[88:91], v[48:51]\n"
    "buffer_load_dwordx4 v1, s[36:39], s67 offen offset:2048 nt lds\n"
    "v_mfma_f32_16x16x32_bf16 v[48:51], a[184:187], a[108:111], v[48:51]\n"
    "ds_read_b64_tr_b16 a[224:225], v7 offset:45056\n"
    "ds_read_b64_tr_b16 a[226:227], v7 offset:47104\n"
    "v_mfma_f32_16x16x32_bf16 v[52:55], a[180:183], a[92:95], v[52:55]\n"
    "v_mfma_f32_16x16x32_bf16 v[52:55], a[184:187], a[112:115], v[52:55]\n"
    "v_mfma_f32_16x16x32_bf16 v[56:59], a[180:183], a[96:99], v[56:59]\n"
    "ds_read_b128 a[180:183], v2 offset:2048\n"
    "v_mfma_f32_16x16x32_bf16 v[56:59], a[184:187], a[116:119], v[56:59]\n"
    "ds_read_b128 a[184:187], v3 offset:2048\n"
    "s_waitcnt lgkmcnt(6)\n"
    "v_mfma_f32_16x16x32_bf16 v[40:43], a[188:191], a[120:123], v[40:43]\n"
    "v_mfma_f32_16x16x32_bf16 v[40:43], a[192:195], a[140:143], v[40:43]\n"
    "v_mfma_f32_16x16x32_bf16 v[44:47], a[188:191], a[124:127], v[44:47]\n"
    "buffer_load_dwordx4 v1, s[36:39], s67 offen offset:3072 nt lds\n"
    "v_mfma_f32_16x16x32_bf16 v[44:47], a[192:195], a[144:147], v[44:47]\n"
    "s_add_u32 s67, s67, 32768\n"
    "v_mfma_f32_16x16x32_bf16 v[48:51], a[188:191], a[128:131], v[48:51]\n"
    "v_mfma_f32_16x16x32_bf16 v[48:51], a[192:195], a[148:151], v[48:51]\n"
    "v_mfma_f32_16x16x32_bf16 v[52:55], a[188:191], a[132:135], v[52:55]\n"
    "v_mfma_f32_16x16x32_bf16 v[52:55], a[192:195], a[152:155], v[52:55]\n"
    "v_mfma_f32_16x16x32_bf16 v[56:59], a[188:191], a[136:139], v[56:59]\n"
    "s_add_u32 m0, s71, 8192\n"
    "ds_read_b128 a[188:191], v2 offset:6144\n"
    "v_mfma_f32_16x16x32_bf16 v[56:59], a[192:195], a[156:159], v[56:59]\n"
    "buffer_load_dwordx4 v1, s[40:43], s68 offen nt lds\n"
    "s_waitcnt lgkmcnt(1)\n"
    "v_mfma_f32_16x16x32_bf16 v[60:63], a[180:183], a[0:3], v[20:23]\n"
    "ds_read_b128 a[192:195], v3 offset:6144\n"
    "v_max3_f32 v10, v40, v41, v91\n"
    "v_mfma_f32_16x16x32_bf16 v[60:63], a[184:187], a[20:23], v[60:63]\n"
    "v_max3_f32 v10, v10, v42, v43\n"
    "v_max3_f32 v10, v10, v44, v45\n"
    "v_mfma_f32_16x16x32_bf16 v[64:67], a[180:183], a[4:7], v[24:27]\n"
    "v_max3_f32 v10, v10, v46, v47\n"
    "v_max3_f32 v10, v10, v48, v49\n"
    "v_mfma_f32_16x16x32_bf16 v[64:67], a[184:187], a[24:27], v[64:67]\n"
    "v_max3_f32 v10, v10, v50, v51\n"
    "v_max3_f32 v10, v10, v52, v53\n"
    "v_mfma_f32_16x16x32_bf16 v[68:71], a[180:183], a[8:11], v[28:31]\n"
    "v_max3_f32 v10, v10, v54, v55\n"
    "v_max3_f32 v10, v10, v56, v57\n"
    "v_mfma_f32_16x16x32_bf16 v[68:71], a[184:187], a[28:31], v[68:71]\n"
    "v_max3_f32 v10, v10, v58, v59\n"
    "v_cmp_lt_f32 vcc, s73, v10\n"
    "v_mfma_f32_16x16x32_bf16 v[72:75], a[180:183], a[12:15], v[32:35]\n"
    "v_mfma_f32_16x16x32_bf16 v[72:75], a[184:187], a[32:35], v[72:75]\n"
    "v_mfma_f32_16x16x32_bf16 v[76:79], a[180:183], a[16:19], v[36:39]\n"
    "ds_read_b128 a[180:183], v2 offset:10240\n"
    "v_mfma_f32_16x16x32_bf16 v[76:79], a[184:187], a[36:39], v[76:79]\n"
    "s_cbranch_vccnz R0_e_%=\n"
    "R0R_e_%=:\n"
    "ds_read_b128 a[184:187], v3 offset:10240\n"
    "s_waitcnt lgkmcnt(2)\n"
    "v_mfma_f32_16x16x32_bf16 v[60:63], a[188:191], a[40:43], v[60:63]\n"
    "v_exp_f32 v40, v40\n"
    "v_mfma_f32_16x16x32_bf16 v[60:63], a[192:195], a[60:63], v[60:63]\n"
    "v_exp_f32 v41, v41\n"
    "v_mfma_f32_16x16x32_bf16 v[64:67], a[188:191], a[44:47], v[64:67]\n"
    "v_exp_f32 v42, v42\n"
    "v_mfma_f32_16x16x32_bf16 v[64:67], a[192:195], a[64:67], v[64:67]\n"
    "v_exp_f32 v43, v43\n"
    "v_mfma_f32_16x16x32_bf16 v[68:71], a[188:191], a[48:51], v[68:71]\n"
    "buffer_load_dwordx4 v1, s[40:43], s68 offen offset:1024 nt lds\n"
    "v_mfma_f32_16x16x32_bf16 v[68:71], a[192:195], a[68:71], v[68:71]\n"
    "v_cvt_pk_bf16_f32 v40, v40, v41\n"
    "v_cvt_pk_bf16_f32 v41, v42, v43\n"
    "v_mfma_f32_16x16x32_bf16 v[72:75], a[188:191], a[52:55], v[72:75]\n"
    "v_exp_f32 v44, v44\n"
    "v_mfma_f32_16x16x32_bf16 v[72:75], a[192:195], a[72:75], v[72:75]\n"
    "v_exp_f32 v45, v45\n"
    "v_mfma_f32_16x16x32_bf16 v[76:79], a[188:191], a[56:59], v[76:79]\n"
    "ds_read_b128 a[188:191], v2 offset:14336\n"
    "v_mfma_f32_16x16x32_bf16 v[76:79], a[192:195], a[76:79], v[76:79]\n"
    "ds_read_b128 a[192:195], v3 offset:14336\n"
    "s_waitcnt lgkmcnt(2)\n"
    "v_mfma_f32_16x16x32_bf16 v[60:63], a[180:183], a[80:83], v[60:63]\n"
    "v_exp_f32 v46, v46\n"
    "v_mfma_f32_16x16x32_bf16 v[60:63], a[184:187], a[100:103], v[60:63]\n"
    "v_exp_f32 v47, v47\n"
    "v_mfma_f32_16x16x32_bf16 v[64:67], a[180:183], a[84:87], v[64:67]\n"
    "buffer_load_dwordx4 v1, s[40:43], s68 offen offset:2048 nt lds\n"
    "v_mfma_f32_16x16x32_bf16 v[64:67], a[184:187], a[104:107], v[64:67]\n"
    "v_cvt_pk_bf16_f32 v44, v44, v45\n"
    "v_cvt_pk_bf16_f32 v45, v46, v47\n"
    "v_mfma_f32_16x16x32_bf16 v[68:71], a[180:183], a[88:91], v[68:71]\n"
    "v_exp_f32 v48, v48\n"
    "v_mfma_f32_16x16x32_bf16 v[68:71], a[184:187], a[108:111], v[68:71]\n"
    "v_exp_f32 v49, v49\n"
    "v_mfma_f32_16x16x32_bf16 v[72:75], a[180:183], a[92:95], v[72:75]\n"
    "v_exp_f32 v50, v50\n"
    "v_mfma_f32_16x16x32_bf16 v[72:75], a[184:187], a[112:115], v[72:75]\n"
    "v_exp_f32 v51, v51\n"
    "v_mfma_f32_16x16x32_bf16 v[76:79], a[180:183], a[96:99], v[76:79]\n"
    "v_cvt_pk_bf16_f32 v48, v48, v49\n"
    "v_cvt_pk_bf16_f32 v49, v50, v51\n"
    "v_mfma_f32_16x16x32_bf16 v[76:79], a[184:187], a[116:119], v[76:79]\n"
    "buffer_load_dwordx4 v1, s[40:43], s68 offen offset:3072 nt lds\n"
    "s_waitcnt lgkmcnt(0)\n"
    "v_mfma_f32_16x16x32_bf16 v[60:63], a[188:191], a[120:123], v[60:63]\n"
    "s_add_u32 s68, s68, 32768\n"
    "v_mfma_f32_16x16x32_bf16 v[60:63], a[192:195], a[140:143], v[60:63]\n"
    "s_waitcnt vmcnt(16)\n"
    "s_barrier\n"
    "v_mfma_f32_16x16x32_bf16 v[64:67], a[188:191], a[124:127], v[64:67]\n"
    "v_exp_f32 v52, v52\n"
    "v_mfma_f32_16x16x32_bf16 v[64:67], a[192:195], a[144:147], v[64:67]\n"
    "v_exp_f32 v53, v53\n"
    "v_mfma_f32_16x16x32_bf16 v[68:71], a[188:191], a[128:131], v[68:71]\n"
    "v_exp_f32 v54, v54\n"
    "v_mfma_f32_16x16x32_bf16 v[68:71], a[192:195], a[148:151], v[68:71]\n"
    "v_exp_f32 v55, v55\n"
    "v_mfma_f32_16x16x32_bf16 v[72:75], a[188:191], a[132:135], v[72:75]\n"
    "v_cvt_pk_bf16_f32 v52, v52, v53\n"
    "v_cvt_pk_bf16_f32 v53, v54, v55\n"
    "v_mfma_f32_16x16x32_bf16 v[72:75], a[192:195], a[152:155], v[72:75]\n"
    "s_add_u32 m0, s70, 0\n"
    "v_mfma_f32_16x16x32_bf16 v[76:79], a[188:191], a[136:139], v[76:79]\n"
    "buffer_load_dwordx4 v1, s[36:39], s66 offen nt lds\n"
    "v_mfma_f32_16x16x32_bf16 v[76:79], a[192:195], a[156:159], v[76:79]\n"
    "v_exp_f32 v56, v56\n"
    "v_exp_f32 v57, v57\n"
    "v_exp_f32 v58, v58\n"
    "v_exp_f32 v59, v59\n"
    "v_mfma_f32_16x16x32_bf16 v[96:99], a[196:199], v[80:83], v[96:99]\n"
    "v_cvt_pk_bf16_f32 v56, v56, v57\n"
    "v_cvt_pk_bf16_f32 v57, v58, v59\n"
    "v_mfma_f32_16x16x32_bf16 v[100:103], a[196:199], a[228:231], v[100:103]\n"
    "v_max3_f32 v10, v60, v61, v61\n"
    "v_max3_f32 v10, v10, v62, v63\n"
    "v_mfma_f32_16x16x32_bf16 v[104:107], a[196:199], a[232:235], v[104:107]\n"
    "v_max3_f32 v10, v10, v64, v65\n"
    "v_max3_f32 v10, v10, v66, v67\n"
    "v_mfma_f32_16x16x32_bf16 v[108:111], a[196:199], a[236:239], v[108:111]\n"
    "v_max3_f32 v10, v10, v68, v69\n"
    "v_max3_f32 v10, v10, v70, v71\n"
    "v_mfma_f32_16x16x32_bf16 v[112:115], a[196:199], a[240:243], v[112:115]\n"
    "v_max3_f32 v10, v10, v72, v73\n"
    "v_max3_f32 v10, v10, v74, v75\n"
    "v_mfma_f32_16x16x32_bf16 v[116:119], a[200:203], v[80:83], v[116:119]\n"
    "v_max3_f32 v10, v10, v76, v77\n"
    "v_max3_f32 v10, v10, v78, v79\n"
    "v_mfma_f32_16x16x32_bf16 v[120:123], a[200:203], a[228:231], v[120:123]\n"
    "v_cmp_lt_f32 vcc, s85, v10\n"
    "v_mfma_f32_16x16x32_bf16 v[124:127], a[200:203], a[232:235], v[124:127]\n"
    "s_cbranch_vccnz R1_e_%=\n"
    "R1R_e_%=:\n"
    "v_mfma_f32_16x16x32_bf16 v[128:131], a[200:203], a[236:239], v[128:131]\n"
    "v_exp_f32 v60, v60\n"
    "v_mfma_f32_16x16x32_bf16 v[132:135], a[200:203], a[240:243], v[132:135]\n"
    "v_exp_f32 v61, v61\n"
    "v_mfma_f32_16x16x32_bf16 v[136:139], a[204:207], v[80:83], v[136:139]\n"
    "v_exp_f32 v62, v62\n"
    "v_mfma_f32_16x16x32_bf16 v[140:143], a[204:207], a[228:231], v[140:143]\n"
    "v_exp_f32 v63, v63\n"
    "v_mfma_f32_16x16x32_bf16 v[144:147], a[204:207], a[232:235], v[144:147]\n"
    "v_cvt_pk_bf16_f32 v42, v60, v61\n"
    "v_cvt_pk_bf16_f32 v43, v62, v63\n"
    "v_mfma_f32_16x16x32_bf16 v[148:151], a[204:207], a[236:239], v[148:151]\n"
    "v_exp_f32 v64, v64\n"
    "v_mfma_f32_16x16x32_bf16 v[152:155], a[204:207], a[240:243], v[152:155]\n"
    "v_exp_f32 v65, v65\n"
    "v_mfma_f32_16x16x32_bf16 v[156:159], a[208:211], v[80:83], v[156:159]\n"
    "v_exp_f32 v66, v66\n"
    "v_mfma_f32_16x16x32_bf16 v[160:163], a[208:211], a[228:231], v[160:163]\n"
    "v_exp_f32 v67, v67\n"
    "v_mfma_f32_16x16x32_bf16 v[164:167], a[208:211], a[232:235], v[164:167]\n"
    "v_cvt_pk_bf16_f32 v46, v64, v65\n"
    "v_cvt_pk_bf16_f32 v47, v66, v67\n"
    "v_mfma_f32_16x16x32_bf16 v[168:171], a[208:211], a[236:239], v[168:171]\n"
    "v_exp_f32 v68, v68\n"
    "v_mfma_f32_16x16x32_bf16 v[172:175], a[208:211], a[240:243], v[172:175]\n"
    "v_exp_f32 v69, v69\n"
    "v_mfma_f32_16x16x32_bf16 v[176:179], a[212:215], v[80:83], v[176:179]\n"
    "buffer_load_dwordx4 v1, s[36:39], s66 offen offset:1024 nt lds\n"
    "v_mfma_f32_16x16x32_bf16 v[180:183], a[212:215], a[228:231], v[180:183]\n"
    "ds_read_b64_tr_b16 a[196:197], v4 offset:0\n"
    "ds_read_b64_tr_b16 a[198:199], v4 offset:2048\n"
    "v_mfma_f32_16x16x32_bf16 v[184:187], a[212:215], a[232:235], v[184:187]\n"
    "v_exp_f32 v70, v70\n"
    "v_mfma_f32_16x16x32_bf16 v[188:191], a[212:215], a[236:239], v[188:191]\n"
    "v_exp_f32 v71, v71\n"
    "v_mfma_f32_16x16x32_bf16 v[192:195], a[212:215], a[240:243], v[192:195]\n"
    "v_cvt_pk_bf16_f32 v50, v68, v69\n"
    "v_cvt_pk_bf16_f32 v51, v70, v71\n"
    "v_mfma_f32_16x16x32_bf16 v[196:199], a[216:219], v[80:83], v[196:199]\n"
    "ds_read_b64_tr_b16 a[200:201], v5 offset:0\n"
    "ds_read_b64_tr_b16 a[202:203], v5 offset:2048\n"
    "v_mfma_f32_16x16x32_bf16 v[200:203], a[216:219], a[228:231], v[200:203]\n"
    "v_exp_f32 v72, v72\n"
    "v_mfma_f32_16x16x32_bf16 v[204:207], a[216:219], a[232:235], v[204:207]\n"
    "v_exp_f32 v73, v73\n"
    "v_mfma_f32_16x16x32_bf16 v[208:211], a[216:219], a[236:239], v[208:211]\n"
    "v_exp_f32 v74, v74\n"
    "v_mfma_f32_16x16x32_bf16 v[212:215], a[216:219], a[240:243], v[212:215]\n"
    "v_exp_f32 v75, v75\n"
    "v_mfma_f32_16x16x32_bf16 v[216:219], a[220:223], v[80:83], v[216:219]\n"
    "v_cvt_pk_bf16_f32 v54, v72, v73\n"
    "v_cvt_pk_bf16_f32 v55, v74, v75\n"
    "v_mfma_f32_16x16x32_bf16 v[220:223], a[220:223], a[228:231], v[220:223]\n"
    "v_exp_f32 v76, v76\n"
    "v_mfma_f32_16x16x32_bf16 v[224:227], a[220:223], a[232:235], v[224:227]\n"
    "v_exp_f32 v77, v77\n"
    "v_mfma_f32_16x16x32_bf16 v[228:231], a[220:223], a[236:239], v[228:231]\n"
    "v_exp_f32 v78, v78\n"
    "v_mfma_f32_16x16x32_bf16 v[232:235], a[220:223], a[240:243], v[232:235]\n"
    "v_exp_f32 v79, v79\n"
    "v_mfma_f32_16x16x32_bf16 v[236:239], a[224:227], v[80:83], v[236:239]\n"
    "v_cvt_pk_bf16_f32 v58, v76, v77\n"
    "v_cvt_pk_bf16_f32 v59, v78, v79\n"
    "v_mfma_f32_16x16x32_bf16 v[240:243], a[224:227], a[228:231], v[240:243]\n"
    "v_mfma_f32_16x16x32_bf16 v[244:247], a[224:227], a[232:235], v[244:247]\n"
    "v_mfma_f32_16x16x32_bf16 v[248:251], a[224:227], a[236:239], v[248:251]\n"
    "buffer_load_dwordx4 v1, s[36:39], s66 offen offset:2048 nt lds\n"
    "v_mfma_f32_16x16x32_bf16 v[252:255], a[224:227], a[240:243], v[252:255]\n"
    "ds_read_b64_tr_b16 a[204:205], v6 offset:0\n"
    "ds_read_b64_tr_b16 a[206:207], v6 offset:2048\n"
    "s_waitcnt lgkmcnt(2)\n"
    "v_mfma_f32_16x16x32_bf16 v[96:99], a[196:199], v[40:43], v[96:99]\n"
    "ds_read_b64_tr_b16 a[208:209], v7 offset:0\n"
    "ds_read_b64_tr_b16 a[210:211], v7 offset:2048\n"
    "v_mfma_f32_16x16x32_bf16 v[100:103], a[196:199], v[44:47], v[100:103]\n"
    "ds_write_b128 v8, v[40:43] offset:0\n"
    "v_mfma_f32_16x16x32_bf16 v[104:107], a[196:199], v[48:51], v[104:107]\n"
    "ds_read_b64_tr_b16 a[212:213], v4 offset:4096\n"
    "ds_read_b64_tr_b16 a[214:215], v4 offset:6144\n"
    "v_mfma_f32_16x16x32_bf16 v[108:111], a[196:199], v[52:55], v[108:111]\n"
    "ds_read_b64_tr_b16 a[216:217], v5 offset:4096\n"
    "ds_read_b64_tr_b16 a[218:219], v5 offset:6144\n"
    "v_mfma_f32_16x16x32_bf16 v[112:115], a[196:199], v[56:59], v[112:115]\n"
    "ds_read_b64_tr_b16 a[220:221], v6 offset:4096\n"
    "ds_read_b64_tr_b16 a[222:223], v6 offset:6144\n"
    "v_mfma_f32_16x16x32_bf16 v[116:119], a[200:203], v[40:43], v[116:119]\n"
    "ds_read_b64_tr_b16 a[224:225], v7 offset:4096\n"
    "ds_read_b64_tr_b16 a[226:227], v7 offset:6144\n"
    "v_mfma_f32_16x16x32_bf16 v[120:123], a[200:203], v[44:47], v[120:123]\n"
    "ds_write_b128 v8, v[44:47] offset:1024\n"
    "v_mfma_f32_16x16x32_bf16 v[124:127], a[200:203], v[48:51], v[124:127]\n"
    "v_mfma_f32_16x16x32_bf16 v[128:131], a[200:203], v[52:55], v[128:131]\n"
    "buffer_load_dwordx4 v1, s[36:39], s66 offen offset:3072 nt lds\n"
    "v_mfma_f32_16x16x32_bf16 v[132:135], a[200:203], v[56:59], v[132:135]\n"
    "s_add_u32 s66, s66, 32768\n"
    "s_waitcnt lgkmcnt(10)\n"
    "v_mfma_f32_16x16x32_bf16 v[136:139], a[204:207], v[40:43], v[136:139]\n"
    "v_mfma_f32_16x16x32_bf16 v[140:143], a[204:207], v[44:47], v[140:143]\n"
    "ds_write_b128 v8, v[48:51] offset:2048\n"
    "v_mfma_f32_16x16x32_bf16 v[144:147], a[204:207], v[48:51], v[144:147]\n"
    "v_mfma_f32_16x16x32_bf16 v[148:151], a[204:207], v[52:55], v[148:151]\n"
    "v_mfma_f32_16x16x32_bf16 v[152:155], a[204:207], v[56:59], v[152:155]\n"
    "v_mfma_f32_16x16x32_bf16 v[156:159], a[208:211], v[40:43], v[156:159]\n"
    "v_mfma_f32_16x16x32_bf16 v[160:163], a[208:211], v[44:47], v[160:163]\n"
    "ds_write_b128 v8, v[52:55] offset:3072\n"
    "v_mfma_f32_16x16x32_bf16 v[164:167], a[208:211], v[48:51], v[164:167]\n"
    "s_add_u32 m0, s72, 8192\n"
    "v_mfma_f32_16x16x32_bf16 v[168:171], a[208:211], v[52:55], v[168:171]\n"
    "buffer_load_dwordx4 v1, s[40:43], s69 offen nt lds\n"
    "v_mfma_f32_16x16x32_bf16 v[172:175], a[208:211], v[56:59], v[172:175]\n"
    "s_waitcnt lgkmcnt(2)\n"
    "v_mfma_f32_16x16x32_bf16 v[176:179], a[212:215], v[40:43], v[176:179]\n"
    "ds_write_b128 v8, v[56:59] offset:4096\n"
    "v_mfma_f32_16x16x32_bf16 v[180:183], a[212:215], v[44:47], v[180:183]\n"
    "v_mfma_f32_16x16x32_bf16 v[184:187], a[212:215], v[48:51], v[184:187]\n"
    "v_mfma_f32_16x16x32_bf16 v[188:191], a[212:215], v[52:55], v[188:191]\n"
    "v_mfma_f32_16x16x32_bf16 v[192:195], a[212:215], v[56:59], v[192:195]\n"
    "v_mfma_f32_16x16x32_bf16 v[196:199], a[216:219], v[40:43], v[196:199]\n"
    "v_mfma_f32_16x16x32_bf16 v[200:203], a[216:219], v[44:47], v[200:203]\n"
    "s_waitcnt vmcnt(9) lgkmcnt(0)\n"
    "s_barrier\n"
    "v_mfma_f32_16x16x32_bf16 v[204:207], a[216:219], v[48:51], v[204:207]\n"
    "v_mfma_f32_16x16x32_bf16 v[208:211], a[216:219], v[52:55], v[208:211]\n"
    "buffer_load_dwordx4 v1, s[40:43], s69 offen offset:1024 nt lds\n"
    "v_mfma_f32_16x16x32_bf16 v[212:215], a[216:219], v[56:59], v[212:215]\n"
    "ds_read_b128 a[180:183], v2 offset:16384\n"
    "v_mfma_f32_16x16x32_bf16 v[216:219], a[220:223], v[40:43], v[216:219]\n"
    "v_mfma_f32_16x16x32_bf16 v[220:223], a[220:223], v[44:47], v[220:223]\n"
    "v_mfma_f32_16x16x32_bf16 v[224:227], a[220:223], v[48:51], v[224:227]\n"
    "v_mfma_f32_16x16x32_bf16 v[228:231], a[220:223], v[52:55], v[228:231]\n"
    "ds_read_b128 a[184:187], v3 offset:16384\n"
    "v_mfma_f32_16x16x32_bf16 v[232:235], a[220:223], v[56:59], v[232:235]\n"
    "v_mfma_f32_16x16x32_bf16 v[236:239], a[224:227], v[40:43], v[236:239]\n"
    "buffer_load_dwordx4 v1, s[40:43], s69 offen offset:2048 nt lds\n"
    "v_mfma_f32_16x16x32_bf16 v[240:243], a[224:227], v[44:47], v[240:243]\n"
    "v_mfma_f32_16x16x32_bf16 v[244:247], a[224:227], v[48:51], v[244:247]\n"
    "ds_read_b128 a[188:191], v2 offset:20480\n"
    "v_mfma_f32_16x16x32_bf16 v[248:251], a[224:227], v[52:55], v[248:251]\n"
    "v_mfma_f32_16x16x32_bf16 v[252:255], a[224:227], v[56:59], v[252:255]\n"
    "v_mfma_f32_16x16x32_bf16 a[160:163], a[244:247], v[40:43], a[160:163]\n"
    "v_mfma_f32_16x16x32_bf16 a[164:167], a[244:247], v[44:47], a[164:167]\n"
    "ds_read_b128 a[192:195], v3 offset:20480\n"
    "v_mfma_f32_16x16x32_bf16 a[168:171], a[244:247], v[48:51], a[168:171]\n"
    "buffer_load_dwordx4 v1, s[40:43], s69 offen offset:3072 nt lds\n"
    "v_mfma_f32_16x16x32_bf16 a[172:175], a[244:247], v[52:55], a[172:175]\n"
    "s_add_u32 s69, s69, 32768\n"
    "v_mfma_f32_16x16x32_bf16 a[176:179], a[244:247], v[56:59], a[176:179]\n"
    "s_sub_u32 s75, s75, 1\n"
    "s_branch LOOP_%=\n"
    "P14_%=:\n"
    "ds_read_b128 v[80:83], v9 offset:0\n"
    "s_waitcnt lgkmcnt(3)\n"
    "v_mfma_f32_16x16x32_bf16 v[40:43], a[180:183], a[0:3], v[20:23]\n"
    "ds_read_b128 a[228:231], v9 offset:1024\n"
    "v_lshlrev_b32 v18, 7, v0\n"
    "v_mfma_f32_16x16x32_bf16 v[40:43], a[184:187], a[20:23], v[40:43]\n"
    "ds_read_b128 a[232:235], v9 offset:2048\n"
    "s_mul_i32 s80, s23, 14336\n"
    "v_mfma_f32_16x16x32_bf16 v[44:47], a[180:183], a[4:7], v[24:27]\n"
    "ds_read_b128 a[236:239], v9 offset:3072\n"
    "s_lshl_b32 s61, s65, 13\n"
    "v_mfma_f32_16x16x32_bf16 v[44:47], a[184:187], a[24:27], v[44:47]\n"
    "ds_read_b128 a[240:243], v9 offset:4096\n"
    "ds_read_b32 v91, v13 offset:1280\n"
    "v_mfma_f32_16x16x32_bf16 v[48:51], a[180:183], a[8:11], v[28:31]\n"
    "ds_read_b64_tr_b16 a[196:197], v4 offset:40960\n"
    "ds_read_b64_tr_b16 a[198:199], v4 offset:43008\n"
    "v_mfma_f32_16x16x32_bf16 v[48:51], a[184:187], a[28:31], v[48:51]\n"
    "s_add_u32 s61, s61, s80\n"
    "v_mfma_f32_16x16x32_bf16 v[52:55], a[180:183], a[12:15], v[32:35]\n"
    "buffer_load_dword v18, v18, s[48:51], s61 offen\n"
    "v_mfma_f32_16x16x32_bf16 v[52:55], a[184:187], a[32:35], v[52:55]\n"
    "s_add_u32 m0, s70, 20480\n"
    "ds_read_b64_tr_b16 a[200:201], v5 offset:40960\n"
    "v_mfma_f32_16x16x32_bf16 v[56:59], a[180:183], a[16:19], v[36:39]\n"
    "buffer_load_dwordx4 v1, s[36:39], s67 offen nt lds\n"
    "v_mfma_f32_16x16x32_bf16 v[56:59], a[184:187], a[36:39], v[56:59]\n"
    "ds_read_b128 a[180:183], v2 offset:8192\n"
    "ds_read_b128 a[184:187], v3 offset:8192\n"
    "s_waitcnt lgkmcnt(9)\n"
    "v_mfma_f32_16x16x32_bf16 v[40:43], a[188:191], a[40:43], v[40:43]\n"
    "ds_read_b64_tr_b16 a[202:203], v5 offset:43008\n"
    "ds_read_b64_tr_b16 a[204:205], v6 offset:40960\n"
    "v_mfma_f32_16x16x32_bf16 v[40:43], a[192:195], a[60:63], v[40:43]\n"
    "ds_read_b64_tr_b16 a[206:207], v6 offset:43008\n"
    "v_lshrrev_b32 v94, 3, v0\n"
    "v_mfma_f32_16x16x32_bf16 v[44:47], a[188:191], a[44:47], v[44:47]\n"
    "v_and_b32 v95, 6, v94\n"
    "v_and_b32 v14, 7, v0\n"
    "v_mfma_f32_16x16x32_bf16 v[44:47], a[192:195], a[64:67], v[44:47]\n"
    "ds_read_b64_tr_b16 a[208:209], v7 offset:40960\n"
    "ds_read_b64_tr_b16 a[210:211], v7 offset:43008\n"
    "v_mfma_f32_16x16x32_bf16 v[48:51], a[188:191], a[48:51], v[48:51]\n"
    "v_xor_b32 v95, v95, v14\n"
    "v_lshlrev_b32 v95, 4, v95\n"
    "v_mfma_f32_16x16x32_bf16 v[48:51], a[192:195], a[68:71], v[48:51]\n"
    "v_mul_u32_u24 v14, 896, v94\n"
    "v_add_u32 v14, v14, v95\n"
    "v_mfma_f32_16x16x32_bf16 v[52:55], a[188:191], a[52:55], v[52:55]\n"
    "buffer_load_dwordx4 v1, s[36:39], s67 offen offset:1024 nt lds\n"
    "v_mfma_f32_16x16x32_bf16 v[52:55], a[192:195], a[72:75], v[52:55]\n"
    "ds_read_b64_tr_b16 a[212:213], v4 offset:45056\n"
    "ds_read_b64_tr_b16 a[214:215], v4 offset:47104\n"
    "v_mfma_f32_16x16x32_bf16 v[56:59], a[188:191], a[56:59], v[56:59]\n"
    "ds_read_b128 a[188:191], v2 offset:12288\n"
    "v_add_u32 v15, 6144, v14\n"
    "v_mfma_f32_16x16x32_bf16 v[56:59], a[192:195], a[76:79], v[56:59]\n"
    "ds_read_b128 a[192:195], v3 offset:12288\n"
    "ds_read_b64_tr_b16 a[216:217], v5 offset:45056\n"
    "s_waitcnt lgkmcnt(8)\n"
    "v_mfma_f32_16x16x32_bf16 v[40:43], a[180:183], a[80:83], v[40:43]\n"
    "ds_read_b64_tr_b16 a[218:219], v5 offset:47104\n"
    "v_add_u32 v16, 12288, v14\n"
    "v_mfma_f32_16x16x32_bf16 v[40:43], a[184:187], a[100:103], v[40:43]\n"
    "v_add_u32 v17, 18432, v14\n"
    "s_lshl_b32 s80, s65, 1\n"
    "v_mfma_f32_16x16x32_bf16 v[44:47], a[180:183], a[84:87], v[44:47]\n"
    "ds_read_b64_tr_b16 a[220:221], v6 offset:45056\n"
    "ds_read_b64_tr_b16 a[222:223], v6 offset:47104\n"
    "v_mfma_f32_16x16x32_bf16 v[44:47], a[184:187], a[104:107], v[44:47]\n"
    "s_add_u32 s80, s80, s64\n"
    "s_mul_i32 s96, s80, 57344\n"
    "v_mfma_f32_16x16x32_bf16 v[48:51], a[180:183], a[88:91], v[48:51]\n"
    "buffer_load_dwordx4 v1, s[36:39], s67 offen offset:2048 nt lds\n"
    "v_mfma_f32_16x16x32_bf16 v[48:51], a[184:187], a[108:111], v[48:51]\n"
    "ds_read_b64_tr_b16 a[224:225], v7 offset:45056\n"
    "ds_read_b64_tr_b16 a[226:227], v7 offset:47104\n"
    "v_mfma_f32_16x16x32_bf16 v[52:55], a[180:183], a[92:95], v[52:55]\n"
    "s_lshl_b32 s62, s80, 13\n"
    "s_add_u32 s62, s62, %3\n"
    "v_mfma_f32_16x16x32_bf16 v[52:55], a[184:187], a[112:115], v[52:55]\n"
    "s_add_u32 s62, s62, 65536\n"
    "v_mfma_f32_16x16x32_bf16 v[56:59], a[180:183], a[96:99], v[56:59]\n"
    "ds_read_b128 a[180:183], v2 offset:2048\n"
    "v_mfma_f32_16x16x32_bf16 v[56:59], a[184:187], a[116:119], v[56:59]\n"
    "ds_read_b128 a[184:187], v3 offset:2048\n"
    "s_waitcnt lgkmcnt(6)\n"
    "v_mfma_f32_16x16x32_bf16 v[40:43], a[188:191], a[120:123], v[40:43]\n"
    "v_mfma_f32_16x16x32_bf16 v[40:43], a[192:195], a[140:143], v[40:43]\n"
    "v_mfma_f32_16x16x32_bf16 v[44:47], a[188:191], a[124:127], v[44:47]\n"
    "buffer_load_dwordx4 v1, s[36:39], s67 offen offset:3072 nt lds\n"
    "v_mfma_f32_16x16x32_bf16 v[44:47], a[192:195], a[144:147], v[44:47]\n"
    "s_add_u32 s67, s67, 32768\n"
    "v_mfma_f32_16x16x32_bf16 v[48:51], a[188:191], a[128:131], v[48:51]\n"
    "v_mfma_f32_16x16x32_bf16 v[48:51], a[192:195], a[148:151], v[48:51]\n"
    "v_mfma_f32_16x16x32_bf16 v[52:55], a[188:191], a[132:135], v[52:55]\n"
    "v_mfma_f32_16x16x32_bf16 v[52:55], a[192:195], a[152:155], v[52:55]\n"
    "v_mfma_f32_16x16x32_bf16 v[56:59], a[188:191], a[136:139], v[56:59]\n"
    "s_add_u32 m0, s71, 8192\n"
    "ds_read_b128 a[188:191], v2 offset:6144\n"
    "v_mfma_f32_16x16x32_bf16 v[56:59], a[192:195], a[156:159], v[56:59]\n"
    "buffer_load_dwordx4 v1, s[40:43], s68 offen nt lds\n"
    "s_waitcnt lgkmcnt(1)\n"
    "v_mfma_f32_16x16x32_bf16 v[60:63], a[180:183], a[0:3], v[20:23]\n"
    "ds_read_b128 a[192:195], v3 offset:6144\n"
    "v_max3_f32 v10, v40, v41, v91\n"
    "v_mfma_f32_16x16x32_bf16 v[60:63], a[184:187], a[20:23], v[60:63]\n"
    "v_max3_f32 v10, v10, v42, v43\n"
    "v_max3_f32 v10, v10, v44, v45\n"
    "v_mfma_f32_16x16x32_bf16 v[64:67], a[180:183], a[4:7], v[24:27]\n"
    "v_max3_f32 v10, v10, v46, v47\n"
    "v_max3_f32 v10, v10, v48, v49\n"
    "v_mfma_f32_16x16x32_bf16 v[64:67], a[184:187], a[24:27], v[64:67]\n"
    "v_max3_f32 v10, v10, v50, v51\n"
    "v_max3_f32 v10, v10, v52, v53\n"
    "v_mfma_f32_16x16x32_bf16 v[68:71], a[180:183], a[8:11], v[28:31]\n"
    "v_max3_f32 v10, v10, v54, v55\n"
    "v_max3_f32 v10, v10, v56, v57\n"
    "v_mfma_f32_16x16x32_bf16 v[68:71], a[184:187], a[28:31], v[68:71]\n"
    "v_max3_f32 v10, v10, v58, v59\n"
    "v_cmp_lt_f32 vcc, s73, v10\n"
    "v_mfma_f32_16x16x32_bf16 v[72:75], a[180:183], a[12:15], v[32:35]\n"
    "v_mfma_f32_16x16x32_bf16 v[72:75], a[184:187], a[32:35], v[72:75]\n"
    "v_mfma_f32_16x16x32_bf16 v[76:79], a[180:183], a[16:19], v[36:39]\n"
    "ds_read_b128 a[180:183], v2 offset:10240\n"
    "v_mfma_f32_16x16x32_bf16 v[76:79], a[184:187], a[36:39], v[76:79]\n"
    "s_cbranch_vccnz R0_x_%=\n"
    "R0R_x_%=:\n"
    "ds_read_b128 a[184:187], v3 offset:10240\n"
    "s_waitcnt lgkmcnt(2)\n"
    "v_mfma_f32_16x16x32_bf16 v[60:63], a[188:191], a[40:43], v[60:63]\n"
    "v_exp_f32 v40, v40\n"
    "v_mfma_f32_16x16x32_bf16 v[60:63], a[192:195], a[60:63], v[60:63]\n"
    "v_exp_f32 v41, v41\n"
    "v_mfma_f32_16x16x32_bf16 v[64:67], a[188:191], a[44:47], v[64:67]\n"
    "v_exp_f32 v42, v42\n"
    "v_mfma_f32_16x16x32_bf16 v[64:67], a[192:195], a[64:67], v[64:67]\n"
    "v_exp_f32 v43, v43\n"
    "v_mfma_f32_16x16x32_bf16 v[68:71], a[188:191], a[48:51], v[68:71]\n"
    "buffer_load_dwordx4 v1, s[40:43], s68 offen offset:1024 nt lds\n"
    "v_mfma_f32_16x16x32_bf16 v[68:71], a[192:195], a[68:71], v[68:71]\n"
    "v_cvt_pk_bf16_f32 v40, v40, v41\n"
    "v_cvt_pk_bf16_f32 v41, v42, v43\n"
    "v_mfma_f32_16x16x32_bf16 v[72:75], a[188:191], a[52:55], v[72:75]\n"
    "v_exp_f32 v44, v44\n"
    "v_mfma_f32_16x16x32_bf16 v[72:75], a[192:195], a[72:75], v[72:75]\n"
    "v_exp_f32 v45, v45\n"
    "v_mfma_f32_16x16x32_bf16 v[76:79], a[188:191], a[56:59], v[76:79]\n"
    "ds_read_b128 a[188:191], v2 offset:14336\n"
    "v_mfma_f32_16x16x32_bf16 v[76:79], a[192:195], a[76:79], v[76:79]\n"
    "ds_read_b128 a[192:195], v3 offset:14336\n"
    "s_waitcnt lgkmcnt(2)\n"
    "v_mfma_f32_16x16x32_bf16 v[60:63], a[180:183], a[80:83], v[60:63]\n"
    "v_exp_f32 v46, v46\n"
    "v_mfma_f32_16x16x32_bf16 v[60:63], a[184:187], a[100:103], v[60:63]\n"
    "v_exp_f32 v47, v47\n"
    "v_mfma_f32_16x16x32_bf16 v[64:67], a[180:183], a[84:87], v[64:67]\n"
    "buffer_load_dwordx4 v1, s[40:43], s68 offen offset:2048 nt lds\n"
    "v_mfma_f32_16x16x32_bf16 v[64:67], a[184:187], a[104:107], v[64:67]\n"
    "v_cvt_pk_bf16_f32 v44, v44, v45\n"
    "v_cvt_pk_bf16_f32 v45, v46, v47\n"
    "v_mfma_f32_16x16x32_bf16 v[68:71], a[180:183], a[88:91], v[68:71]\n"
    "v_exp_f32 v48, v48\n"
    "v_mfma_f32_16x16x32_bf16 v[68:71], a[184:187], a[108:111], v[68:71]\n"
    "v_exp_f32 v49, v49\n"
    "v_mfma_f32_16x16x32_bf16 v[72:75], a[180:183], a[92:95], v[72:75]\n"
    "v_exp_f32 v50, v50\n"
    "v_mfma_f32_16x16x32_bf16 v[72:75], a[184:187], a[112:115], v[72:75]\n"
    "v_exp_f32 v51, v51\n"
    "v_mfma_f32_16x16x32_bf16 v[76:79], a[180:183], a[96:99], v[76:79]\n"
    "v_cvt_pk_bf16_f32 v48, v48, v49\n"
    "v_cvt_pk_bf16_f32 v49, v50, v51\n"
    "v_mfma_f32_16x16x32_bf16 v[76:79], a[184:187], a[116:119], v[76:79]\n"
    "buffer_load_dwordx4 v1, s[40:43], s68 offen offset:3072 nt lds\n"
    "s_waitcnt lgkmcnt(0)\n"
    "v_mfma_f32_16x16x32_bf16 v[60:63], a[188:191], a[120:123], v[60:63]\n"
    "s_add_u32 s68, s68, 32768\n"
    "v_mfma_f32_16x16x32_bf16 v[60:63], a[192:195], a[140:143], v[60:63]\n"
    "s_waitcnt vmcnt(17)\n"
    "s_barrier\n"
    "v_mfma_f32_16x16x32_bf16 v[64:67], a[188:191], a[124:127], v[64:67]\n"
    "v_exp_f32 v52, v52\n"
    "v_mfma_f32_16x16x32_bf16 v[64:67], a[192:195], a[144:147], v[64:67]\n"
    "v_exp_f32 v53, v53\n"
    "v_mfma_f32_16x16x32_bf16 v[68:71], a[188:191], a[128:131], v[68:71]\n"
    "v_exp_f32 v54, v54\n"
    "v_mfma_f32_16x16x32_bf16 v[68:71], a[192:195], a[148:151], v[68:71]\n"
    "v_exp_f32 v55, v55\n"
    "v_mfma_f32_16x16x32_bf16 v[72:75], a[188:191], a[132:135], v[72:75]\n"
    "v_cvt_pk_bf16_f32 v52, v52, v53\n"
    "v_cvt_pk_bf16_f32 v53, v54, v55\n"
    "v_mfma_f32_16x16x32_bf16 v[72:75], a[192:195], a[152:155], v[72:75]\n"
    "s_add_u32 m0, s70, 0\n"
    "s_add_u32 s61, s96, 0\n"
    "v_mfma_f32_16x16x32_bf16 v[76:79], a[188:191], a[136:139], v[76:79]\n"
    "buffer_load_dwordx4 v14, s[48:51], s61 offen lds\n"
    "v_mfma_f32_16x16x32_bf16 v[76:79], a[192:195], a[156:159], v[76:79]\n"
    "v_exp_f32 v56, v56\n"
    "v_exp_f32 v57, v57\n"
    "v_exp_f32 v58, v58\n"
    "v_exp_f32 v59, v59\n"
    "v_mfma_f32_16x16x32_bf16 v[96:99], a[196:199], v[80:83], v[96:99]\n"
    "v_cvt_pk_bf16_f32 v56, v56, v57\n"
    "v_cvt_pk_bf16_f32 v57, v58, v59\n"
    "v_mfma_f32_16x16x32_bf16 v[100:103], a[196:199], a[228:231], v[100:103]\n"
    "v_max3_f32 v10, v60, v61, v61\n"
    "v_max3_f32 v10, v10, v62, v63\n"
    "v_mfma_f32_16x16x32_bf16 v[104:107], a[196:199], a[232:235], v[104:107]\n"
    "v_max3_f32 v10, v10, v64, v65\n"
    "v_max3_f32 v10, v10, v66, v67\n"
    "v_mfma_f32_16x16x32_bf16 v[108:111], a[196:199], a[236:239], v[108:111]\n"
    "v_max3_f32 v10, v10, v68, v69\n"
    "v_max3_f32 v10, v10, v70, v71\n"
    "v_mfma_f32_16x16x32_bf16 v[112:115], a[196:199], a[240:243], v[112:115]\n"
    "v_max3_f32 v10, v10, v72, v73\n"
    "v_max3_f32 v10, v10, v74, v75\n"
    "v_mfma_f32_16x16x32_bf16 v[116:119], a[200:203], v[80:83], v[116:119]\n"
    "v_max3_f32 v10, v10, v76, v77\n"
    "v_max3_f32 v10, v10, v78, v79\n"
    "v_mfma_f32_16x16x32_bf16 v[120:123], a[200:203], a[228:231], v[120:123]\n"
    "v_cmp_lt_f32 vcc, s85, v10\n"
    "v_mfma_f32_16x16x32_bf16 v[124:127], a[200:203], a[232:235], v[124:127]\n"
    "s_cbranch_vccnz R1_x_%=\n"
    "R1R_x_%=:\n"
    "v_mfma_f32_16x16x32_bf16 v[128:131], a[200:203], a[236:239], v[128:131]\n"
    "v_exp_f32 v60, v60\n"
    "v_mfma_f32_16x16x32_bf16 v[132:135], a[200:203], a[240:243], v[132:135]\n"
    "v_exp_f32 v61, v61\n"
    "v_mfma_f32_16x16x32_bf16 v[136:139], a[204:207], v[80:83], v[136:139]\n"
    "v_exp_f32 v62, v62\n"
    "v_mfma_f32_16x16x32_bf16 v[140:143], a[204:207], a[228:231], v[140:143]\n"
    "v_exp_f32 v63, v63\n"
    "v_mfma_f32_16x16x32_bf16 v[144:147], a[204:207], a[232:235], v[144:147]\n"
    "v_cvt_pk_bf16_f32 v42, v60, v61\n"
    "v_cvt_pk_bf16_f32 v43, v62, v63\n"
    "v_mfma_f32_16x16x32_bf16 v[148:151], a[204:207], a[236:239], v[148:151]\n"
    "v_exp_f32 v64, v64\n"
    "v_mfma_f32_16x16x32_bf16 v[152:155], a[204:207], a[240:243], v[152:155]\n"
    "v_exp_f32 v65, v65\n"
    "v_mfma_f32_16x16x32_bf16 v[156:159], a[208:211], v[80:83], v[156:159]\n"
    "v_exp_f32 v66, v66\n"
    "v_mfma_f32_16x16x32_bf16 v[160:163], a[208:211], a[228:231], v[160:163]\n"
    "v_exp_f32 v67, v67\n"
    "v_mfma_f32_16x16x32_bf16 v[164:167], a[208:211], a[232:235], v[164:167]\n"
    "v_cvt_pk_bf16_f32 v46, v64, v65\n"
    "v_cvt_pk_bf16_f32 v47, v66, v67\n"
    "v_mfma_f32_16x16x32_bf16 v[168:171], a[208:211], a[236:239], v[168:171]\n"
    "v_exp_f32 v68, v68\n"
    "v_mfma_f32_16x16x32_bf16 v[172:175], a[208:211], a[240:243], v[172:175]\n"
    "v_exp_f32 v69, v69\n"
    "v_mfma_f32_16x16x32_bf16 v[176:179], a[212:215], v[80:83], v[176:179]\n"
    "buffer_load_dwordx4 v15, s[48:51], s61 offen offset:1024 lds\n"
    "v_mfma_f32_16x16x32_bf16 v[180:183], a[212:215], a[228:231], v[180:183]\n"
    "ds_read_b64_tr_b16 a[196:197], v4 offset:0\n"
    "ds_read_b64_tr_b16 a[198:199], v4 offset:2048\n"
    "v_mfma_f32_16x16x32_bf16 v[184:187], a[212:215], a[232:235], v[184:187]\n"
    "v_exp_f32 v70, v70\n"
    "v_mfma_f32_16x16x32_bf16 v[188:191], a[212:215], a[236:239], v[188:191]\n"
    "v_exp_f32 v71, v71\n"
    "v_mfma_f32_16x16x32_bf16 v[192:195], a[212:215], a[240:243], v[192:195]\n"
    "v_cvt_pk_bf16_f32 v50, v68, v69\n"
    "v_cvt_pk_bf16_f32 v51, v70, v71\n"
    "v_mfma_f32_16x16x32_bf16 v[196:199], a[216:219], v[80:83], v[196:199]\n"
    "ds_read_b64_tr_b16 a[200:201], v5 offset:0\n"
    "ds_read_b64_tr_b16 a[202:203], v5 offset:2048\n"
    "v_mfma_f32_16x16x32_bf16 v[200:203], a[216:219], a[228:231], v[200:203]\n"
    "v_exp_f32 v72, v72\n"
    "v_mfma_f32_16x16x32_bf16 v[204:207], a[216:219], a[232:235], v[204:207]\n"
    "v_exp_f32 v73, v73\n"
    "v_mfma_f32_16x16x32_bf16 v[208:211], a[216:219], a[236:239], v[208:211]\n"
    "v_exp_f32 v74, v74\n"
    "v_mfma_f32_16x16x32_bf16 v[212:215], a[216:219], a[240:243], v[212:215]\n"
    "v_exp_f32 v75, v75\n"
    "v_mfma_f32_16x16x32_bf16 v[216:219], a[220:223], v[80:83], v[216:219]\n"
    "v_cvt_pk_bf16_f32 v54, v72, v73\n"
    "v_cvt_pk_bf16_f32 v55, v74, v75\n"
    "v_mfma_f32_16x16x32_bf16 v[220:223], a[220:223], a[228:231], v[220:223]\n"
    "v_exp_f32 v76, v76\n"
    "v_mfma_f32_16x16x32_bf16 v[224:227], a[220:223], a[232:235], v[224:227]\n"
    "v_exp_f32 v77, v77\n"
    "v_mfma_f32_16x16x32_bf16 v[228:231], a[220:223], a[236:239], v[228:231]\n"
    "v_exp_f32 v78, v78\n"
    "v_mfma_f32_16x16x32_bf16 v[232:235], a[220:223], a[240:243], v[232:235]\n"
    "v_exp_f32 v79, v79\n"
    "v_mfma_f32_16x16x32_bf16 v[236:239], a[224:227], v[80:83], v[236:239]\n"
    "v_cvt_pk_bf16_f32 v58, v76, v77\n"
    "v_cvt_pk_bf16_f32 v59, v78, v79\n"
    "v_mfma_f32_16x16x32_bf16 v[240:243], a[224:227], a[228:231], v[240:243]\n"
    "v_mfma_f32_16x16x32_bf16 v[244:247], a[224:227], a[232:235], v[244:247]\n"
    "v_mfma_f32_16x16x32_bf16 v[248:251], a[224:227], a[236:239], v[248:251]\n"
    "buffer_load_dwordx4 v16, s[48:51], s61 offen offset:2048 lds\n"
    "v_mfma_f32_16x16x32_bf16 v[252:255], a[224:227], a[240:243], v[252:255]\n"
    "ds_read_b64_tr_b16 a[204:205], v6 offset:0\n"
    "ds_read_b64_tr_b16 a[206:207], v6 offset:2048\n"
    "s_waitcnt lgkmcnt(2)\n"
    "v_mfma_f32_16x16x32_bf16 v[96:99], a[196:199], v[40:43], v[96:99]\n"
    "ds_read_b64_tr_b16 a[208:209], v7 offset:0\n"
    "ds_read_b64_tr_b16 a[210:211], v7 offset:2048\n"
    "v_mfma_f32_16x16x32_bf16 v[100:103], a[196:199], v[44:47], v[100:103]\n"
    "ds_write_b128 v8, v[40:43] offset:0\n"
    "v_mfma_f32_16x16x32_bf16 v[104:107], a[196:199], v[48:51], v[104:107]\n"
    "ds_read_b64_tr_b16 a[212:213], v4 offset:4096\n"
    "ds_read_b64_tr_b16 a[214:215], v4 offset:6144\n"
    "v_mfma_f32_16x16x32_bf16 v[108:111], a[196:199], v[52:55], v[108:111]\n"
    "ds_read_b64_tr_b16 a[216:217], v5 offset:4096\n"
    "ds_read_b64_tr_b16 a[218:219], v5 offset:6144\n"
    "v_mfma_f32_16x16x32_bf16 v[112:115], a[196:199], v[56:59], v[112:115]\n"
    "ds_read_b64_tr_b16 a[220:221], v6 offset:4096\n"
    "ds_read_b64_tr_b16 a[222:223], v6 offset:6144\n"
    "v_mfma_f32_16x16x32_bf16 v[116:119], a[200:203], v[40:43], v[116:119]\n"
    "ds_read_b64_tr_b16 a[224:225], v7 offset:4096\n"
    "ds_read_b64_tr_b16 a[226:227], v7 offset:6144\n"
    "v_mfma_f32_16x16x32_bf16 v[120:123], a[200:203], v[44:47], v[120:123]\n"
    "ds_write_b128 v8, v[44:47] offset:1024\n"
    "v_mfma_f32_16x16x32_bf16 v[124:127], a[200:203], v[48:51], v[124:127]\n"
    "v_mfma_f32_16x16x32_bf16 v[128:131], a[200:203], v[52:55], v[128:131]\n"
    "buffer_load_dwordx4 v17, s[48:51], s61 offen offset:3072 lds\n"
    "v_mfma_f32_16x16x32_bf16 v[132:135], a[200:203], v[56:59], v[132:135]\n"
    "s_waitcnt lgkmcnt(10)\n"
    "v_mfma_f32_16x16x32_bf16 v[136:139], a[204:207], v[40:43], v[136:139]\n"
    "v_mfma_f32_16x16x32_bf16 v[140:143], a[204:207], v[44:47], v[140:143]\n"
    "ds_write_b128 v8, v[48:51] offset:2048\n"
    "v_mfma_f32_16x16x32_bf16 v[144:147], a[204:207], v[48:51], v[144:147]\n"
    "v_mfma_f32_16x16x32_bf16 v[148:151], a[204:207], v[52:55], v[148:151]\n"
    "v_mfma_f32_16x16x32_bf16 v[152:155], a[204:207], v[56:59], v[152:155]\n"
    "v_mfma_f32_16x16x32_bf16 v[156:159], a[208:211], v[40:43], v[156:159]\n"
    "v_mfma_f32_16x16x32_bf16 v[160:163], a[208:211], v[44:47], v[160:163]\n"
    "ds_write_b128 v8, v[52:55] offset:3072\n"
    "v_mfma_f32_16x16x32_bf16 v[164:167], a[208:211], v[48:51], v[164:167]\n"
    "s_add_u32 m0, s72, 8192\n"
    "v_mfma_f32_16x16x32_bf16 v[168:171], a[208:211], v[52:55], v[168:171]\n"
    "buffer_load_dwordx4 v1, s[40:43], s69 offen nt lds\n"
    "v_mfma_f32_16x16x32_bf16 v[172:175], a[208:211], v[56:59], v[172:175]\n"
    "s_waitcnt lgkmcnt(2)\n"
    "v_mfma_f32_16x16x32_bf16 v[176:179], a[212:215], v[40:43], v[176:179]\n"
    "ds_write_b128 v8, v[56:59] offset:4096\n"
    "v_mfma_f32_16x16x32_bf16 v[180:183], a[212:215], v[44:47], v[180:183]\n"
    "v_mfma_f32_16x16x32_bf16 v[184:187], a[212:215], v[48:51], v[184:187]\n"
    "v_mfma_f32_16x16x32_bf16 v[188:191], a[212:215], v[52:55], v[188:191]\n"
    "v_mfma_f32_16x16x32_bf16 v[192:195], a[212:215], v[56:59], v[192:195]\n"
    "v_mfma_f32_16x16x32_bf16 v[196:199], a[216:219], v[40:43], v[196:199]\n"
    "v_mfma_f32_16x16x32_bf16 v[200:203], a[216:219], v[44:47], v[200:203]\n"
    "s_waitcnt vmcnt(9) lgkmcnt(0)\n"
    "s_barrier\n"
    "v_mfma_f32_16x16x32_bf16 v[204:207], a[216:219], v[48:51], v[204:207]\n"
    "v_mfma_f32_16x16x32_bf16 v[208:211], a[216:219], v[52:55], v[208:211]\n"
    "buffer_load_dwordx4 v1, s[40:43], s69 offen offset:1024 nt lds\n"
    "v_mfma_f32_16x16x32_bf16 v[212:215], a[216:219], v[56:59], v[212:215]\n"
    "ds_read_b128 a[180:183], v2 offset:16384\n"
    "v_mfma_f32_16x16x32_bf16 v[216:219], a[220:223], v[40:43], v[216:219]\n"
    "v_mfma_f32_16x16x32_bf16 v[220:223], a[220:223], v[44:47], v[220:223]\n"
    "v_mfma_f32_16x16x32_bf16 v[224:227], a[220:223], v[48:51], v[224:227]\n"
    "v_mfma_f32_16x16x32_bf16 v[228:231], a[220:223], v[52:55], v[228:231]\n"
    "ds_read_b128 a[184:187], v3 offset:16384\n"
    "v_mfma_f32_16x16x32_bf16 v[232:235], a[220:223], v[56:59], v[232:235]\n"
    "v_mfma_f32_16x16x32_bf16 v[236:239], a[224:227], v[40:43], v[236:239]\n"
    "buffer_load_dwordx4 v1, s[40:43], s69 offen offset:2048 nt lds\n"
    "v_mfma_f32_16x16x32_bf16 v[240:243], a[224:227], v[44:47], v[240:243]\n"
    "v_mfma_f32_16x16x32_bf16 v[244:247], a[224:227], v[48:51], v[244:247]\n"
    "ds_read_b128 a[188:191], v2 offset:20480\n"
    "v_mfma_f32_16x16x32_bf16 v[248:251], a[224:227], v[52:55], v[248:251]\n"
    "v_mfma_f32_16x16x32_bf16 v[252:255], a[224:227], v[56:59], v[252:255]\n"
    "v_mfma_f32_16x16x32_bf16 a[160:163], a[244:247], v[40:43], a[160:163]\n"
    "v_mfma_f32_16x16x32_bf16 a[164:167], a[244:247], v[44:47], a[164:167]\n"
    "ds_read_b128 a[192:195], v3 offset:20480\n"
    "v_mfma_f32_16x16x32_bf16 a[168:171], a[244:247], v[48:51], a[168:171]\n"
    "buffer_load_dwordx4 v1, s[40:43], s69 offen offset:3072 nt lds\n"
    "v_mfma_f32_16x16x32_bf16 a[172:175], a[244:247], v[52:55], a[172:175]\n"
    "s_add_u32 s69, s69, 32768\n"
    "v_mfma_f32_16x16x32_bf16 a[176:179], a[244:247], v[56:59], a[176:179]\n"
    "ds_read_b128 v[80:83], v9 offset:0\n"
    "s_waitcnt lgkmcnt(3)\n"
    "v_mfma_f32_16x16x32_bf16 v[40:43], a[180:183], a[0:3], v[20:23]\n"
    "ds_read_b128 a[228:231], v9 offset:1024\n"
    "v_and_b32 v18, 15, v0\n"
    "v_mfma_f32_16x16x32_bf16 v[40:43], a[184:187], a[20:23], v[40:43]\n"
    "ds_read_b128 a[232:235], v9 offset:2048\n"
    "v_lshlrev_b32 v18, 3, v18\n"
    "v_mfma_f32_16x16x32_bf16 v[44:47], a[180:183], a[4:7], v[24:27]\n"
    "ds_read_b128 a[236:239], v9 offset:3072\n"
    "s_add_u32 s80, %3, 151552\n"
    "v_mfma_f32_16x16x32_bf16 v[44:47], a[184:187], a[24:27], v[44:47]\n"
    "ds_read_b128 a[240:243], v9 offset:4096\n"
    "ds_read_b32 v91, v13 offset:1280\n"
    "v_mfma_f32_16x16x32_bf16 v[48:51], a[180:183], a[8:11], v[28:31]\n"
    "ds_read_b64_tr_b16 a[196:197], v4 offset:32768\n"
    "ds_read_b64_tr_b16 a[198:199], v4 offset:34816\n"
    "v_mfma_f32_16x16x32_bf16 v[48:51], a[184:187], a[28:31], v[48:51]\n"
    "v_add_u32 v11, s80, v18\n"
    "s_mul_i32 s80, %2, 3072\n"
    "v_mfma_f32_16x16x32_bf16 v[52:55], a[180:183], a[12:15], v[32:35]\n"
    "v_add_u32 v19, s80, v11\n"
    "s_xor_b32 s80, %2, 1\n"
    "v_mfma_f32_16x16x32_bf16 v[52:55], a[184:187], a[32:35], v[52:55]\n"
    "s_add_u32 m0, s70, 4096\n"
    "s_add_u32 s61, s96, 28672\n"
    "v_mfma_f32_16x16x32_bf16 v[56:59], a[180:183], a[16:19], v[36:39]\n"
    "buffer_load_dwordx4 v14, s[48:51], s61 offen lds\n"
    "v_mfma_f32_16x16x32_bf16 v[56:59], a[184:187], a[36:39], v[56:59]\n"
    "ds_read_b128 a[180:183], v2 offset:24576\n"
    "ds_read_b128 a[184:187], v3 offset:24576\n"
    "s_waitcnt lgkmcnt(8)\n"
    "v_mfma_f32_16x16x32_bf16 v[40:43], a[188:191], a[40:43], v[40:43]\n"
    "ds_read_b64_tr_b16 a[200:201], v5 offset:32768\n"
    "ds_read_b64_tr_b16 a[202:203], v5 offset:34816\n"
    "v_mfma_f32_16x16x32_bf16 v[40:43], a[192:195], a[60:63], v[40:43]\n"
    "ds_read_b64_tr_b16 a[204:205], v6 offset:32768\n"
    "ds_read_b64_tr_b16 a[206:207], v6 offset:34816\n"
    "v_mfma_f32_16x16x32_bf16 v[44:47], a[188:191], a[44:47], v[44:47]\n"
    "s_mul_i32 s80, s80, 3072\n"
    "v_add_u32 v18, s80, v11\n"
    "v_mfma_f32_16x16x32_bf16 v[44:47], a[192:195], a[64:67], v[44:47]\n"
    "ds_read_b64_tr_b16 a[208:209], v7 offset:32768\n"
    "ds_read_b64_tr_b16 a[210:211], v7 offset:34816\n"
    "v_mfma_f32_16x16x32_bf16 v[48:51], a[188:191], a[48:51], v[48:51]\n"
    "v_mfma_f32_16x16x32_bf16 v[48:51], a[192:195], a[68:71], v[48:51]\n"
    "v_mfma_f32_16x16x32_bf16 v[52:55], a[188:191], a[52:55], v[52:55]\n"
    "buffer_load_dwordx4 v15, s[48:51], s61 offen offset:1024 lds\n"
    "v_mfma_f32_16x16x32_bf16 v[52:55], a[192:195], a[72:75], v[52:55]\n"
    "ds_read_b64_tr_b16 a[212:213], v4 offset:36864\n"
    "ds_read_b64_tr_b16 a[214:215], v4 offset:38912\n"
    "v_mfma_f32_16x16x32_bf16 v[56:59], a[188:191], a[56:59], v[56:59]\n"
    "ds_read_b128 a[188:191], v2 offset:28672\n"
    "v_mfma_f32_16x16x32_bf16 v[56:59], a[192:195], a[76:79], v[56:59]\n"
    "ds_read_b128 a[192:195], v3 offset:28672\n"
    "ds_read_b64_tr_b16 a[216:217], v5 offset:36864\n"
    "s_waitcnt lgkmcnt(9)\n"
    "v_mfma_f32_16x16x32_bf16 v[40:43], a[180:183], a[80:83], v[40:43]\n"
    "ds_read_b64_tr_b16 a[218:219], v5 offset:38912\n"
    "v_mfma_f32_16x16x32_bf16 v[40:43], a[184:187], a[100:103], v[40:43]\n"
    "v_mfma_f32_16x16x32_bf16 v[44:47], a[180:183], a[84:87], v[44:47]\n"
    "ds_read_b64_tr_b16 a[220:221], v6 offset:36864\n"
    "ds_read_b64_tr_b16 a[222:223], v6 offset:38912\n"
    "v_mfma_f32_16x16x32_bf16 v[44:47], a[184:187], a[104:107], v[44:47]\n"
    "v_mfma_f32_16x16x32_bf16 v[48:51], a[180:183], a[88:91], v[48:51]\n"
    "buffer_load_dwordx4 v16, s[48:51], s61 offen offset:2048 lds\n"
    "v_mfma_f32_16x16x32_bf16 v[48:51], a[184:187], a[108:111], v[48:51]\n"
    "ds_read_b64_tr_b16 a[224:225], v7 offset:36864\n"
    "ds_read_b64_tr_b16 a[226:227], v7 offset:38912\n"
    "v_mfma_f32_16x16x32_bf16 v[52:55], a[180:183], a[92:95], v[52:55]\n"
    "v_mfma_f32_16x16x32_bf16 v[52:55], a[184:187], a[112:115], v[52:55]\n"
    "v_mfma_f32_16x16x32_bf16 v[56:59], a[180:183], a[96:99], v[56:59]\n"
    "ds_read_b128 a[180:183], v2 offset:18432\n"
    "v_mfma_f32_16x16x32_bf16 v[56:59], a[184:187], a[116:119], v[56:59]\n"
    "ds_read_b128 a[184:187], v3 offset:18432\n"
    "s_waitcnt lgkmcnt(6)\n"
    "v_mfma_f32_16x16x32_bf16 v[40:43], a[188:191], a[120:123], v[40:43]\n"
    "v_mfma_f32_16x16x32_bf16 v[40:43], a[192:195], a[140:143], v[40:43]\n"
    "v_mfma_f32_16x16x32_bf16 v[44:47], a[188:191], a[124:127], v[44:47]\n"
    "buffer_load_dwordx4 v17, s[48:51], s61 offen offset:3072 lds\n"
    "v_mfma_f32_16x16x32_bf16 v[44:47], a[192:195], a[144:147], v[44:47]\n"
    "v_mfma_f32_16x16x32_bf16 v[48:51], a[188:191], a[128:131], v[48:51]\n"
    "v_mfma_f32_16x16x32_bf16 v[48:51], a[192:195], a[148:151], v[48:51]\n"
    "v_mfma_f32_16x16x32_bf16 v[52:55], a[188:191], a[132:135], v[52:55]\n"
    "v_mfma_f32_16x16x32_bf16 v[52:55], a[192:195], a[152:155], v[52:55]\n"
    "v_mfma_f32_16x16x32_bf16 v[56:59], a[188:191], a[136:139], v[56:59]\n"
    "ds_read_b128 a[188:191], v2 offset:22528\n"
    "v_mfma_f32_16x16x32_bf16 v[56:59], a[192:195], a[156:159], v[56:59]\n"
    "ds_read_b128 a[192:195], v3 offset:22528\n"
    "s_waitcnt lgkmcnt(2)\n"
    "v_mfma_f32_16x16x32_bf16 v[60:63], a[180:183], a[0:3], v[20:23]\n"
    "v_max3_f32 v10, v40, v41, v91\n"
    "v_max3_f32 v10, v10, v42, v43\n"
    "v_mfma_f32_16x16x32_bf16 v[60:63], a[184:187], a[20:23], v[60:63]\n"
    "v_max3_f32 v10, v10, v44, v45\n"
    "v_max3_f32 v10, v10, v46, v47\n"
    "v_mfma_f32_16x16x32_bf16 v[64:67], a[180:183], a[4:7], v[24:27]\n"
    "v_max3_f32 v10, v10, v48, v49\n"
    "v_max3_f32 v10, v10, v50, v51\n"
    "v_mfma_f32_16x16x32_bf16 v[64:67], a[184:187], a[24:27], v[64:67]\n"
    "v_max3_f32 v10, v10, v52, v53\n"
    "v_max3_f32 v10, v10, v54, v55\n"
    "v_mfma_f32_16x16x32_bf16 v[68:71], a[180:183], a[8:11], v[28:31]\n"
    "v_max3_f32 v10, v10, v56, v57\n"
    "v_max3_f32 v10, v10, v58, v59\n"
    "v_mfma_f32_16x16x32_bf16 v[68:71], a[184:187], a[28:31], v[68:71]\n"
    "v_cmp_lt_f32 vcc, s73, v10\n"
    "v_mfma_f32_16x16x32_bf16 v[72:75], a[180:183], a[12:15], v[32:35]\n"
    "v_mfma_f32_16x16x32_bf16 v[72:75], a[184:187], a[32:35], v[72:75]\n"
    "v_mfma_f32_16x16x32_bf16 v[76:79], a[180:183], a[16:19], v[36:39]\n"
    "ds_read_b128 a[180:183], v2 offset:26624\n"
    "v_mfma_f32_16x16x32_bf16 v[76:79], a[184:187], a[36:39], v[76:79]\n"
    "s_cbranch_vccnz R0_y_%=\n"
    "R0R_y_%=:\n"
    "ds_read_b128 a[184:187], v3 offset:26624\n"
    "s_waitcnt lgkmcnt(2)\n"
    "v_mfma_f32_16x16x32_bf16 v[60:63], a[188:191], a[40:43], v[60:63]\n"
    "v_exp_f32 v40, v40\n"
    "v_mfma_f32_16x16x32_bf16 v[60:63], a[192:195], a[60:63], v[60:63]\n"
    "v_exp_f32 v41, v41\n"
    "v_mfma_f32_16x16x32_bf16 v[64:67], a[188:191], a[44:47], v[64:67]\n"
    "v_exp_f32 v42, v42\n"
    "v_mfma_f32_16x16x32_bf16 v[64:67], a[192:195], a[64:67], v[64:67]\n"
    "v_exp_f32 v43, v43\n"
    "v_mfma_f32_16x16x32_bf16 v[68:71], a[188:191], a[48:51], v[68:71]\n"
    "v_cvt_pk_bf16_f32 v40, v40, v41\n"
    "v_cvt_pk_bf16_f32 v41, v42, v43\n"
    "v_mfma_f32_16x16x32_bf16 v[68:71], a[192:195], a[68:71], v[68:71]\n"
    "v_exp_f32 v44, v44\n"
    "v_mfma_f32_16x16x32_bf16 v[72:75], a[188:191], a[52:55], v[72:75]\n"
    "v_exp_f32 v45, v45\n"
    "v_mfma_f32_16x16x32_bf16 v[72:75], a[192:195], a[72:75], v[72:75]\n"
    "v_exp_f32 v46, v46\n"
    "v_mfma_f32_16x16x32_bf16 v[76:79], a[188:191], a[56:59], v[76:79]\n"
    "ds_read_b128 a[188:191], v2 offset:30720\n"
    "v_mfma_f32_16x16x32_bf16 v[76:79], a[192:195], a[76:79], v[76:79]\n"
    "ds_read_b128 a[192:195], v3 offset:30720\n"
    "s_waitcnt lgkmcnt(2)\n"
    "v_mfma_f32_16x16x32_bf16 v[60:63], a[180:183], a[80:83], v[60:63]\n"
    "v_exp_f32 v47, v47\n"
    "v_mfma_f32_16x16x32_bf16 v[60:63], a[184:187], a[100:103], v[60:63]\n"
    "v_cvt_pk_bf16_f32 v44, v44, v45\n"
    "v_cvt_pk_bf16_f32 v45, v46, v47\n"
    "v_mfma_f32_16x16x32_bf16 v[64:67], a[180:183], a[84:87], v[64:67]\n"
    "v_exp_f32 v48, v48\n"
    "v_mfma_f32_16x16x32_bf16 v[64:67], a[184:187], a[104:107], v[64:67]\n"
    "v_exp_f32 v49, v49\n"
    "v_mfma_f32_16x16x32_bf16 v[68:71], a[180:183], a[88:91], v[68:71]\n"
    "v_exp_f32 v50, v50\n"
    "v_mfma_f32_16x16x32_bf16 v[68:71], a[184:187], a[108:111], v[68:71]\n"
    "v_exp_f32 v51, v51\n"
    "v_mfma_f32_16x16x32_bf16 v[72:75], a[180:183], a[92:95], v[72:75]\n"
    "v_cvt_pk_bf16_f32 v48, v48, v49\n"
    "v_cvt_pk_bf16_f32 v49, v50, v51\n"
    "v_mfma_f32_16x16x32_bf16 v[72:75], a[184:187], a[112:115], v[72:75]\n"
    "v_exp_f32 v52, v52\n"
    "v_mfma_f32_16x16x32_bf16 v[76:79], a[180:183], a[96:99], v[76:79]\n"
    "v_exp_f32 v53, v53\n"
    "v_mfma_f32_16x16x32_bf16 v[76:79], a[184:187], a[116:119], v[76:79]\n"
    "v_exp_f32 v54, v54\n"
    "s_waitcnt lgkmcnt(0)\n"
    "v_mfma_f32_16x16x32_bf16 v[60:63], a[188:191], a[120:123], v[60:63]\n"
    "v_exp_f32 v55, v55\n"
    "v_mfma_f32_16x16x32_bf16 v[60:63], a[192:195], a[140:143], v[60:63]\n"
    "s_waitcnt vmcnt(12)\n"
    "s_barrier\n"
    "v_cvt_pk_bf16_f32 v52, v52, v53\n"
    "v_mfma_f32_16x16x32_bf16 v[64:67], a[188:191], a[124:127], v[64:67]\n"
    "v_cvt_pk_bf16_f32 v53, v54, v55\n"
    "v_mfma_f32_16x16x32_bf16 v[64:67], a[192:195], a[144:147], v[64:67]\n"
    "v_exp_f32 v56, v56\n"
    "v_mfma_f32_16x16x32_bf16 v[68:71], a[188:191], a[128:131], v[68:71]\n"
    "v_exp_f32 v57, v57\n"
    "v_mfma_f32_16x16x32_bf16 v[68:71], a[192:195], a[148:151], v[68:71]\n"
    "v_exp_f32 v58, v58\n"
    "v_mfma_f32_16x16x32_bf16 v[72:75], a[188:191], a[132:135], v[72:75]\n"
    "v_exp_f32 v59, v59\n"
    "v_mfma_f32_16x16x32_bf16 v[72:75], a[192:195], a[152:155], v[72:75]\n"
    "s_add_u32 m0, s70, 16384\n"
    "s_add_u32 s61, s96, 229376\n"
    "v_mfma_f32_16x16x32_bf16 v[76:79], a[188:191], a[136:139], v[76:79]\n"
    "buffer_load_dwordx4 v14, s[48:51], s61 offen lds\n"
    "v_mfma_f32_16x16x32_bf16 v[76:79], a[192:195], a[156:159], v[76:79]\n"
    "v_cvt_pk_bf16_f32 v56, v56, v57\n"
    "v_cvt_pk_bf16_f32 v57, v58, v59\n"
    "v_mfma_f32_16x16x32_bf16 v[96:99], a[196:199], v[80:83], v[96:99]\n"
    "v_max3_f32 v10, v60, v61, v61\n"
    "v_max3_f32 v10, v10, v62, v63\n"
    "v_mfma_f32_16x16x32_bf16 v[100:103], a[196:199], a[228:231], v[100:103]\n"
    "v_max3_f32 v10, v10, v64, v65\n"
    "v_max3_f32 v10, v10, v66, v67\n"
    "v_mfma_f32_16x16x32_bf16 v[104:107], a[196:199], a[232:235], v[104:107]\n"
    "v_max3_f32 v10, v10, v68, v69\n"
    "v_max3_f32 v10, v10, v70, v71\n"
    "v_mfma_f32_16x16x32_bf16 v[108:111], a[196:199], a[236:239], v[108:111]\n"
    "v_max3_f32 v10, v10, v72, v73\n"
    "v_max3_f32 v10, v10, v74, v75\n"
    "v_mfma_f32_16x16x32_bf16 v[112:115], a[196:199], a[240:243], v[112:115]\n"
    "v_max3_f32 v10, v10, v76, v77\n"
    "v_max3_f32 v10, v10, v78, v79\n"
    "v_mfma_f32_16x16x32_bf16 v[116:119], a[200:203], v[80:83], v[116:119]\n"
    "v_cmp_lt_f32 vcc, s85, v10\n"
    "v_mfma_f32_16x16x32_bf16 v[120:123], a[200:203], a[228:231], v[120:123]\n"
    "buffer_load_dword v95, off, s[44:47], s98 sc1\n"
    "v_mfma_f32_16x16x32_bf16 v[124:127], a[200:203], a[232:235], v[124:127]\n"
    "s_cbranch_vccnz R1_y_%=\n"
    "R1R_y_%=:\n"
    "v_mfma_f32_16x16x32_bf16 v[128:131], a[200:203], a[236:239], v[128:131]\n"
    "v_exp_f32 v60, v60\n"
    "v_mfma_f32_16x16x32_bf16 v[132:135], a[200:203], a[240:243], v[132:135]\n"
    "v_exp_f32 v61, v61\n"
    "v_mfma_f32_16x16x32_bf16 v[136:139], a[204:207], v[80:83], v[136:139]\n"
    "v_exp_f32 v62, v62\n"
    "v_mfma_f32_16x16x32_bf16 v[140:143], a[204:207], a[228:231], v[140:143]\n"
    "v_exp_f32 v63, v63\n"
    "v_mfma_f32_16x16x32_bf16 v[144:147], a[204:207], a[232:235], v[144:147]\n"
    "buffer_load_dwordx4 v15, s[48:51], s61 offen offset:1024 lds\n"
    "v_mfma_f32_16x16x32_bf16 v[148:151], a[204:207], a[236:239], v[148:151]\n"
    "v_cvt_pk_bf16_f32 v42, v60, v61\n"
    "v_cvt_pk_bf16_f32 v43, v62, v63\n"
    "v_mfma_f32_16x16x32_bf16 v[152:155], a[204:207], a[240:243], v[152:155]\n"
    "v_exp_f32 v64, v64\n"
    "v_mfma_f32_16x16x32_bf16 v[156:159], a[208:211], v[80:83], v[156:159]\n"
    "v_exp_f32 v65, v65\n"
    "v_mfma_f32_16x16x32_bf16 v[160:163], a[208:211], a[228:231], v[160:163]\n"
    "v_exp_f32 v66, v66\n"
    "v_mfma_f32_16x16x32_bf16 v[164:167], a[208:211], a[232:235], v[164:167]\n"
    "v_exp_f32 v67, v67\n"
    "v_mfma_f32_16x16x32_bf16 v[168:171], a[208:211], a[236:239], v[168:171]\n"
    "v_cvt_pk_bf16_f32 v46, v64, v65\n"
    "v_cvt_pk_bf16_f32 v47, v66, v67\n"
    "v_mfma_f32_16x16x32_bf16 v[172:175], a[208:211], a[240:243], v[172:175]\n"
    "v_exp_f32 v68, v68\n"
    "v_mfma_f32_16x16x32_bf16 v[176:179], a[212:215], v[80:83], v[176:179]\n"
    "ds_read_b64_tr_b16 a[196:197], v4 offset:8192\n"
    "ds_read_b64_tr_b16 a[198:199], v4 offset:10240\n"
    "v_mfma_f32_16x16x32_bf16 v[180:183], a[212:215], a[228:231], v[180:183]\n"
    "v_exp_f32 v69, v69\n"
    "v_mfma_f32_16x16x32_bf16 v[184:187], a[212:215], a[232:235], v[184:187]\n"
    "v_exp_f32 v70, v70\n"
    "v_mfma_f32_16x16x32_bf16 v[188:191], a[212:215], a[236:239], v[188:191]\n"
    "v_exp_f32 v71, v71\n"
    "v_mfma_f32_16x16x32_bf16 v[192:195], a[212:215], a[240:243], v[192:195]\n"
    "buffer_load_dwordx4 v16, s[48:51], s61 offen offset:2048 lds\n"
    "v_mfma_f32_16x16x32_bf16 v[196:199], a[216:219], v[80:83], v[196:199]\n"
    "ds_read_b64_tr_b16 a[200:201], v5 offset:8192\n"
    "ds_read_b64_tr_b16 a[202:203], v5 offset:10240\n"
    "v_mfma_f32_16x16x32_bf16 v[200:203], a[216:219], a[228:231], v[200:203]\n"
    "v_cvt_pk_bf16_f32 v50, v68, v69\n"
    "v_cvt_pk_bf16_f32 v51, v70, v71\n"
    "v_mfma_f32_16x16x32_bf16 v[204:207], a[216:219], a[232:235], v[204:207]\n"
    "v_exp_f32 v72, v72\n"
    "v_mfma_f32_16x16x32_bf16 v[208:211], a[216:219], a[236:239], v[208:211]\n"
    "v_exp_f32 v73, v73\n"
    "v_mfma_f32_16x16x32_bf16 v[212:215], a[216:219], a[240:243], v[212:215]\n"
    "v_exp_f32 v74, v74\n"
    "v_mfma_f32_16x16x32_bf16 v[216:219], a[220:223], v[80:83], v[216:219]\n"
    "v_exp_f32 v75, v75\n"
    "v_mfma_f32_16x16x32_bf16 v[220:223], a[220:223], a[228:231], v[220:223]\n"
    "v_cvt_pk_bf16_f32 v54, v72, v73\n"
    "v_cvt_pk_bf16_f32 v55, v74, v75\n"
    "v_mfma_f32_16x16x32_bf16 v[224:227], a[220:223], a[232:235], v[224:227]\n"
    "v_exp_f32 v76, v76\n"
    "v_mfma_f32_16x16x32_bf16 v[228:231], a[220:223], a[236:239], v[228:231]\n"
    "v_exp_f32 v77, v77\n"
    "v_mfma_f32_16x16x32_bf16 v[232:235], a[220:223], a[240:243], v[232:235]\n"
    "v_exp_f32 v78, v78\n"
    "v_mfma_f32_16x16x32_bf16 v[236:239], a[224:227], v[80:83], v[236:239]\n"
    "v_exp_f32 v79, v79\n"
    "v_mfma_f32_16x16x32_bf16 v[240:243], a[224:227], a[228:231], v[240:243]\n"
    "buffer_load_dwordx4 v17, s[48:51], s61 offen offset:3072 lds\n"
    "v_mfma_f32_16x16x32_bf16 v[244:247], a[224:227], a[232:235], v[244:247]\n"
    "v_cvt_pk_bf16_f32 v58, v76, v77\n"
    "v_cvt_pk_bf16_f32 v59, v78, v79\n"
    "v_mfma_f32_16x16x32_bf16 v[248:251], a[224:227], a[236:239], v[248:251]\n"
    "ds_read_b64_tr_b16 a[204:205], v6 offset:8192\n"
    "ds_read_b64_tr_b16 a[206:207], v6 offset:10240\n"
    "v_mfma_f32_16x16x32_bf16 v[252:255], a[224:227], a[240:243], v[252:255]\n"
    "ds_read_b64_tr_b16 a[208:209], v7 offset:8192\n"
    "ds_read_b64_tr_b16 a[210:211], v7 offset:10240\n"
    "v_mfma_f32_16x16x32_bf16 a[160:163], a[244:247], v[40:43], a[160:163]\n"
    "ds_write_b128 v8, v[40:43] offset:0\n"
    "v_mfma_f32_16x16x32_bf16 a[164:167], a[244:247], v[44:47], a[164:167]\n"
    "ds_read_b64_tr_b16 a[212:213], v4 offset:12288\n"
    "ds_read_b64_tr_b16 a[214:215], v4 offset:14336\n"
    "v_mfma_f32_16x16x32_bf16 a[168:171], a[244:247], v[48:51], a[168:171]\n"
    "ds_read_b64_tr_b16 a[216:217], v5 offset:12288\n"
    "ds_read_b64_tr_b16 a[218:219], v5 offset:14336\n"
    "v_mfma_f32_16x16x32_bf16 a[172:175], a[244:247], v[52:55], a[172:175]\n"
    "ds_write_b128 v8, v[44:47] offset:1024\n"
    "v_mfma_f32_16x16x32_bf16 a[176:179], a[244:247], v[56:59], a[176:179]\n"
    "ds_read_b64_tr_b16 a[220:221], v6 offset:12288\n"
    "ds_read_b64_tr_b16 a[222:223], v6 offset:14336\n"
    "s_waitcnt lgkmcnt(12)\n"
    "v_mfma_f32_16x16x32_bf16 v[96:99], a[196:199], v[40:43], v[96:99]\n"
    "ds_read_b64_tr_b16 a[224:225], v7 offset:12288\n"
    "ds_read_b64_tr_b16 a[226:227], v7 offset:14336\n"
    "v_mfma_f32_16x16x32_bf16 v[100:103], a[196:199], v[44:47], v[100:103]\n"
    "ds_write_b128 v8, v[48:51] offset:2048\n"
    "s_add_u32 m0, s70, 20480\n"
    "s_add_u32 s61, s96, 258048\n"
    "v_mfma_f32_16x16x32_bf16 v[104:107], a[196:199], v[48:51], v[104:107]\n"
    "buffer_load_dwordx4 v14, s[48:51], s61 offen lds\n"
    "v_mfma_f32_16x16x32_bf16 v[108:111], a[196:199], v[52:55], v[108:111]\n"
    "v_mfma_f32_16x16x32_bf16 v[112:115], a[196:199], v[56:59], v[112:115]\n"
    "ds_write_b128 v8, v[52:55] offset:3072\n"
    "v_mfma_f32_16x16x32_bf16 v[116:119], a[200:203], v[40:43], v[116:119]\n"
    "v_accvgpr_read_b32 v61, a160\n"
    "v_sub_f32 v60, 0, v20\n"
    "v_mfma_f32_16x16x32_bf16 v[120:123], a[200:203], v[44:47], v[120:123]\n"
    "ds_write_b64 v19, v[60:61] offset:1536\n"
    "v_accvgpr_read_b32 v63, a164\n"
    "v_mfma_f32_16x16x32_bf16 v[124:127], a[200:203], v[48:51], v[124:127]\n"
    "ds_write_b128 v8, v[56:59] offset:4096\n"
    "v_mfma_f32_16x16x32_bf16 v[128:131], a[200:203], v[52:55], v[128:131]\n"
    "v_sub_f32 v62, 0, v24\n"
    "ds_write_b64 v19, v[62:63] offset:1664\n"
    "v_mfma_f32_16x16x32_bf16 v[132:135], a[200:203], v[56:59], v[132:135]\n"
    "v_accvgpr_read_b32 v65, a168\n"
    "v_sub_f32 v64, 0, v28\n"
    "s_waitcnt lgkmcnt(4)\n"
    "v_mfma_f32_16x16x32_bf16 v[136:139], a[204:207], v[40:43], v[136:139]\n"
    "buffer_load_dwordx4 v15, s[48:51], s61 offen offset:1024 lds\n"
    "v_mfma_f32_16x16x32_bf16 v[140:143], a[204:207], v[44:47], v[140:143]\n"
    "ds_write_b64 v19, v[64:65] offset:1792\n"
    "v_accvgpr_read_b32 v67, a172\n"
    "v_mfma_f32_16x16x32_bf16 v[144:147], a[204:207], v[48:51], v[144:147]\n"
    "v_sub_f32 v66, 0, v32\n"
    "ds_write_b64 v19, v[66:67] offset:1920\n"
    "v_accvgpr_read_b32 v69, a176\n"
    "v_sub_f32 v68, 0, v36\n"
    "v_mfma_f32_16x16x32_bf16 v[148:151], a[204:207], v[52:55], v[148:151]\n"
    "ds_write_b64 v19, v[68:69] offset:2048\n"
    "v_mfma_f32_16x16x32_bf16 v[152:155], a[204:207], v[56:59], v[152:155]\n"
    "v_mfma_f32_16x16x32_bf16 v[156:159], a[208:211], v[40:43], v[156:159]\n"
    "s_waitcnt vmcnt(11) lgkmcnt(0)\n"
    "s_barrier\n"
    "v_mfma_f32_16x16x32_bf16 v[160:163], a[208:211], v[44:47], v[160:163]\n"
    "ds_read_b64 v[70:71], v18 offset:1536\n"
    "ds_read_b64 v[72:73], v18 offset:1664\n"
    "v_mfma_f32_16x16x32_bf16 v[164:167], a[208:211], v[48:51], v[164:167]\n"
    "ds_read_b64 v[74:75], v18 offset:1792\n"
    "ds_read_b64 v[76:77], v18 offset:1920\n"
    "v_mfma_f32_16x16x32_bf16 v[168:171], a[208:211], v[52:55], v[168:171]\n"
    "ds_read_b64 v[78:79], v18 offset:2048\n"
    "ds_read_b32 v91, v13 offset:1280\n"
    "v_mfma_f32_16x16x32_bf16 v[172:175], a[208:211], v[56:59], v[172:175]\n"
    "ds_read_b128 v[80:83], v9 offset:0\n"
    "ds_read_b128 a[228:231], v9 offset:1024\n"
    "v_mfma_f32_16x16x32_bf16 v[176:179], a[212:215], v[40:43], v[176:179]\n"
    "ds_read_b128 a[232:235], v9 offset:2048\n"
    "ds_read_b128 a[236:239], v9 offset:3072\n"
    "v_mfma_f32_16x16x32_bf16 v[180:183], a[212:215], v[44:47], v[180:183]\n"
    "ds_read_b128 a[240:243], v9 offset:4096\n"
    "ds_read_b64_tr_b16 a[0:1], v4 offset:40960\n"
    "v_mfma_f32_16x16x32_bf16 v[184:187], a[212:215], v[48:51], v[184:187]\n"
    "ds_read_b64_tr_b16 a[2:3], v4 offset:43008\n"
    "ds_read_b64_tr_b16 a[4:5], v5 offset:40960\n"
    "v_mfma_f32_16x16x32_bf16 v[188:191], a[212:215], v[52:55], v[188:191]\n"
    "ds_read_b64_tr_b16 a[6:7], v5 offset:43008\n"
    "ds_read_b64_tr_b16 a[8:9], v6 offset:40960\n"
    "v_mfma_f32_16x16x32_bf16 v[192:195], a[212:215], v[56:59], v[192:195]\n"
    "ds_read_b64_tr_b16 a[10:11], v6 offset:43008\n"
    "ds_read_b64_tr_b16 a[12:13], v7 offset:40960\n"
    "v_mfma_f32_16x16x32_bf16 v[196:199], a[216:219], v[40:43], v[196:199]\n"
    "buffer_load_dwordx4 v16, s[48:51], s61 offen offset:2048 lds\n"
    "v_mfma_f32_16x16x32_bf16 v[200:203], a[216:219], v[44:47], v[200:203]\n"
    "ds_read_b64_tr_b16 a[14:15], v7 offset:43008\n"
    "ds_read_b64_tr_b16 a[16:17], v4 offset:45056\n"
    "v_mfma_f32_16x16x32_bf16 v[204:207], a[216:219], v[48:51], v[204:207]\n"
    "ds_read_b64_tr_b16 a[18:19], v4 offset:47104\n"
    "ds_read_b64_tr_b16 a[20:21], v5 offset:45056\n"
    "v_mfma_f32_16x16x32_bf16 v[208:211], a[216:219], v[52:55], v[208:211]\n"
    "ds_read_b64_tr_b16 a[22:23], v5 offset:47104\n"
    "ds_read_b64_tr_b16 a[24:25], v6 offset:45056\n"
    "v_mfma_f32_16x16x32_bf16 v[212:215], a[216:219], v[56:59], v[212:215]\n"
    "ds_read_b64_tr_b16 a[26:27], v6 offset:47104\n"
    "ds_read_b64_tr_b16 a[28:29], v7 offset:45056\n"
    "v_mfma_f32_16x16x32_bf16 v[216:219], a[220:223], v[40:43], v[216:219]\n"
    "ds_read_b64_tr_b16 a[30:31], v7 offset:47104\n"
    "v_and_b32 v8, 15, v0\n"
    "v_mfma_f32_16x16x32_bf16 v[220:223], a[220:223], v[44:47], v[220:223]\n"
    "v_lshlrev_b32 v8, 3, v8\n"
    "s_waitcnt vmcnt(6)\n"
    "v_mov_b32 v6, v95\n"
    "v_mfma_f32_16x16x32_bf16 v[224:227], a[220:223], v[48:51], v[224:227]\n"
    "s_lshl_b32 s67, s22, 11\n"
    "s_xor_b32 s66, s67, 4096\n"
    "v_mfma_f32_16x16x32_bf16 v[228:231], a[220:223], v[52:55], v[228:231]\n"
    "s_mul_i32 s79, s64, 640\n"
    "s_add_u32 s66, s66, s79\n"
    "v_mfma_f32_16x16x32_bf16 v[232:235], a[220:223], v[56:59], v[232:235]\n"
    "v_lshlrev_b32 v1, 4, v0\n"
    "s_add_u32 s82, %3, 65536\n"
    "v_mfma_f32_16x16x32_bf16 v[236:239], a[224:227], v[40:43], v[236:239]\n"
    "buffer_load_dwordx4 v17, s[48:51], s61 offen offset:3072 lds\n"
    "v_mfma_f32_16x16x32_bf16 v[240:243], a[224:227], v[44:47], v[240:243]\n"
    "v_add_u32 v10, s82, v1\n"
    "s_mul_i32 s82, %2, 20480\n"
    "v_mfma_f32_16x16x32_bf16 v[244:247], a[224:227], v[48:51], v[244:247]\n"
    "v_add_u32 v5, s82, v10\n"
    "s_waitcnt lgkmcnt(0)\n"
    "s_barrier\n"
    "v_mfma_f32_16x16x32_bf16 v[248:251], a[224:227], v[52:55], v[248:251]\n"
    "v_cmp_lt_f32 vcc, s73, v91\n"
    "v_mfma_f32_16x16x32_bf16 v[252:255], a[224:227], v[56:59], v[252:255]\n"
    "s_cbranch_vccnz RD_d_%=\n"
    "RDR_d_%=:\n"
    "v_max_f32 v94, v60, v70\n"
    "v_sub_f32 v60, v60, v94\n"
    "v_mfma_f32_16x16x32_bf16 v[96:99], a[0:3], v[80:83], v[96:99]\n"
    "v_sub_f32 v70, v70, v94\n"
    "v_mfma_f32_16x16x32_bf16 v[100:103], a[0:3], a[228:231], v[100:103]\n"
    "v_exp_f32 v60, v60\n"
    "v_mfma_f32_16x16x32_bf16 v[104:107], a[0:3], a[232:235], v[104:107]\n"
    "v_exp_f32 v70, v70\n"
    "v_mfma_f32_16x16x32_bf16 v[108:111], a[0:3], a[236:239], v[108:111]\n"
    "v_mul_f32 v61, v61, v60\n"
    "v_fma_f32 v61, v71, v70, v61\n"
    "v_mfma_f32_16x16x32_bf16 v[112:115], a[0:3], a[240:243], v[112:115]\n"
    "v_log_f32 v70, v61\n"
    "v_mfma_f32_16x16x32_bf16 v[116:119], a[4:7], v[80:83], v[116:119]\n"
    "v_mov_b32 v71, v6\n"
    "v_add_f32 v70, v70, v94\n"
    "v_mfma_f32_16x16x32_bf16 v[120:123], a[4:7], a[228:231], v[120:123]\n"
    "v_mov_b32 v60, v70\n"
    "v_mfma_f32_16x16x32_bf16 v[124:127], a[4:7], a[232:235], v[124:127]\n"
    "buffer_store_dwordx2 v[70:71], v8, s[44:47], s66 offen offset:0 sc1\n"
    "v_mfma_f32_16x16x32_bf16 v[128:131], a[4:7], a[236:239], v[128:131]\n"
    "ds_write_b64 v19, v[60:61] offset:2176\n"
    "v_max_f32 v94, v62, v72\n"
    "v_mfma_f32_16x16x32_bf16 v[132:135], a[4:7], a[240:243], v[132:135]\n"
    "v_sub_f32 v62, v62, v94\n"
    "v_sub_f32 v72, v72, v94\n"
    "v_mfma_f32_16x16x32_bf16 v[136:139], a[8:11], v[80:83], v[136:139]\n"
    "v_exp_f32 v62, v62\n"
    "v_mfma_f32_16x16x32_bf16 v[140:143], a[8:11], a[228:231], v[140:143]\n"
    "v_exp_f32 v72, v72\n"
    "v_mfma_f32_16x16x32_bf16 v[144:147], a[8:11], a[232:235], v[144:147]\n"
    "v_mul_f32 v63, v63, v62\n"
    "v_fma_f32 v63, v73, v72, v63\n"
    "v_mfma_f32_16x16x32_bf16 v[148:151], a[8:11], a[236:239], v[148:151]\n"
    "v_log_f32 v72, v63\n"
    "v_mfma_f32_16x16x32_bf16 v[152:155], a[8:11], a[240:243], v[152:155]\n"
    "v_mov_b32 v73, v6\n"
    "v_add_f32 v72, v72, v94\n"
    "v_mfma_f32_16x16x32_bf16 v[156:159], a[12:15], v[80:83], v[156:159]\n"
    "v_mov_b32 v62, v72\n"
    "v_mfma_f32_16x16x32_bf16 v[160:163], a[12:15], a[228:231], v[160:163]\n"
    "buffer_store_dwordx2 v[72:73], v8, s[44:47], s66 offen offset:128 sc1\n"
    "v_mfma_f32_16x16x32_bf16 v[164:167], a[12:15], a[232:235], v[164:167]\n"
    "ds_write_b64 v19, v[62:63] offset:2304\n"
    "v_max_f32 v94, v64, v74\n"
    "v_mfma_f32_16x16x32_bf16 v[168:171], a[12:15], a[236:239], v[168:171]\n"
    "v_sub_f32 v64, v64, v94\n"
    "v_sub_f32 v74, v74, v94\n"
    "v_mfma_f32_16x16x32_bf16 v[172:175], a[12:15], a[240:243], v[172:175]\n"
    "v_exp_f32 v64, v64\n"
    "v_mfma_f32_16x16x32_bf16 v[176:179], a[16:19], v[80:83], v[176:179]\n"
    "v_exp_f32 v74, v74\n"
    "v_mfma_f32_16x16x32_bf16 v[180:183], a[16:19], a[228:231], v[180:183]\n"
    "v_mul_f32 v65, v65, v64\n"
    "v_fma_f32 v65, v75, v74, v65\n"
    "v_mfma_f32_16x16x32_bf16 v[184:187], a[16:19], a[232:235], v[184:187]\n"
    "v_log_f32 v74, v65\n"
    "v_mov_b32 v75, v6\n"
    "v_add_f32 v74, v74, v94\n"
    "v_mov_b32 v64, v74\n"
    "buffer_store_dwordx2 v[74:75], v8, s[44:47], s66 offen offset:256 sc1\n"
    "ds_write_b64 v19, v[64:65] offset:2432\n"
    "v_mfma_f32_16x16x32_bf16 v[188:191], a[16:19], a[236:239], v[188:191]\n"
    "v_max_f32 v94, v66, v76\n"
    "v_sub_f32 v66, v66, v94\n"
    "v_mfma_f32_16x16x32_bf16 v[192:195], a[16:19], a[240:243], v[192:195]\n"
    "v_sub_f32 v76, v76, v94\n"
    "v_mfma_f32_16x16x32_bf16 v[196:199], a[20:23], v[80:83], v[196:199]\n"
    "v_exp_f32 v66, v66\n"
    "v_mfma_f32_16x16x32_bf16 v[200:203], a[20:23], a[228:231], v[200:203]\n"
    "v_exp_f32 v76, v76\n"
    "v_mfma_f32_16x16x32_bf16 v[204:207], a[20:23], a[232:235], v[204:207]\n"
    "v_mul_f32 v67, v67, v66\n"
    "v_fma_f32 v67, v77, v76, v67\n"
    "v_log_f32 v76, v67\n"
    "v_mov_b32 v77, v6\n"
    "v_add_f32 v76, v76, v94\n"
    "v_mov_b32 v66, v76\n"
    "buffer_store_dwordx2 v[76:77], v8, s[44:47], s66 offen offset:384 sc1\n"
    "ds_write_b64 v19, v[66:67] offset:2560\n"
    "v_mfma_f32_16x16x32_bf16 v[208:211], a[20:23], a[236:239], v[208:211]\n"
    "v_max_f32 v94, v68, v78\n"
    "v_sub_f32 v68, v68, v94\n"
    "v_mfma_f32_16x16x32_bf16 v[212:215], a[20:23], a[240:243], v[212:215]\n"
    "v_sub_f32 v78, v78, v94\n"
    "v_mfma_f32_16x16x32_bf16 v[216:219], a[24:27], v[80:83], v[216:219]\n"
    "v_exp_f32 v68, v68\n"
    "v_mfma_f32_16x16x32_bf16 v[220:223], a[24:27], a[228:231], v[220:223]\n"
    "v_exp_f32 v78, v78\n"
    "v_mfma_f32_16x16x32_bf16 v[224:227], a[24:27], a[232:235], v[224:227]\n"
    "v_mul_f32 v69, v69, v68\n"
    "v_fma_f32 v69, v79, v78, v69\n"
    "v_log_f32 v78, v69\n"
    "v_mov_b32 v79, v6\n"
    "v_add_f32 v78, v78, v94\n"
    "v_mov_b32 v68, v78\n"
    "buffer_store_dwordx2 v[78:79], v8, s[44:47], s66 offen offset:512 sc1\n"
    "ds_write_b64 v19, v[68:69] offset:2688\n"
    "v_mfma_f32_16x16x32_bf16 v[228:231], a[24:27], a[236:239], v[228:231]\n"
    "s_lshl_b32 s79, %2, 5\n"
    "v_xor_b32 v3, s79, v4\n"
    "v_mfma_f32_16x16x32_bf16 v[232:235], a[24:27], a[240:243], v[232:235]\n"
    "s_lshl_b32 s79, s65, 14\n"
    "v_subrev_u32 v3, s79, v3\n"
    "v_mfma_f32_16x16x32_bf16 v[236:239], a[28:31], v[80:83], v[236:239]\n"
    "v_subrev_u32 v2, 0x10000, v3\n"
    "v_lshlrev_b32 v4, 3, v0\n"
    "v_mfma_f32_16x16x32_bf16 v[240:243], a[28:31], a[228:231], v[240:243]\n"
    "v_add_u32 v9, 0xa000, v10\n"
    "s_mul_i32 s83, s57, 73728\n"
    "v_mfma_f32_16x16x32_bf16 v[244:247], a[28:31], a[232:235], v[244:247]\n"
    "s_lshl_b32 s81, %2, 11\n"
    "s_add_u32 s81, s81, s83\n"
    "v_mfma_f32_16x16x32_bf16 v[248:251], a[28:31], a[236:239], v[248:251]\n"
    "s_lshl_b32 s84, %2, 10\n"
    "s_add_u32 s84, s84, s83\n"
    "v_mfma_f32_16x16x32_bf16 v[252:255], a[28:31], a[240:243], v[252:255]\n"
    "s_add_u32 s84, s84, 16384\n"
    "v_cvt_pk_bf16_f32 v96, v96, v97\n"
    "v_cvt_pk_bf16_f32 v97, v98, v99\n"
    "v_cvt_pk_bf16_f32 v98, v116, v117\n"
    "v_cvt_pk_bf16_f32 v99, v118, v119\n"
    "ds_write_b128 v5, v[96:99] offset:0\n"
    "v_cvt_pk_bf16_f32 v100, v100, v101\n"
    "v_cvt_pk_bf16_f32 v101, v102, v103\n"
    "v_cvt_pk_bf16_f32 v102, v120, v121\n"
    "v_cvt_pk_bf16_f32 v103, v122, v123\n"
    "ds_write_b128 v5, v[100:103] offset:1024\n"
    "v_cvt_pk_bf16_f32 v104, v104, v105\n"
    "v_cvt_pk_bf16_f32 v105, v106, v107\n"
    "v_cvt_pk_bf16_f32 v106, v124, v125\n"
    "v_cvt_pk_bf16_f32 v107, v126, v127\n"
    "ds_write_b128 v5, v[104:107] offset:2048\n"
    "v_cvt_pk_bf16_f32 v108, v108, v109\n"
    "v_cvt_pk_bf16_f32 v109, v110, v111\n"
    "v_cvt_pk_bf16_f32 v110, v128, v129\n"
    "v_cvt_pk_bf16_f32 v111, v130, v131\n"
    "ds_write_b128 v5, v[108:111] offset:3072\n"
    "v_cvt_pk_bf16_f32 v112, v112, v113\n"
    "v_cvt_pk_bf16_f32 v113, v114, v115\n"
    "v_cvt_pk_bf16_f32 v114, v132, v133\n"
    "v_cvt_pk_bf16_f32 v115, v134, v135\n"
    "ds_write_b128 v5, v[112:115] offset:4096\n"
    "v_cvt_pk_bf16_f32 v136, v136, v137\n"
    "v_cvt_pk_bf16_f32 v137, v138, v139\n"
    "v_cvt_pk_bf16_f32 v138, v156, v157\n"
    "v_cvt_pk_bf16_f32 v139, v158, v159\n"
    "ds_write_b128 v5, v[136:139] offset:5120\n"
    "v_cvt_pk_bf16_f32 v140, v140, v141\n"
    "v_cvt_pk_bf16_f32 v141, v142, v143\n"
    "v_cvt_pk_bf16_f32 v142, v160, v161\n"
    "v_cvt_pk_bf16_f32 v143, v162, v163\n"
    "ds_write_b128 v5, v[140:143] offset:6144\n"
    "v_cvt_pk_bf16_f32 v144, v144, v145\n"
    "v_cvt_pk_bf16_f32 v145, v146, v147\n"
    "v_cvt_pk_bf16_f32 v146, v164, v165\n"
    "v_cvt_pk_bf16_f32 v147, v166, v167\n"
    "ds_write_b128 v5, v[144:147] offset:7168\n"
    "v_cvt_pk_bf16_f32 v148, v148, v149\n"
    "v_cvt_pk_bf16_f32 v149, v150, v151\n"
    "v_cvt_pk_bf16_f32 v150, v168, v169\n"
    "v_cvt_pk_bf16_f32 v151, v170, v171\n"
    "ds_write_b128 v5, v[148:151] offset:8192\n"
    "v_cvt_pk_bf16_f32 v152, v152, v153\n"
    "v_cvt_pk_bf16_f32 v153, v154, v155\n"
    "v_cvt_pk_bf16_f32 v154, v172, v173\n"
    "v_cvt_pk_bf16_f32 v155, v174, v175\n"
    "ds_write_b128 v5, v[152:155] offset:9216\n"
    "v_cvt_pk_bf16_f32 v176, v176, v177\n"
    "v_cvt_pk_bf16_f32 v177, v178, v179\n"
    "v_cvt_pk_bf16_f32 v178, v196, v197\n"
    "v_cvt_pk_bf16_f32 v179, v198, v199\n"
    "ds_write_b128 v5, v[176:179] offset:10240\n"
    "v_cvt_pk_bf16_f32 v180, v180, v181\n"
    "v_cvt_pk_bf16_f32 v181, v182, v183\n"
    "v_cvt_pk_bf16_f32 v182, v200, v201\n"
    "v_cvt_pk_bf16_f32 v183, v202, v203\n"
    "ds_write_b128 v5, v[180:183] offset:11264\n"
    "v_cvt_pk_bf16_f32 v184, v184, v185\n"
    "v_cvt_pk_bf16_f32 v185, v186, v187\n"
    "v_cvt_pk_bf16_f32 v186, v204, v205\n"
    "v_cvt_pk_bf16_f32 v187, v206, v207\n"
    "ds_write_b128 v5, v[184:187] offset:12288\n"
    "v_cvt_pk_bf16_f32 v188, v188, v189\n"
    "v_cvt_pk_bf16_f32 v189, v190, v191\n"
    "v_cvt_pk_bf16_f32 v190, v208, v209\n"
    "v_cvt_pk_bf16_f32 v191, v210, v211\n"
    "ds_write_b128 v5, v[188:191] offset:13312\n"
    "v_cvt_pk_bf16_f32 v192, v192, v193\n"
    "v_cvt_pk_bf16_f32 v193, v194, v195\n"
    "v_cvt_pk_bf16_f32 v194, v212, v213\n"
    "v_cvt_pk_bf16_f32 v195, v214, v215\n"
    "ds_write_b128 v5, v[192:195] offset:14336\n"
    "v_cvt_pk_bf16_f32 v216, v216, v217\n"
    "v_cvt_pk_bf16_f32 v217, v218, v219\n"
    "v_cvt_pk_bf16_f32 v218, v236, v237\n"
    "v_cvt_pk_bf16_f32 v219, v238, v239\n"
    "ds_write_b128 v5, v[216:219] offset:15360\n"
    "v_cvt_pk_bf16_f32 v220, v220, v221\n"
    "v_cvt_pk_bf16_f32 v221, v222, v223\n"
    "v_cvt_pk_bf16_f32 v222, v240, v241\n"
    "v_cvt_pk_bf16_f32 v223, v242, v243\n"
    "ds_write_b128 v5, v[220:223] offset:16384\n"
    "v_cvt_pk_bf16_f32 v224, v224, v225\n"
    "v_cvt_pk_bf16_f32 v225, v226, v227\n"
    "v_cvt_pk_bf16_f32 v226, v244, v245\n"
    "v_cvt_pk_bf16_f32 v227, v246, v247\n"
    "ds_write_b128 v5, v[224:227] offset:17408\n"
    "v_cvt_pk_bf16_f32 v228, v228, v229\n"
    "v_cvt_pk_bf16_f32 v229, v230, v231\n"
    "v_cvt_pk_bf16_f32 v230, v248, v249\n"
    "v_cvt_pk_bf16_f32 v231, v250, v251\n"
    "ds_write_b128 v5, v[228:231] offset:18432\n"
    "v_cvt_pk_bf16_f32 v232, v232, v233\n"
    "v_cvt_pk_bf16_f32 v233, v234, v235\n"
    "v_cvt_pk_bf16_f32 v234, v252, v253\n"
    "v_cvt_pk_bf16_f32 v235, v254, v255\n"
    "ds_write_b128 v5, v[232:235] offset:19456\n"
    "s_waitcnt lgkmcnt(0)\n"
    "s_barrier\n"
    "ds_read_b64_tr_b16 a[240:241], v2 offset:0\n"
    "ds_read_b64_tr_b16 a[242:243], v2 offset:2048\n"
    "ds_read_b64_tr_b16 a[244:245], v2 offset:4096\n"
    "ds_read_b64_tr_b16 a[246:247], v2 offset:6144\n"
    "ds_read_b128 a[0:3], v10 offset:0\n"
    "ds_read_b128 a[20:23], v10 offset:5120\n"
    "ds_read_b128 a[4:7], v10 offset:1024\n"
    "ds_read_b128 a[24:27], v10 offset:6144\n"
    "ds_read_b128 a[8:11], v10 offset:2048\n"
    "ds_read_b128 a[28:31], v10 offset:7168\n"
    "ds_read_b128 a[12:15], v10 offset:3072\n"
    "ds_read_b128 a[32:35], v10 offset:8192\n"
    "s_waitcnt lgkmcnt(7)\n"
    "v_mfma_f32_16x16x32_bf16 v[116:119], a[240:243], a[0:3], 0\n"
    "ds_read_b128 a[16:19], v10 offset:4096\n"
    "ds_read_b128 a[36:39], v10 offset:9216\n"
    "s_waitcnt lgkmcnt(8)\n"
    "v_mfma_f32_16x16x32_bf16 v[116:119], a[244:247], a[20:23], v[116:119]\n"
    "ds_read_b64_tr_b16 a[248:249], v2 offset:8192\n"
    "ds_read_b64_tr_b16 a[250:251], v2 offset:10240\n"
    "s_waitcnt lgkmcnt(9)\n"
    "v_mfma_f32_16x16x32_bf16 v[120:123], a[240:243], a[4:7], 0\n"
    "ds_read_b128 a[40:43], v10 offset:10240\n"
    "ds_read_b64_tr_b16 a[252:253], v2 offset:12288\n"
    "s_waitcnt lgkmcnt(10)\n"
    "v_mfma_f32_16x16x32_bf16 v[120:123], a[244:247], a[24:27], v[120:123]\n"
    "ds_read_b64_tr_b16 a[254:255], v2 offset:14336\n"
    "ds_read_b128 a[60:63], v10 offset:15360\n"
    "s_waitcnt lgkmcnt(11)\n"
    "v_mfma_f32_16x16x32_bf16 v[124:127], a[240:243], a[8:11], 0\n"
    "ds_read_b128 a[44:47], v10 offset:11264\n"
    "ds_read_b128 a[64:67], v10 offset:16384\n"
    "s_waitcnt lgkmcnt(12)\n"
    "v_mfma_f32_16x16x32_bf16 v[124:127], a[244:247], a[28:31], v[124:127]\n"
    "ds_read_b128 a[48:51], v10 offset:12288\n"
    "ds_read_b128 a[68:71], v10 offset:17408\n"
    "s_waitcnt lgkmcnt(13)\n"
    "v_mfma_f32_16x16x32_bf16 v[128:131], a[240:243], a[12:15], 0\n"
    "ds_read_b128 a[52:55], v10 offset:13312\n"
    "ds_read_b128 a[72:75], v10 offset:18432\n"
    "s_waitcnt lgkmcnt(14)\n"
    "v_mfma_f32_16x16x32_bf16 v[128:131], a[244:247], a[32:35], v[128:131]\n"
    "ds_read_b128 a[56:59], v10 offset:14336\n"
    "ds_read_b128 a[76:79], v10 offset:19456\n"
    "s_waitcnt lgkmcnt(14)\n"
    "v_mfma_f32_16x16x32_bf16 v[132:135], a[240:243], a[16:19], 0\n"
    "ds_read_b64_tr_b16 a[240:241], v2 offset:32768\n"
    "ds_read_b64_tr_b16 a[242:243], v2 offset:34816\n"
    "v_mfma_f32_16x16x32_bf16 v[132:135], a[244:247], a[36:39], v[132:135]\n"
    "ds_read_b128 a[80:83], v10 offset:20480\n"
    "ds_read_b64_tr_b16 a[244:245], v2 offset:36864\n"
    "s_waitcnt lgkmcnt(14)\n"
    "v_mfma_f32_16x16x32_bf16 v[116:119], a[248:251], a[40:43], v[116:119]\n"
    "ds_read_b64_tr_b16 a[246:247], v2 offset:38912\n"
    "ds_read_b128 a[100:103], v10 offset:25600\n"
    "s_waitcnt lgkmcnt(14)\n"
    "v_mfma_f32_16x16x32_bf16 v[116:119], a[252:255], a[60:63], v[116:119]\n"
    "ds_read_b128 a[84:87], v10 offset:21504\n"
    "ds_read_b128 a[104:107], v10 offset:26624\n"
    "s_waitcnt lgkmcnt(14)\n"
    "v_mfma_f32_16x16x32_bf16 v[120:123], a[248:251], a[44:47], v[120:123]\n"
    "ds_read_b128 a[88:91], v10 offset:22528\n"
    "ds_read_b128 a[108:111], v10 offset:27648\n"
    "v_mfma_f32_16x16x32_bf16 v[120:123], a[252:255], a[64:67], v[120:123]\n"
    "ds_read_b128 a[92:95], v10 offset:23552\n"
    "ds_read_b128 a[112:115], v10 offset:28672\n"
    "s_waitcnt lgkmcnt(15)\n"
    "v_mfma_f32_16x16x32_bf16 v[124:127], a[248:251], a[48:51], v[124:127]\n"
    "ds_read_b128 a[96:99], v10 offset:24576\n"
    "v_mfma_f32_16x16x32_bf16 v[124:127], a[252:255], a[68:71], v[124:127]\n"
    "ds_read_b128 a[116:119], v10 offset:29696\n"
    "v_mfma_f32_16x16x32_bf16 v[128:131], a[248:251], a[52:55], v[128:131]\n"
    "ds_read_b128 a[120:123], v10 offset:30720\n"
    "s_waitcnt lgkmcnt(13)\n"
    "v_mfma_f32_16x16x32_bf16 v[128:131], a[252:255], a[72:75], v[128:131]\n"
    "ds_read_b128 a[140:143], v10 offset:35840\n"
    "v_mfma_f32_16x16x32_bf16 v[132:135], a[248:251], a[56:59], v[132:135]\n"
    "ds_read_b64_tr_b16 a[248:249], v2 offset:40960\n"
    "ds_read_b64_tr_b16 a[250:251], v2 offset:43008\n"
    "v_mfma_f32_16x16x32_bf16 v[132:135], a[252:255], a[76:79], v[132:135]\n"
    "ds_read_b64_tr_b16 a[252:253], v2 offset:45056\n"
    "ds_read_b64_tr_b16 a[254:255], v2 offset:47104\n"
    "s_waitcnt lgkmcnt(12)\n"
    "v_mfma_f32_16x16x32_bf16 v[116:119], a[240:243], a[80:83], v[116:119]\n"
    "ds_read_b128 a[124:127], v10 offset:31744\n"
    "ds_read_b128 a[144:147], v10 offset:36864\n"
    "v_mfma_f32_16x16x32_bf16 v[116:119], a[244:247], a[100:103], v[116:119]\n"
    "ds_read_b128 a[128:131], v10 offset:32768\n"
    "ds_read_b128 a[148:151], v10 offset:37888\n"
    "v_mfma_f32_16x16x32_bf16 v[120:123], a[240:243], a[84:87], v[120:123]\n"
    "ds_read_b128 a[132:135], v10 offset:33792\n"
    "v_mfma_f32_16x16x32_bf16 v[120:123], a[244:247], a[104:107], v[120:123]\n"
    "ds_read_b128 a[152:155], v10 offset:38912\n"
    "s_waitcnt lgkmcnt(13)\n"
    "v_mfma_f32_16x16x32_bf16 v[124:127], a[240:243], a[88:91], v[124:127]\n"
    "ds_read_b128 a[136:139], v10 offset:34816\n"
    "v_mfma_f32_16x16x32_bf16 v[124:127], a[244:247], a[108:111], v[124:127]\n"
    "ds_read_b128 a[156:159], v10 offset:39936\n"
    "v_mfma_f32_16x16x32_bf16 v[128:131], a[240:243], a[92:95], v[128:131]\n"
    "ds_read_b128 a[160:163], v9 offset:0\n"
    "v_mfma_f32_16x16x32_bf16 v[128:131], a[244:247], a[112:115], v[128:131]\n"
    "ds_read_b128 a[180:183], v9 offset:5120\n"
    "v_mfma_f32_16x16x32_bf16 v[132:135], a[240:243], a[96:99], v[132:135]\n"
    "ds_read_b128 a[164:167], v9 offset:1024\n"
    "s_waitcnt lgkmcnt(11)\n"
    "v_mfma_f32_16x16x32_bf16 v[132:135], a[244:247], a[116:119], v[132:135]\n"
    "ds_read_b128 a[184:187], v9 offset:6144\n"
    "v_mfma_f32_16x16x32_bf16 v[116:119], a[248:251], a[120:123], v[116:119]\n"
    "ds_read_b128 a[168:171], v9 offset:2048\n"
    "v_mfma_f32_16x16x32_bf16 v[116:119], a[252:255], a[140:143], v[116:119]\n"
    "ds_read_b128 a[188:191], v9 offset:7168\n"
    "s_waitcnt lgkmcnt(9)\n"
    "v_mfma_f32_16x16x32_bf16 v[120:123], a[248:251], a[124:127], v[120:123]\n"
    "ds_read_b128 a[172:175], v9 offset:3072\n"
    "v_mfma_f32_16x16x32_bf16 v[120:123], a[252:255], a[144:147], v[120:123]\n"
    "s_waitcnt vmcnt(5) lgkmcnt(0)\n"
    "s_barrier\n"
    "ds_read_b64_tr_b16 a[240:241], v2 offset:16384\n"
    "ds_read_b64_tr_b16 a[242:243], v2 offset:18432\n"
    "ds_read_b64_tr_b16 a[244:245], v2 offset:20480\n"
    "ds_read_b64_tr_b16 a[246:247], v2 offset:22528\n"
    "v_mfma_f32_16x16x32_bf16 v[124:127], a[248:251], a[128:131], v[124:127]\n"
    "s_add_u32 m0, s70, 0\n"
    "s_add_u32 s61, s96, 128\n"
    "v_mfma_f32_16x16x32_bf16 v[124:127], a[252:255], a[148:151], v[124:127]\n"
    "buffer_load_dwordx4 v14, s[48:51], s61 offen lds\n"
    "v_mfma_f32_16x16x32_bf16 v[128:131], a[248:251], a[132:135], v[128:131]\n"
    "ds_read_b128 a[192:195], v9 offset:8192\n"
    "ds_read_b128 a[176:179], v9 offset:4096\n"
    "v_mfma_f32_16x16x32_bf16 v[128:131], a[252:255], a[152:155], v[128:131]\n"
    "ds_read_b128 a[196:199], v9 offset:9216\n"
    "ds_read_b128 a[200:203], v9 offset:10240\n"
    "v_mfma_f32_16x16x32_bf16 v[132:135], a[248:251], a[136:139], v[132:135]\n"
    "buffer_load_dwordx4 v15, s[48:51], s61 offen offset:1024 lds\n"
    "v_mfma_f32_16x16x32_bf16 v[132:135], a[252:255], a[156:159], v[132:135]\n"
    "ds_read_b64_tr_b16 a[248:249], v2 offset:24576\n"
    "ds_read_b64_tr_b16 a[250:251], v2 offset:26624\n"
    "s_waitcnt lgkmcnt(8)\n"
    "v_mfma_f32_16x16x32_bf16 v[156:159], a[240:243], a[160:163], 0\n"
    "ds_read_b64_tr_b16 a[252:253], v2 offset:28672\n"
    "ds_read_b64_tr_b16 a[254:255], v2 offset:30720\n"
    "s_waitcnt lgkmcnt(8)\n"
    "v_mfma_f32_16x16x32_bf16 v[156:159], a[244:247], a[180:183], v[156:159]\n"
    "buffer_load_dwordx4 v16, s[48:51], s61 offen offset:2048 lds\n"
    "v_mfma_f32_16x16x32_bf16 v[160:163], a[240:243], a[164:167], 0\n"
    "ds_read_b128 a[220:223], v9 offset:15360\n"
    "ds_read_b128 a[204:207], v9 offset:11264\n"
    "v_mfma_f32_16x16x32_bf16 v[160:163], a[244:247], a[184:187], v[160:163]\n"
    "ds_read_b128 a[224:227], v9 offset:16384\n"
    "ds_read_b128 a[208:211], v9 offset:12288\n"
    "v_mfma_f32_16x16x32_bf16 v[164:167], a[240:243], a[168:171], 0\n"
    "buffer_load_dwordx4 v17, s[48:51], s61 offen offset:3072 lds\n"
    "v_mfma_f32_16x16x32_bf16 v[164:167], a[244:247], a[188:191], v[164:167]\n"
    "ds_read_b128 a[228:231], v9 offset:17408\n"
    "ds_read_b128 a[212:215], v9 offset:13312\n"
    "v_mfma_f32_16x16x32_bf16 v[168:171], a[240:243], a[172:175], 0\n"
    "ds_read_b128 a[232:235], v9 offset:18432\n"
    "ds_read_b128 a[216:219], v9 offset:14336\n"
    "s_waitcnt lgkmcnt(12)\n"
    "v_mfma_f32_16x16x32_bf16 v[168:171], a[244:247], a[192:195], v[168:171]\n"
    "s_add_u32 m0, s70, 4096\n"
    "s_add_u32 s61, s96, 28800\n"
    "v_mfma_f32_16x16x32_bf16 v[172:175], a[240:243], a[176:179], 0\n"
    "buffer_load_dwordx4 v14, s[48:51], s61 offen lds\n"
    "v_mfma_f32_16x16x32_bf16 v[172:175], a[244:247], a[196:199], v[172:175]\n"
    "ds_read_b128 a[236:239], v9 offset:19456\n"
    "ds_read_b64_tr_b16 a[240:241], v2 offset:49152\n"
    "s_waitcnt lgkmcnt(10)\n"
    "v_mfma_f32_16x16x32_bf16 v[156:159], a[248:251], a[200:203], v[156:159]\n"
    "ds_read_b64_tr_b16 a[242:243], v2 offset:51200\n"
    "ds_read_b128 v[96:99], v9 offset:20480\n"
    "s_waitcnt lgkmcnt(10)\n"
    "v_mfma_f32_16x16x32_bf16 v[156:159], a[252:255], a[220:223], v[156:159]\n"
    "buffer_load_dwordx4 v15, s[48:51], s61 offen offset:1024 lds\n"
    "v_mfma_f32_16x16x32_bf16 v[160:163], a[248:251], a[204:207], v[160:163]\n"
    "ds_read_b64_tr_b16 a[244:245], v2 offset:53248\n"
    "ds_read_b64_tr_b16 a[246:247], v2 offset:55296\n"
    "s_waitcnt lgkmcnt(10)\n"
    "v_mfma_f32_16x16x32_bf16 v[160:163], a[252:255], a[224:227], v[160:163]\n"
    "ds_read_b128 v[136:139], v9 offset:25600\n"
    "ds_read_b128 v[100:103], v9 offset:21504\n"
    "v_mfma_f32_16x16x32_bf16 v[164:167], a[248:251], a[208:211], v[164:167]\n"
    "buffer_load_dwordx4 v16, s[48:51], s61 offen offset:2048 lds\n"
    "s_waitcnt lgkmcnt(8)\n"
    "v_mfma_f32_16x16x32_bf16 v[164:167], a[252:255], a[228:231], v[164:167]\n"
    "ds_read_b128 v[140:143], v9 offset:26624\n"
    "ds_read_b128 v[104:107], v9 offset:22528\n"
    "v_mfma_f32_16x16x32_bf16 v[168:171], a[248:251], a[212:215], v[168:171]\n"
    "ds_read_b128 v[144:147], v9 offset:27648\n"
    "ds_read_b128 v[108:111], v9 offset:23552\n"
    "v_mfma_f32_16x16x32_bf16 v[168:171], a[252:255], a[232:235], v[168:171]\n"
    "ds_read_b128 v[148:151], v9 offset:28672\n"
    "ds_read_b128 v[112:115], v9 offset:24576\n"
    "v_mfma_f32_16x16x32_bf16 v[172:175], a[248:251], a[216:219], v[172:175]\n"
    "buffer_load_dwordx4 v17, s[48:51], s61 offen offset:3072 lds\n"
    "s_waitcnt lgkmcnt(10)\n"
    "v_mfma_f32_16x16x32_bf16 v[172:175], a[252:255], a[236:239], v[172:175]\n"
    "ds_read_b128 v[152:155], v9 offset:29696\n"
    "ds_read_b64_tr_b16 a[248:249], v2 offset:57344\n"
    "v_mfma_f32_16x16x32_bf16 v[156:159], a[240:243], v[96:99], v[156:159]\n"
    "s_add_u32 m0, s62, 0\n"
    "s_add_u32 s61, s96, 229504\n"
    "s_waitcnt lgkmcnt(9)\n"
    "v_mfma_f32_16x16x32_bf16 v[156:159], a[244:247], v[136:139], v[156:159]\n"
    "buffer_load_dwordx4 v14, s[48:51], s61 offen lds\n"
    "s_waitcnt lgkmcnt(8)\n"
    "v_mfma_f32_16x16x32_bf16 v[160:163], a[240:243], v[100:103], v[160:163]\n"
    "ds_read_b64_tr_b16 a[250:251], v2 offset:59392\n"
    "ds_read_b128 v[176:179], v9 offset:30720\n"
    "s_waitcnt lgkmcnt(9)\n"
    "v_mfma_f32_16x16x32_bf16 v[160:163], a[244:247], v[140:143], v[160:163]\n"
    "ds_read_b64_tr_b16 a[252:253], v2 offset:61440\n"
    "ds_read_b64_tr_b16 a[254:255], v2 offset:63488\n"
    "ds_read_b128 v[216:219], v9 offset:35840\n"
    "s_waitcnt lgkmcnt(10)\n"
    "v_mfma_f32_16x16x32_bf16 v[164:167], a[240:243], v[104:107], v[164:167]\n"
    "ds_read_b128 v[180:183], v9 offset:31744\n"
    "ds_read_b128 v[220:223], v9 offset:36864\n"
    "v_mfma_f32_16x16x32_bf16 v[164:167], a[244:247], v[144:147], v[164:167]\n"
    "buffer_load_dwordx4 v15, s[48:51], s61 offen offset:1024 lds\n"
    "s_waitcnt lgkmcnt(9)\n"
    "v_mfma_f32_16x16x32_bf16 v[168:171], a[240:243], v[108:111], v[168:171]\n"
    "ds_read_b128 v[184:187], v9 offset:32768\n"
    "ds_read_b128 v[224:227], v9 offset:37888\n"
    "v_mfma_f32_16x16x32_bf16 v[168:171], a[244:247], v[148:151], v[168:171]\n"
    "ds_read_b128 v[188:191], v9 offset:33792\n"
    "ds_read_b128 v[228:231], v9 offset:38912\n"
    "v_mfma_f32_16x16x32_bf16 v[172:175], a[240:243], v[112:115], v[172:175]\n"
    "ds_read_b128 v[192:195], v9 offset:34816\n"
    "ds_read_b128 v[232:235], v9 offset:39936\n"
    "s_waitcnt lgkmcnt(13)\n"
    "v_mfma_f32_16x16x32_bf16 v[172:175], a[244:247], v[152:155], v[172:175]\n"
    "s_waitcnt lgkmcnt(11)\n"
    "v_mfma_f32_16x16x32_bf16 v[156:159], a[248:251], v[176:179], v[156:159]\n"
    "buffer_load_dwordx4 v16, s[48:51], s61 offen offset:2048 lds\n"
    "s_waitcnt lgkmcnt(8)\n"
    "v_mfma_f32_16x16x32_bf16 v[156:159], a[252:255], v[216:219], v[156:159]\n"
    "s_waitcnt lgkmcnt(7)\n"
    "v_mfma_f32_16x16x32_bf16 v[160:163], a[248:251], v[180:183], v[160:163]\n"
    "s_waitcnt lgkmcnt(6)\n"
    "v_mfma_f32_16x16x32_bf16 v[160:163], a[252:255], v[220:223], v[160:163]\n"
    "s_waitcnt lgkmcnt(5)\n"
    "v_mfma_f32_16x16x32_bf16 v[164:167], a[248:251], v[184:187], v[164:167]\n"
    "s_waitcnt vmcnt(3) lgkmcnt(0)\n"
    "s_barrier\n"
    "ds_read_b64_tr_b16 a[240:241], v2 offset:0\n"
    "ds_read_b64_tr_b16 a[242:243], v2 offset:2048\n"
    "ds_read_b64_tr_b16 a[244:245], v2 offset:4096\n"
    "ds_read_b64_tr_b16 a[246:247], v2 offset:6144\n"
    "buffer_load_dwordx4 v17, s[48:51], s61 offen offset:3072 lds\n"
    "v_mfma_f32_16x16x32_bf16 v[164:167], a[252:255], v[224:227], v[164:167]\n"
    "v_mfma_f32_16x16x32_bf16 v[168:171], a[248:251], v[188:191], v[168:171]\n"
    "ds_read_b64 v[60:61], v11 offset:2176\n"
    "ds_read_b64 v[62:63], v11 offset:2304\n"
    "v_mfma_f32_16x16x32_bf16 v[168:171], a[252:255], v[228:231], v[168:171]\n"
    "ds_read_b64 v[64:65], v11 offset:2432\n"
    "ds_read_b64 v[66:67], v11 offset:2560\n"
    "v_mfma_f32_16x16x32_bf16 v[172:175], a[248:251], v[192:195], v[172:175]\n"
    "s_add_u32 m0, s62, 4096\n"
    "s_add_u32 s61, s96, 258176\n"
    "v_mfma_f32_16x16x32_bf16 v[172:175], a[252:255], v[232:235], v[172:175]\n"
    "buffer_load_dwordx4 v14, s[48:51], s61 offen lds\n"
    "s_waitcnt lgkmcnt(6)\n"
    "v_mfma_f32_16x16x32_bf16 v[196:199], a[240:243], a[0:3], 0\n"
    "ds_read_b64_tr_b16 a[248:249], v2 offset:8192\n"
    "ds_read_b64_tr_b16 a[250:251], v2 offset:10240\n"
    "s_waitcnt lgkmcnt(6)\n"
    "v_mfma_f32_16x16x32_bf16 v[196:199], a[244:247], a[20:23], v[196:199]\n"
    "ds_read_b64_tr_b16 a[252:253], v2 offset:12288\n"
    "ds_read_b64_tr_b16 a[254:255], v2 offset:14336\n"
    "v_mfma_f32_16x16x32_bf16 v[200:203], a[240:243], a[4:7], 0\n"
    "ds_read_b64 v[68:69], v11 offset:2688\n"
    "ds_read_b64 v[70:71], v11 offset:8320\n"
    "v_mfma_f32_16x16x32_bf16 v[200:203], a[244:247], a[24:27], v[200:203]\n"
    "buffer_load_dwordx4 v15, s[48:51], s61 offen offset:1024 lds\n"
    "v_mfma_f32_16x16x32_bf16 v[204:207], a[240:243], a[8:11], 0\n"
    "ds_read_b64 v[72:73], v11 offset:8448\n"
    "ds_read_b64 v[74:75], v11 offset:8576\n"
    "v_mfma_f32_16x16x32_bf16 v[204:207], a[244:247], a[28:31], v[204:207]\n"
    "ds_read_b64 v[76:77], v11 offset:8704\n"
    "ds_read_b64 v[78:79], v11 offset:8832\n"
    "v_mfma_f32_16x16x32_bf16 v[208:211], a[240:243], a[12:15], 0\n"
    "v_and_b32 v9, 15, v0\n"
    "v_lshlrev_b32 v9, 2, v9\n"
    "v_mfma_f32_16x16x32_bf16 v[208:211], a[244:247], a[32:35], v[208:211]\n"
    "v_add_u32 v9, %3, v9\n"
    "v_add_u32 v9, 163648, v9\n"
    "v_mfma_f32_16x16x32_bf16 v[212:215], a[240:243], a[16:19], 0\n"
    "buffer_load_dwordx4 v16, s[48:51], s61 offen offset:2048 lds\n"
    "v_mfma_f32_16x16x32_bf16 v[212:215], a[244:247], a[36:39], v[212:215]\n"
    "ds_read_b64_tr_b16 a[240:241], v2 offset:32768\n"
    "ds_read_b64_tr_b16 a[242:243], v2 offset:34816\n"
    "s_waitcnt lgkmcnt(10)\n"
    "v_mfma_f32_16x16x32_bf16 v[196:199], a[248:251], a[40:43], v[196:199]\n"
    "ds_read_b64_tr_b16 a[244:245], v2 offset:36864\n"
    "ds_read_b64_tr_b16 a[246:247], v2 offset:38912\n"
    "s_waitcnt lgkmcnt(9)\n"
    "v_mfma_f32_16x16x32_bf16 v[196:199], a[252:255], a[60:63], v[196:199]\n"
    "ds_read_b32 v9, v9 offset:0\n"
    "v_mfma_f32_16x16x32_bf16 v[200:203], a[248:251], a[44:47], v[200:203]\n"
    "buffer_load_dwordx4 v17, s[48:51], s61 offen offset:3072 lds\n"
    "buffer_load_dwordx2 v[80:81], v8, s[44:47], s67 offen offset:0 sc1\n"
    "buffer_load_dwordx2 v[82:83], v8, s[44:47], s67 offen offset:128 sc1\n"
    "buffer_load_dwordx2 v[84:85], v8, s[44:47], s67 offen offset:256 sc1\n"
    "v_mfma_f32_16x16x32_bf16 v[200:203], a[252:255], a[64:67], v[200:203]\n"
    "buffer_load_dwordx2 v[86:87], v8, s[44:47], s67 offen offset:384 sc1\n"
    "buffer_load_dwordx2 v[88:89], v8, s[44:47], s67 offen offset:512 sc1\n"
    "buffer_load_dwordx2 v[90:91], v8, s[44:47], s67 offen offset:640 sc1\n"
    "v_mfma_f32_16x16x32_bf16 v[204:207], a[248:251], a[48:51], v[204:207]\n"
    "buffer_load_dwordx2 v[92:93], v8, s[44:47], s67 offen offset:768 sc1\n"
    "buffer_load_dwordx2 v[94:95], v8, s[44:47], s67 offen offset:896 sc1\n"
    "buffer_load_dwordx2 v[18:19], v8, s[44:47], s67 offen offset:1024 sc1\n"
    "v_mfma_f32_16x16x32_bf16 v[204:207], a[252:255], a[68:71], v[204:207]\n"
    "buffer_load_dwordx2 v[12:13], v8, s[44:47], s67 offen offset:1152 sc1\n"
    "v_mfma_f32_16x16x32_bf16 v[208:211], a[248:251], a[52:55], v[208:211]\n"
    "s_add_u32 m0, s70, 16384\n"
    "s_add_u32 s61, s96, 256\n"
    "v_mfma_f32_16x16x32_bf16 v[208:211], a[252:255], a[72:75], v[208:211]\n"
    "buffer_load_dwordx4 v14, s[48:51], s61 offen lds\n"
    "v_mfma_f32_16x16x32_bf16 v[212:215], a[248:251], a[56:59], v[212:215]\n"
    "ds_read_b64_tr_b16 a[248:249], v2 offset:40960\n"
    "ds_read_b64_tr_b16 a[250:251], v2 offset:43008\n"
    "v_mfma_f32_16x16x32_bf16 v[212:215], a[252:255], a[76:79], v[212:215]\n"
    "ds_read_b64_tr_b16 a[252:253], v2 offset:45056\n"
    "ds_read_b64_tr_b16 a[254:255], v2 offset:47104\n"
    "s_waitcnt lgkmcnt(4)\n"
    "v_mfma_f32_16x16x32_bf16 v[196:199], a[240:243], a[80:83], v[196:199]\n"
    "v_mfma_f32_16x16x32_bf16 v[196:199], a[244:247], a[100:103], v[196:199]\n"
    "buffer_load_dwordx4 v15, s[48:51], s61 offen offset:1024 lds\n"
    "v_mfma_f32_16x16x32_bf16 v[200:203], a[240:243], a[84:87], v[200:203]\n"
    "v_mfma_f32_16x16x32_bf16 v[200:203], a[244:247], a[104:107], v[200:203]\n"
    "v_mfma_f32_16x16x32_bf16 v[204:207], a[240:243], a[88:91], v[204:207]\n"
    "v_mfma_f32_16x16x32_bf16 v[204:207], a[244:247], a[108:111], v[204:207]\n"
    "v_mfma_f32_16x16x32_bf16 v[208:211], a[240:243], a[92:95], v[208:211]\n"
    "buffer_load_dwordx4 v16, s[48:51], s61 offen offset:2048 lds\n"
    "v_mfma_f32_16x16x32_bf16 v[208:211], a[244:247], a[112:115], v[208:211]\n"
    "s_waitcnt vmcnt(13) lgkmcnt(0)\n"
    "s_barrier\n"
    "v_mfma_f32_16x16x32_bf16 v[212:215], a[240:243], a[96:99], v[212:215]\n"
    "ds_read_b64_tr_b16 a[240:241], v3 offset:0\n"
    "ds_read_b64_tr_b16 a[242:243], v3 offset:2048\n"
    "v_mfma_f32_16x16x32_bf16 v[212:215], a[244:247], a[116:119], v[212:215]\n"
    "ds_read_b64_tr_b16 a[244:245], v3 offset:4096\n"
    "ds_read_b64_tr_b16 a[246:247], v3 offset:6144\n"
    "v_mfma_f32_16x16x32_bf16 v[196:199], a[248:251], a[120:123], v[196:199]\n"
    "buffer_load_dwordx4 v17, s[48:51], s61 offen offset:3072 lds\n"
    "v_mfma_f32_16x16x32_bf16 v[196:199], a[252:255], a[140:143], v[196:199]\n"
    "v_mfma_f32_16x16x32_bf16 v[200:203], a[248:251], a[124:127], v[200:203]\n"
    "v_mfma_f32_16x16x32_bf16 v[200:203], a[252:255], a[144:147], v[200:203]\n"
    "v_mfma_f32_16x16x32_bf16 v[204:207], a[248:251], a[128:131], v[204:207]\n"
    "s_add_u32 m0, s70, 20480\n"
    "s_add_u32 s61, s96, 28928\n"
    "v_mfma_f32_16x16x32_bf16 v[204:207], a[252:255], a[148:151], v[204:207]\n"
    "buffer_load_dwordx4 v14, s[48:51], s61 offen lds\n"
    "v_mfma_f32_16x16x32_bf16 v[208:211], a[248:251], a[132:135], v[208:211]\n"
    "v_mfma_f32_16x16x32_bf16 v[208:211], a[252:255], a[152:155], v[208:211]\n"
    "v_mfma_f32_16x16x32_bf16 v[212:215], a[248:251], a[136:139], v[212:215]\n"
    "ds_read_b64_tr_b16 a[248:249], v3 offset:8192\n"
    "ds_read_b64_tr_b16 a[250:251], v3 offset:10240\n"
    "v_mfma_f32_16x16x32_bf16 v[212:215], a[252:255], a[156:159], v[212:215]\n"
    "buffer_load_dwordx4 v15, s[48:51], s61 offen offset:1024 lds\n"
    "s_waitcnt lgkmcnt(2)\n"
    "v_mfma_f32_16x16x32_bf16 v[236:239], a[240:243], a[160:163], 0\n"
    "ds_read_b64_tr_b16 a[252:253], v3 offset:12288\n"
    "ds_read_b64_tr_b16 a[254:255], v3 offset:14336\n"
    "v_mfma_f32_16x16x32_bf16 v[236:239], a[244:247], a[180:183], v[236:239]\n"
    "v_mfma_f32_16x16x32_bf16 v[240:243], a[240:243], a[164:167], 0\n"
    "v_mfma_f32_16x16x32_bf16 v[240:243], a[244:247], a[184:187], v[240:243]\n"
    "v_mfma_f32_16x16x32_bf16 v[244:247], a[240:243], a[168:171], 0\n"
    "buffer_load_dwordx4 v16, s[48:51], s61 offen offset:2048 lds\n"
    "v_mfma_f32_16x16x32_bf16 v[244:247], a[244:247], a[188:191], v[244:247]\n"
    "v_mfma_f32_16x16x32_bf16 v[248:251], a[240:243], a[172:175], 0\n"
    "v_mfma_f32_16x16x32_bf16 v[248:251], a[244:247], a[192:195], v[248:251]\n"
    "v_mfma_f32_16x16x32_bf16 v[252:255], a[240:243], a[176:179], 0\n"
    "buffer_load_dwordx4 v17, s[48:51], s61 offen offset:3072 lds\n"
    "v_mfma_f32_16x16x32_bf16 v[252:255], a[244:247], a[196:199], v[252:255]\n"
    "ds_read_b64_tr_b16 a[240:241], v3 offset:16384\n"
    "ds_read_b64_tr_b16 a[242:243], v3 offset:18432\n"
    "s_waitcnt lgkmcnt(2)\n"
    "v_mfma_f32_16x16x32_bf16 v[236:239], a[248:251], a[200:203], v[236:239]\n"
    "ds_read_b64_tr_b16 a[244:245], v3 offset:20480\n"
    "ds_read_b64_tr_b16 a[246:247], v3 offset:22528\n"
    "v_mfma_f32_16x16x32_bf16 v[236:239], a[252:255], a[220:223], v[236:239]\n"
    "v_mfma_f32_16x16x32_bf16 v[240:243], a[248:251], a[204:207], v[240:243]\n"
    "s_add_u32 m0, s62, 32768\n"
    "s_add_u32 s61, s96, 229632\n"
    "v_mfma_f32_16x16x32_bf16 v[240:243], a[252:255], a[224:227], v[240:243]\n"
    "buffer_load_dwordx4 v14, s[48:51], s61 offen lds\n"
    "v_mfma_f32_16x16x32_bf16 v[244:247], a[248:251], a[208:211], v[244:247]\n"
    "v_mfma_f32_16x16x32_bf16 v[244:247], a[252:255], a[228:231], v[244:247]\n"
    "s_waitcnt vmcnt(18)\n"
    "v_xor_b32 v5, s76, v81\n"
    "s_waitcnt vmcnt(17)\n"
    "v_xor_b32 v83, s76, v83\n"
    "v_mfma_f32_16x16x32_bf16 v[248:251], a[248:251], a[212:215], v[248:251]\n"
    "v_or_b32 v5, v5, v83\n"
    "s_waitcnt vmcnt(16)\n"
    "v_xor_b32 v85, s76, v85\n"
    "v_mfma_f32_16x16x32_bf16 v[248:251], a[252:255], a[232:235], v[248:251]\n"
    "buffer_load_dwordx4 v15, s[48:51], s61 offen offset:1024 lds\n"
    "v_mfma_f32_16x16x32_bf16 v[252:255], a[248:251], a[216:219], v[252:255]\n"
    "ds_read_b64_tr_b16 a[248:249], v3 offset:24576\n"
    "ds_read_b64_tr_b16 a[250:251], v3 offset:26624\n"
    "v_mfma_f32_16x16x32_bf16 v[252:255], a[252:255], a[236:239], v[252:255]\n"
    "ds_read_b64_tr_b16 a[252:253], v3 offset:28672\n"
    "ds_read_b64_tr_b16 a[254:255], v3 offset:30720\n"
    "s_waitcnt lgkmcnt(4)\n"
    "v_mfma_f32_16x16x32_bf16 v[236:239], a[240:243], v[96:99], v[236:239]\n"
    "v_or_b32 v5, v5, v85\n"
    "s_waitcnt vmcnt(16)\n"
    "v_xor_b32 v87, s76, v87\n"
    "v_mfma_f32_16x16x32_bf16 v[236:239], a[244:247], v[136:139], v[236:239]\n"
    "v_or_b32 v5, v5, v87\n"
    "s_waitcnt vmcnt(15)\n"
    "v_xor_b32 v89, s76, v89\n"
    "v_mfma_f32_16x16x32_bf16 v[240:243], a[240:243], v[100:103], v[240:243]\n"
    "buffer_load_dwordx4 v16, s[48:51], s61 offen offset:2048 lds\n"
    "v_mfma_f32_16x16x32_bf16 v[240:243], a[244:247], v[140:143], v[240:243]\n"
    "v_or_b32 v5, v5, v89\n"
    "s_waitcnt vmcnt(15)\n"
    "v_xor_b32 v91, s76, v91\n"
    "v_mfma_f32_16x16x32_bf16 v[244:247], a[240:243], v[104:107], v[244:247]\n"
    "v_or_b32 v5, v5, v91\n"
    "s_waitcnt vmcnt(14)\n"
    "v_xor_b32 v93, s76, v93\n"
    "v_mfma_f32_16x16x32_bf16 v[244:247], a[244:247], v[144:147], v[244:247]\n"
    "v_or_b32 v5, v5, v93\n"
    "s_waitcnt vmcnt(13)\n"
    "v_xor_b32 v95, s76, v95\n"
    "v_mfma_f32_16x16x32_bf16 v[248:251], a[240:243], v[108:111], v[248:251]\n"
    "buffer_load_dwordx4 v17, s[48:51], s61 offen offset:3072 lds\n"
    "v_mfma_f32_16x16x32_bf16 v[248:251], a[244:247], v[148:151], v[248:251]\n"
    "s_waitcnt vmcnt(4) lgkmcnt(0)\n"
    "s_barrier\n"
    "v_or_b32 v5, v5, v95\n"
    "v_xor_b32 v19, s76, v19\n"
    "v_or_b32 v5, v5, v19\n"
    "v_xor_b32 v13, s76, v13\n"
    "v_or_b32 v5, v5, v13\n"
    "v_cmp_ne_u32 vcc, 0, v5\n"
    "v_mfma_f32_16x16x32_bf16 v[252:255], a[240:243], v[112:115], v[252:255]\n"
    "s_cbranch_vccnz SPIN_%=\n"
    "SPINR_%=:\n"
    "ds_read_b64_tr_b16 a[240:241], v2 offset:16384\n"
    "v_mfma_f32_16x16x32_bf16 v[252:255], a[244:247], v[152:155], v[252:255]\n"
    "ds_read_b64_tr_b16 a[242:243], v2 offset:18432\n"
    "ds_read_b64_tr_b16 a[244:245], v2 offset:20480\n"
    "v_mfma_f32_16x16x32_bf16 v[236:239], a[248:251], v[176:179], v[236:239]\n"
    "s_add_u32 m0, s62, 36864\n"
    "s_add_u32 s61, s96, 258304\n"
    "v_mfma_f32_16x16x32_bf16 v[236:239], a[252:255], v[216:219], v[236:239]\n"
    "buffer_load_dwordx4 v14, s[48:51], s61 offen lds\n"
    "v_mfma_f32_16x16x32_bf16 v[240:243], a[248:251], v[180:183], v[240:243]\n"
    "ds_read_b64_tr_b16 a[246:247], v2 offset:22528\n"
    "v_mfma_f32_16x16x32_bf16 v[240:243], a[252:255], v[220:223], v[240:243]\n"
    "buffer_load_dword v7, off, s[44:47], s98 sc1\n"
    "v_mfma_f32_16x16x32_bf16 v[244:247], a[248:251], v[184:187], v[244:247]\n"
    "v_add_u32 v5, 0x10000, v3\n"
    "v_sub_f32 v80, v80, v60\n"
    "v_mfma_f32_16x16x32_bf16 v[244:247], a[252:255], v[224:227], v[244:247]\n"
    "buffer_load_dwordx4 v15, s[48:51], s61 offen offset:1024 lds\n"
    "v_mfma_f32_16x16x32_bf16 v[248:251], a[248:251], v[188:191], v[248:251]\n"
    "v_exp_f32 v80, v80\n"
    "v_mfma_f32_16x16x32_bf16 v[248:251], a[252:255], v[228:231], v[248:251]\n"
    "v_fma_f32 v61, v61, v80, v61\n"
    "v_mfma_f32_16x16x32_bf16 v[252:255], a[248:251], v[192:195], v[252:255]\n"
    "ds_read_b64_tr_b16 a[248:249], v2 offset:24576\n"
    "ds_read_b64_tr_b16 a[250:251], v2 offset:26624\n"
    "v_mfma_f32_16x16x32_bf16 v[252:255], a[252:255], v[232:235], v[252:255]\n"
    "ds_read_b64_tr_b16 a[252:253], v2 offset:28672\n"
    "ds_read_b64_tr_b16 a[254:255], v2 offset:30720\n"
    "s_waitcnt lgkmcnt(5)\n"
    "v_mfma_f32_16x16x32_bf16 v[20:23], a[240:243], a[0:3], 0\n"
    "buffer_load_dwordx4 v16, s[48:51], s61 offen offset:2048 lds\n"
    "s_waitcnt lgkmcnt(4)\n"
    "v_mfma_f32_16x16x32_bf16 v[20:23], a[244:247], a[20:23], v[20:23]\n"
    "v_rcp_f32 v61, v61\n"
    "v_mfma_f32_16x16x32_bf16 v[24:27], a[240:243], a[4:7], 0\n"
    "v_sub_f32 v82, v82, v62\n"
    "v_mfma_f32_16x16x32_bf16 v[24:27], a[244:247], a[24:27], v[24:27]\n"
    "v_exp_f32 v82, v82\n"
    "v_mfma_f32_16x16x32_bf16 v[28:31], a[240:243], a[8:11], 0\n"
    "buffer_load_dwordx4 v17, s[48:51], s61 offen offset:3072 lds\n"
    "v_mfma_f32_16x16x32_bf16 v[28:31], a[244:247], a[28:31], v[28:31]\n"
    "v_fma_f32 v63, v63, v82, v63\n"
    "v_mfma_f32_16x16x32_bf16 v[32:35], a[240:243], a[12:15], 0\n"
    "v_rcp_f32 v63, v63\n"
    "v_mfma_f32_16x16x32_bf16 v[32:35], a[244:247], a[32:35], v[32:35]\n"
    "v_sub_f32 v84, v84, v64\n"
    "v_mfma_f32_16x16x32_bf16 v[36:39], a[240:243], a[16:19], 0\n"
    "s_add_u32 m0, s62, 65536\n"
    "s_add_u32 s61, s96, 384\n"
    "v_mfma_f32_16x16x32_bf16 v[36:39], a[244:247], a[36:39], v[36:39]\n"
    "buffer_load_dwordx4 v14, s[48:51], s61 offen lds\n"
    "s_waitcnt lgkmcnt(0)\n"
    "v_mfma_f32_16x16x32_bf16 v[20:23], a[248:251], a[40:43], v[20:23]\n"
    "ds_read_b64_tr_b16 a[240:241], v2 offset:49152\n"
    "ds_read_b64_tr_b16 a[242:243], v2 offset:51200\n"
    "v_mfma_f32_16x16x32_bf16 v[20:23], a[252:255], a[60:63], v[20:23]\n"
    "ds_read_b64_tr_b16 a[244:245], v2 offset:53248\n"
    "ds_read_b64_tr_b16 a[246:247], v2 offset:55296\n"
    "v_mfma_f32_16x16x32_bf16 v[24:27], a[248:251], a[44:47], v[24:27]\n"
    "v_exp_f32 v84, v84\n"
    "v_mfma_f32_16x16x32_bf16 v[24:27], a[252:255], a[64:67], v[24:27]\n"
    "buffer_load_dwordx4 v15, s[48:51], s61 offen offset:1024 lds\n"
    "v_mfma_f32_16x16x32_bf16 v[28:31], a[248:251], a[48:51], v[28:31]\n"
    "v_fma_f32 v65, v65, v84, v65\n"
    "v_mfma_f32_16x16x32_bf16 v[28:31], a[252:255], a[68:71], v[28:31]\n"
    "v_rcp_f32 v65, v65\n"
    "v_mfma_f32_16x16x32_bf16 v[32:35], a[248:251], a[52:55], v[32:35]\n"
    "v_sub_f32 v86, v86, v66\n"
    "v_mfma_f32_16x16x32_bf16 v[32:35], a[252:255], a[72:75], v[32:35]\n"
    "v_exp_f32 v86, v86\n"
    "v_mfma_f32_16x16x32_bf16 v[36:39], a[248:251], a[56:59], v[36:39]\n"
    "buffer_load_dwordx4 v16, s[48:51], s61 offen offset:2048 lds\n"
    "v_mfma_f32_16x16x32_bf16 v[36:39], a[252:255], a[76:79], v[36:39]\n"
    "v_fma_f32 v67, v67, v86, v67\n"
    "ds_read_b64_tr_b16 a[248:249], v2 offset:57344\n"
    "s_waitcnt lgkmcnt(3)\n"
    "v_mfma_f32_16x16x32_bf16 v[20:23], a[240:243], a[80:83], v[20:23]\n"
    "v_rcp_f32 v67, v67\n"
    "v_sub_f32 v88, v88, v68\n"
    "v_exp_f32 v88, v88\n"
    "s_nop 0\n"
    "v_fma_f32 v69, v69, v88, v69\n"
    "v_rcp_f32 v69, v69\n"
    "v_sub_f32 v90, v90, v70\n"
    "v_exp_f32 v90, v90\n"
    "s_nop 0\n"
    "v_fma_f32 v71, v71, v90, v71\n"
    "v_rcp_f32 v71, v71\n"
    "v_sub_f32 v92, v92, v72\n"
    "v_exp_f32 v92, v92\n"
    "s_nop 0\n"
    "v_fma_f32 v73, v73, v92, v73\n"
    "v_rcp_f32 v73, v73\n"
    "v_sub_f32 v94, v94, v74\n"
    "v_exp_f32 v94, v94\n"
    "s_nop 0\n"
    "v_fma_f32 v75, v75, v94, v75\n"
    "v_rcp_f32 v75, v75\n"
    "v_sub_f32 v18, v18, v76\n"
    "v_exp_f32 v18, v18\n"
    "s_nop 0\n"
    "v_fma_f32 v77, v77, v18, v77\n"
    "v_rcp_f32 v77, v77\n"
    "v_sub_f32 v12, v12, v78\n"
    "v_exp_f32 v12, v12\n"
    "s_nop 0\n"
    "v_fma_f32 v79, v79, v12, v79\n"
    "v_rcp_f32 v79, v79\n"
    "s_waitcnt lgkmcnt(0)\n"
    "v_mfma_f32_16x16x32_bf16 v[20:23], a[244:247], a[100:103], v[20:23]\n"
    "ds_read_b64_tr_b16 a[250:251], v2 offset:59392\n"
    "ds_read_b64_tr_b16 a[252:253], v2 offset:61440\n"
    "v_mfma_f32_16x16x32_bf16 v[24:27], a[240:243], a[84:87], v[24:27]\n"
    "buffer_load_dwordx4 v17, s[48:51], s61 offen offset:3072 lds\n"
    "v_mfma_f32_16x16x32_bf16 v[24:27], a[244:247], a[104:107], v[24:27]\n"
    "ds_read_b64_tr_b16 a[254:255], v2 offset:63488\n"
    "v_mul_f32 v116, v116, v61\n"
    "v_mfma_f32_16x16x32_bf16 v[28:31], a[240:243], a[88:91], v[28:31]\n"
    "v_fma_f32 v116, v156, v71, v116\n"
    "v_mul_f32 v117, v117, v61\n"
    "v_mfma_f32_16x16x32_bf16 v[28:31], a[244:247], a[108:111], v[28:31]\n"
    "v_fma_f32 v117, v157, v71, v117\n"
    "v_mul_f32 v118, v118, v61\n"
    "v_mfma_f32_16x16x32_bf16 v[32:35], a[240:243], a[92:95], v[32:35]\n"
    "s_add_u32 m0, s62, 69632\n"
    "s_add_u32 s61, s96, 29056\n"
    "v_mfma_f32_16x16x32_bf16 v[32:35], a[244:247], a[112:115], v[32:35]\n"
    "s_waitcnt vmcnt(4) lgkmcnt(0)\n"
    "s_barrier\n"
    "buffer_load_dwordx4 v14, s[48:51], s61 offen lds\n"
    "v_mfma_f32_16x16x32_bf16 v[36:39], a[240:243], a[96:99], v[36:39]\n"
    "ds_read_b64_tr_b16 a[240:241], v3 offset:32768\n"
    "ds_read_b64_tr_b16 a[242:243], v3 offset:34816\n"
    "v_mfma_f32_16x16x32_bf16 v[36:39], a[244:247], a[116:119], v[36:39]\n"
    "ds_read_b64_tr_b16 a[244:245], v3 offset:36864\n"
    "ds_read_b64_tr_b16 a[246:247], v3 offset:38912\n"
    "v_mfma_f32_16x16x32_bf16 v[20:23], a[248:251], a[120:123], v[20:23]\n"
    "v_fma_f32 v118, v158, v71, v118\n"
    "v_mul_f32 v119, v119, v61\n"
    "v_mfma_f32_16x16x32_bf16 v[20:23], a[252:255], a[140:143], v[20:23]\n"
    "buffer_load_dwordx4 v15, s[48:51], s61 offen offset:1024 lds\n"
    "v_mfma_f32_16x16x32_bf16 v[24:27], a[248:251], a[124:127], v[24:27]\n"
    "v_fma_f32 v119, v159, v71, v119\n"
    "v_cvt_pk_bf16_f32 v116, v116, v117\n"
    "v_mfma_f32_16x16x32_bf16 v[24:27], a[252:255], a[144:147], v[24:27]\n"
    "v_cvt_pk_bf16_f32 v117, v118, v119\n"
    "v_mul_f32 v120, v120, v63\n"
    "v_mfma_f32_16x16x32_bf16 v[28:31], a[248:251], a[128:131], v[28:31]\n"
    "v_fma_f32 v120, v160, v73, v120\n"
    "v_mul_f32 v121, v121, v63\n"
    "v_mfma_f32_16x16x32_bf16 v[28:31], a[252:255], a[148:151], v[28:31]\n"
    "v_fma_f32 v121, v161, v73, v121\n"
    "v_mul_f32 v122, v122, v63\n"
    "v_mfma_f32_16x16x32_bf16 v[32:35], a[248:251], a[132:135], v[32:35]\n"
    "buffer_load_dwordx4 v16, s[48:51], s61 offen offset:2048 lds\n"
    "v_mfma_f32_16x16x32_bf16 v[32:35], a[252:255], a[152:155], v[32:35]\n"
    "v_fma_f32 v122, v162, v73, v122\n"
    "v_mul_f32 v123, v123, v63\n"
    "v_mfma_f32_16x16x32_bf16 v[36:39], a[248:251], a[136:139], v[36:39]\n"
    "ds_read_b64_tr_b16 a[248:249], v3 offset:40960\n"
    "ds_read_b64_tr_b16 a[250:251], v3 offset:43008\n"
    "v_mfma_f32_16x16x32_bf16 v[36:39], a[252:255], a[156:159], v[36:39]\n"
    "ds_read_b64_tr_b16 a[252:253], v3 offset:45056\n"
    "ds_read_b64_tr_b16 a[254:255], v3 offset:47104\n"
    "s_waitcnt lgkmcnt(4)\n"
    "v_mfma_f32_16x16x32_bf16 v[40:43], a[240:243], a[160:163], 0\n"
    "buffer_load_dwordx4 v17, s[48:51], s61 offen offset:3072 lds\n"
    "v_mfma_f32_16x16x32_bf16 v[40:43], a[244:247], a[180:183], v[40:43]\n"
    "v_fma_f32 v123, v163, v73, v123\n"
    "v_cvt_pk_bf16_f32 v118, v120, v121\n"
    "v_mfma_f32_16x16x32_bf16 v[44:47], a[240:243], a[164:167], 0\n"
    "v_cvt_pk_bf16_f32 v119, v122, v123\n"
    "v_mfma_f32_16x16x32_bf16 v[44:47], a[244:247], a[184:187], v[44:47]\n"
    "buffer_store_dwordx4 v[116:119], v1, s[52:55], s81 offen offset:0 sc1\n"
    "v_mfma_f32_16x16x32_bf16 v[48:51], a[240:243], a[168:171], 0\n"
    "s_add_u32 m0, s70, 0\n"
    "s_add_u32 s61, s96, 229760\n"
    "v_mfma_f32_16x16x32_bf16 v[48:51], a[244:247], a[188:191], v[48:51]\n"
    "buffer_load_dwordx4 v14, s[48:51], s61 offen lds\n"
    "v_mfma_f32_16x16x32_bf16 v[52:55], a[240:243], a[172:175], 0\n"
    "v_mul_f32 v124, v124, v65\n"
    "v_fma_f32 v124, v164, v75, v124\n"
    "v_mfma_f32_16x16x32_bf16 v[52:55], a[244:247], a[192:195], v[52:55]\n"
    "v_mul_f32 v125, v125, v65\n"
    "v_fma_f32 v125, v165, v75, v125\n"
    "v_mfma_f32_16x16x32_bf16 v[56:59], a[240:243], a[176:179], 0\n"
    "v_mul_f32 v126, v126, v65\n"
    "v_fma_f32 v126, v166, v75, v126\n"
    "v_mfma_f32_16x16x32_bf16 v[56:59], a[244:247], a[196:199], v[56:59]\n"
    "buffer_load_dwordx4 v15, s[48:51], s61 offen offset:1024 lds\n"
    "s_waitcnt lgkmcnt(0)\n"
    "v_mfma_f32_16x16x32_bf16 v[40:43], a[248:251], a[200:203], v[40:43]\n"
    "v_mul_f32 v127, v127, v65\n"
    "v_fma_f32 v127, v167, v75, v127\n"
    "v_cvt_pk_bf16_f32 v124, v124, v125\n"
    "v_cvt_pk_bf16_f32 v125, v126, v127\n"
    "v_mul_f32 v128, v128, v67\n"
    "v_fma_f32 v128, v168, v77, v128\n"
    "v_mul_f32 v129, v129, v67\n"
    "v_fma_f32 v129, v169, v77, v129\n"
    "v_mul_f32 v130, v130, v67\n"
    "v_fma_f32 v130, v170, v77, v130\n"
    "v_mul_f32 v131, v131, v67\n"
    "v_fma_f32 v131, v171, v77, v131\n"
    "v_cvt_pk_bf16_f32 v126, v128, v129\n"
    "v_cvt_pk_bf16_f32 v127, v130, v131\n"
    "buffer_store_dwordx4 v[124:127], v1, s[52:55], s81 offen offset:1024 sc1\n"
    "v_mul_f32 v132, v132, v69\n"
    "v_fma_f32 v132, v172, v79, v132\n"
    "v_mul_f32 v133, v133, v69\n"
    "v_fma_f32 v133, v173, v79, v133\n"
    "v_mul_f32 v134, v134, v69\n"
    "v_fma_f32 v134, v174, v79, v134\n"
    "v_mul_f32 v135, v135, v69\n"
    "v_fma_f32 v135, v175, v79, v135\n"
    "v_cvt_pk_bf16_f32 v92, v132, v133\n"
    "v_cvt_pk_bf16_f32 v93, v134, v135\n"
    "s_add_u32 s81, s81, 8192\n"
    "v_mfma_f32_16x16x32_bf16 v[40:43], a[252:255], a[220:223], v[40:43]\n"
    "ds_read_b64_tr_b16 a[240:241], v3 offset:49152\n"
    "ds_read_b64_tr_b16 a[242:243], v3 offset:51200\n"
    "v_mfma_f32_16x16x32_bf16 v[44:47], a[248:251], a[204:207], v[44:47]\n"
    "ds_read_b64_tr_b16 a[244:245], v3 offset:53248\n"
    "ds_read_b64_tr_b16 a[246:247], v3 offset:55296\n"
    "v_mfma_f32_16x16x32_bf16 v[44:47], a[252:255], a[224:227], v[44:47]\n"
    "v_mul_f32 v196, v196, v61\n"
    "v_fma_f32 v196, v236, v71, v196\n"
    "v_mfma_f32_16x16x32_bf16 v[48:51], a[248:251], a[208:211], v[48:51]\n"
    "buffer_load_dwordx4 v16, s[48:51], s61 offen offset:2048 lds\n"
    "v_mfma_f32_16x16x32_bf16 v[48:51], a[252:255], a[228:231], v[48:51]\n"
    "v_mul_f32 v197, v197, v61\n"
    "v_fma_f32 v197, v237, v71, v197\n"
    "v_mfma_f32_16x16x32_bf16 v[52:55], a[248:251], a[212:215], v[52:55]\n"
    "v_mul_f32 v198, v198, v61\n"
    "v_fma_f32 v198, v238, v71, v198\n"
    "v_mfma_f32_16x16x32_bf16 v[52:55], a[252:255], a[232:235], v[52:55]\n"
    "v_mul_f32 v199, v199, v61\n"
    "v_fma_f32 v199, v239, v71, v199\n"
    "v_mfma_f32_16x16x32_bf16 v[56:59], a[248:251], a[216:219], v[56:59]\n"
    "buffer_load_dwordx4 v17, s[48:51], s61 offen offset:3072 lds\n"
    "v_mfma_f32_16x16x32_bf16 v[56:59], a[252:255], a[236:239], v[56:59]\n"
    "ds_read_b64_tr_b16 a[248:249], v3 offset:57344\n"
    "ds_read_b64_tr_b16 a[250:251], v3 offset:59392\n"
    "s_waitcnt lgkmcnt(4)\n"
    "v_mfma_f32_16x16x32_bf16 v[40:43], a[240:243], v[96:99], v[40:43]\n"
    "ds_read_b64_tr_b16 a[252:253], v3 offset:61440\n"
    "ds_read_b64_tr_b16 a[254:255], v3 offset:63488\n"
    "s_waitcnt lgkmcnt(4)\n"
    "v_mfma_f32_16x16x32_bf16 v[40:43], a[244:247], v[136:139], v[40:43]\n"
    "v_cvt_pk_bf16_f32 v196, v196, v197\n"
    "v_cvt_pk_bf16_f32 v197, v198, v199\n"
    "v_mfma_f32_16x16x32_bf16 v[44:47], a[240:243], v[100:103], v[44:47]\n"
    "s_add_u32 m0, s70, 4096\n"
    "s_add_u32 s61, s96, 258432\n"
    "v_mfma_f32_16x16x32_bf16 v[44:47], a[244:247], v[140:143], v[44:47]\n"
    "buffer_load_dwordx4 v14, s[48:51], s61 offen lds\n"
    "v_mfma_f32_16x16x32_bf16 v[48:51], a[240:243], v[104:107], v[48:51]\n"
    "v_mul_f32 v200, v200, v63\n"
    "v_fma_f32 v200, v240, v73, v200\n"
    "v_mfma_f32_16x16x32_bf16 v[48:51], a[244:247], v[144:147], v[48:51]\n"
    "v_mul_f32 v201, v201, v63\n"
    "v_fma_f32 v201, v241, v73, v201\n"
    "v_mfma_f32_16x16x32_bf16 v[52:55], a[240:243], v[108:111], v[52:55]\n"
    "v_mul_f32 v202, v202, v63\n"
    "v_fma_f32 v202, v242, v73, v202\n"
    "v_mfma_f32_16x16x32_bf16 v[52:55], a[244:247], v[148:151], v[52:55]\n"
    "s_waitcnt vmcnt(7) lgkmcnt(0)\n"
    "s_barrier\n"
    "buffer_load_dwordx4 v15, s[48:51], s61 offen offset:1024 lds\n"
    "v_mfma_f32_16x16x32_bf16 v[56:59], a[240:243], v[112:115], v[56:59]\n"
    "ds_read_b64_tr_b16 a[240:241], v5 offset:0\n"
    "ds_read_b64_tr_b16 a[242:243], v5 offset:2048\n"
    "v_mfma_f32_16x16x32_bf16 v[56:59], a[244:247], v[152:155], v[56:59]\n"
    "ds_read_b64_tr_b16 a[244:245], v5 offset:4096\n"
    "ds_read_b64_tr_b16 a[246:247], v5 offset:6144\n"
    "v_mfma_f32_16x16x32_bf16 v[40:43], a[248:251], v[176:179], v[40:43]\n"
    "v_mul_f32 v203, v203, v63\n"
    "v_fma_f32 v203, v243, v73, v203\n"
    "v_mfma_f32_16x16x32_bf16 v[40:43], a[252:255], v[216:219], v[40:43]\n"
    "v_cvt_pk_bf16_f32 v198, v200, v201\n"
    "v_cvt_pk_bf16_f32 v199, v202, v203\n"
    "v_mfma_f32_16x16x32_bf16 v[44:47], a[248:251], v[180:183], v[44:47]\n"
    "buffer_load_dwordx4 v16, s[48:51], s61 offen offset:2048 lds\n"
    "v_mfma_f32_16x16x32_bf16 v[44:47], a[252:255], v[220:223], v[44:47]\n"
    "buffer_store_dwordx4 v[196:199], v1, s[52:55], s81 offen offset:0 sc1\n"
    "v_mfma_f32_16x16x32_bf16 v[48:51], a[248:251], v[184:187], v[48:51]\n"
    "v_mul_f32 v204, v204, v65\n"
    "v_fma_f32 v204, v244, v75, v204\n"
    "v_mfma_f32_16x16x32_bf16 v[48:51], a[252:255], v[224:227], v[48:51]\n"
    "v_mul_f32 v205, v205, v65\n"
    "v_fma_f32 v205, v245, v75, v205\n"
    "v_mfma_f32_16x16x32_bf16 v[52:55], a[248:251], v[188:191], v[52:55]\n"
    "buffer_load_dwordx4 v17, s[48:51], s61 offen offset:3072 lds\n"
    "v_mfma_f32_16x16x32_bf16 v[52:55], a[252:255], v[228:231], v[52:55]\n"
    "v_mul_f32 v206, v206, v65\n"
    "v_fma_f32 v206, v246, v75, v206\n"
    "v_mfma_f32_16x16x32_bf16 v[56:59], a[248:251], v[192:195], v[56:59]\n"
    "ds_read_b64_tr_b16 a[248:249], v5 offset:8192\n"
    "ds_read_b64_tr_b16 a[250:251], v5 offset:10240\n"
    "v_mfma_f32_16x16x32_bf16 v[56:59], a[252:255], v[232:235], v[56:59]\n"
    "ds_read_b64_tr_b16 a[252:253], v5 offset:12288\n"
    "ds_read_b64_tr_b16 a[254:255], v5 offset:14336\n"
    "s_waitcnt lgkmcnt(4)\n"
    "v_mfma_f32_16x16x32_bf16 v[116:119], a[240:243], a[0:3], 0\n"
    "s_add_u32 m0, s62, 0\n"
    "s_add_u32 s61, s96, 512\n"
    "v_mfma_f32_16x16x32_bf16 v[116:119], a[244:247], a[20:23], v[116:119]\n"
    "buffer_load_dwordx4 v14, s[48:51], s61 offen lds\n"
    "v_mfma_f32_16x16x32_bf16 v[120:123], a[240:243], a[4:7], 0\n"
    "v_mul_f32 v207, v207, v65\n"
    "v_fma_f32 v207, v247, v75, v207\n"
    "v_mfma_f32_16x16x32_bf16 v[120:123], a[244:247], a[24:27], v[120:123]\n"
    "v_cvt_pk_bf16_f32 v204, v204, v205\n"
    "v_cvt_pk_bf16_f32 v205, v206, v207\n"
    "v_mfma_f32_16x16x32_bf16 v[124:127], a[240:243], a[8:11], 0\n"
    "v_mul_f32 v208, v208, v67\n"
    "v_fma_f32 v208, v248, v77, v208\n"
    "v_mfma_f32_16x16x32_bf16 v[124:127], a[244:247], a[28:31], v[124:127]\n"
    "buffer_load_dwordx4 v15, s[48:51], s61 offen offset:1024 lds\n"
    "v_mfma_f32_16x16x32_bf16 v[128:131], a[240:243], a[12:15], 0\n"
    "v_cmp_ne_u32 vcc, v7, v6\n"
    "v_mul_f32 v209, v209, v67\n"
    "v_mfma_f32_16x16x32_bf16 v[128:131], a[244:247], a[32:35], v[128:131]\n"
    "s_cbranch_vccnz RP_%=\n"
    "RPR_%=:\n"
    "v_fma_f32 v209, v249, v77, v209\n"
    "v_mfma_f32_16x16x32_bf16 v[132:135], a[240:243], a[16:19], 0\n"
    "ds_read_b64_tr_b16 a[240:241], v5 offset:16384\n"
    "ds_read_b64_tr_b16 a[242:243], v5 offset:18432\n"
    "v_mfma_f32_16x16x32_bf16 v[132:135], a[244:247], a[36:39], v[132:135]\n"
    "ds_read_b64_tr_b16 a[244:245], v5 offset:20480\n"
    "ds_read_b64_tr_b16 a[246:247], v5 offset:22528\n"
    "s_waitcnt lgkmcnt(4)\n"
    "v_mfma_f32_16x16x32_bf16 v[116:119], a[248:251], a[40:43], v[116:119]\n"
    "buffer_load_dwordx4 v16, s[48:51], s61 offen offset:2048 lds\n"
    "v_mfma_f32_16x16x32_bf16 v[116:119], a[252:255], a[60:63], v[116:119]\n"
    "v_mul_f32 v210, v210, v67\n"
    "v_fma_f32 v210, v250, v77, v210\n"
    "v_mfma_f32_16x16x32_bf16 v[120:123], a[248:251], a[44:47], v[120:123]\n"
    "v_mul_f32 v211, v211, v67\n"
    "v_fma_f32 v211, v251, v77, v211\n"
    "v_mfma_f32_16x16x32_bf16 v[120:123], a[252:255], a[64:67], v[120:123]\n"
    "v_cvt_pk_bf16_f32 v206, v208, v209\n"
    "v_cvt_pk_bf16_f32 v207, v210, v211\n"
    "v_mfma_f32_16x16x32_bf16 v[124:127], a[248:251], a[48:51], v[124:127]\n"
    "buffer_load_dwordx4 v17, s[48:51], s61 offen offset:3072 lds\n"
    "v_mfma_f32_16x16x32_bf16 v[124:127], a[252:255], a[68:71], v[124:127]\n"
    "buffer_store_dwordx4 v[204:207], v1, s[52:55], s81 offen offset:1024 sc1\n"
    "v_mul_f32 v212, v212, v69\n"
    "v_fma_f32 v212, v252, v79, v212\n"
    "v_mul_f32 v213, v213, v69\n"
    "v_fma_f32 v213, v253, v79, v213\n"
    "v_mul_f32 v214, v214, v69\n"
    "v_fma_f32 v214, v254, v79, v214\n"
    "v_mul_f32 v215, v215, v69\n"
    "v_fma_f32 v215, v255, v79, v215\n"
    "v_cvt_pk_bf16_f32 v94, v212, v213\n"
    "v_cvt_pk_bf16_f32 v95, v214, v215\n"
    "buffer_store_dwordx4 v[92:95], v1, s[52:55], s84 offen sc1\n"
    "s_add_u32 s84, s84, 20480\n"
    "s_add_u32 s81, s81, 12288\n"
    "v_mfma_f32_16x16x32_bf16 v[128:131], a[248:251], a[52:55], v[128:131]\n"
    "v_and_b32 v84, 15, v0\n"
    "v_lshlrev_b32 v85, 6, v84\n"
    "v_mfma_f32_16x16x32_bf16 v[128:131], a[252:255], a[72:75], v[128:131]\n"
    "v_lshlrev_b32 v84, 2, v84\n"
    "s_lshl_b32 s66, s57, 6\n"
    "v_mfma_f32_16x16x32_bf16 v[132:135], a[248:251], a[56:59], v[132:135]\n"
    "s_add_u32 m0, s62, 4096\n"
    "s_add_u32 s61, s96, 29184\n"
    "v_mfma_f32_16x16x32_bf16 v[132:135], a[252:255], a[76:79], v[132:135]\n"
    "buffer_load_dwordx4 v14, s[48:51], s61 offen lds\n"
    "s_waitcnt lgkmcnt(0)\n"
    "v_mfma_f32_16x16x32_bf16 v[116:119], a[240:243], a[80:83], v[116:119]\n"
    "ds_read_b64_tr_b16 a[248:249], v5 offset:24576\n"
    "ds_read_b64_tr_b16 a[250:251], v5 offset:26624\n"
    "v_mfma_f32_16x16x32_bf16 v[116:119], a[244:247], a[100:103], v[116:119]\n"
    "ds_read_b64_tr_b16 a[252:253], v5 offset:28672\n"
    "ds_read_b64_tr_b16 a[254:255], v5 offset:30720\n"
    "v_mfma_f32_16x16x32_bf16 v[120:123], a[240:243], a[84:87], v[120:123]\n"
    "s_add_u32 s66, s66, 1179648\n"
    "s_lshl_b32 s67, s57, 2\n"
    "v_mfma_f32_16x16x32_bf16 v[120:123], a[244:247], a[104:107], v[120:123]\n"
    "buffer_load_dwordx4 v15, s[48:51], s61 offen offset:1024 lds\n"
    "v_mfma_f32_16x16x32_bf16 v[124:127], a[240:243], a[88:91], v[124:127]\n"
    "s_add_u32 s67, s67, 1179648\n"
    "v_mul_f32 v20, v20, v61\n"
    "v_mfma_f32_16x16x32_bf16 v[124:127], a[244:247], a[108:111], v[124:127]\n"
    "buffer_load_dword v83, v84, s[52:55], s66 offen sc1\n"
    "v_mfma_f32_16x16x32_bf16 v[128:131], a[240:243], a[92:95], v[128:131]\n"
    "v_fma_f32 v20, v40, v71, v20\n"
    "v_mul_f32 v21, v21, v61\n"
    "v_mfma_f32_16x16x32_bf16 v[128:131], a[244:247], a[112:115], v[128:131]\n"
    "s_waitcnt vmcnt(9) lgkmcnt(0)\n"
    "s_barrier\n"
    "v_fma_f32 v21, v41, v71, v21\n"
    "v_mfma_f32_16x16x32_bf16 v[132:135], a[240:243], a[96:99], v[132:135]\n"
    "buffer_load_dwordx4 v16, s[48:51], s61 offen offset:2048 lds\n"
    "v_mfma_f32_16x16x32_bf16 v[132:135], a[244:247], a[116:119], v[132:135]\n"
    "ds_read_b64_tr_b16 a[240:241], v2 offset:0\n"
    "ds_read_b64_tr_b16 a[242:243], v2 offset:2048\n"
    "v_mfma_f32_16x16x32_bf16 v[116:119], a[248:251], a[120:123], v[116:119]\n"
    "ds_read_b64_tr_b16 a[244:245], v2 offset:4096\n"
    "ds_read_b64_tr_b16 a[246:247], v2 offset:6144\n"
    "v_mfma_f32_16x16x32_bf16 v[116:119], a[252:255], a[140:143], v[116:119]\n"
    "v_mul_f32 v22, v22, v61\n"
    "v_fma_f32 v22, v42, v71, v22\n"
    "v_mfma_f32_16x16x32_bf16 v[120:123], a[248:251], a[124:127], v[120:123]\n"
    "buffer_load_dwordx4 v17, s[48:51], s61 offen offset:3072 lds\n"
    "v_mfma_f32_16x16x32_bf16 v[120:123], a[252:255], a[144:147], v[120:123]\n"
    "v_mul_f32 v23, v23, v61\n"
    "v_fma_f32 v23, v43, v71, v23\n"
    "v_mfma_f32_16x16x32_bf16 v[124:127], a[248:251], a[128:131], v[124:127]\n"
    "v_cvt_pk_bf16_f32 v20, v20, v21\n"
    "v_cvt_pk_bf16_f32 v21, v22, v23\n"
    "v_mfma_f32_16x16x32_bf16 v[124:127], a[252:255], a[148:151], v[124:127]\n"
    "v_mul_f32 v24, v24, v63\n"
    "v_fma_f32 v24, v44, v73, v24\n"
    "v_mfma_f32_16x16x32_bf16 v[128:131], a[248:251], a[132:135], v[128:131]\n"
    "s_add_u32 m0, s70, 16384\n"
    "s_add_u32 s61, s96, 229888\n"
    "v_mfma_f32_16x16x32_bf16 v[128:131], a[252:255], a[152:155], v[128:131]\n"
    "buffer_load_dwordx4 v14, s[48:51], s61 offen lds\n"
    "v_mfma_f32_16x16x32_bf16 v[132:135], a[248:251], a[136:139], v[132:135]\n"
    "ds_read_b64_tr_b16 a[248:249], v2 offset:8192\n"
    "ds_read_b64_tr_b16 a[250:251], v2 offset:10240\n"
    "v_mfma_f32_16x16x32_bf16 v[132:135], a[252:255], a[156:159], v[132:135]\n"
    "ds_read_b64_tr_b16 a[252:253], v2 offset:12288\n"
    "ds_read_b64_tr_b16 a[254:255], v2 offset:14336\n"
    "s_waitcnt lgkmcnt(4)\n"
    "v_mfma_f32_16x16x32_bf16 v[156:159], a[240:243], a[160:163], 0\n"
    "v_mul_f32 v25, v25, v63\n"
    "v_fma_f32 v25, v45, v73, v25\n"
    "v_mfma_f32_16x16x32_bf16 v[156:159], a[244:247], a[180:183], v[156:159]\n"
    "buffer_load_dwordx4 v15, s[48:51], s61 offen offset:1024 lds\n"
    "v_mfma_f32_16x16x32_bf16 v[160:163], a[240:243], a[164:167], 0\n"
    "v_mul_f32 v26, v26, v63\n"
    "v_fma_f32 v26, v46, v73, v26\n"
    "v_mfma_f32_16x16x32_bf16 v[160:163], a[244:247], a[184:187], v[160:163]\n"
    "v_mul_f32 v27, v27, v63\n"
    "v_fma_f32 v27, v47, v73, v27\n"
    "v_mfma_f32_16x16x32_bf16 v[164:167], a[240:243], a[168:171], 0\n"
    "v_cvt_pk_bf16_f32 v22, v24, v25\n"
    "v_cvt_pk_bf16_f32 v23, v26, v27\n"
    "v_mfma_f32_16x16x32_bf16 v[164:167], a[244:247], a[188:191], v[164:167]\n"
    "buffer_store_dwordx4 v[20:23], v1, s[52:55], s81 offen offset:0 sc1\n"
    "v_mfma_f32_16x16x32_bf16 v[168:171], a[240:243], a[172:175], 0\n"
    "buffer_load_dwordx4 v16, s[48:51], s61 offen offset:2048 lds\n"
    "v_mfma_f32_16x16x32_bf16 v[168:171], a[244:247], a[192:195], v[168:171]\n"
    "v_mul_f32 v28, v28, v65\n"
    "v_fma_f32 v28, v48, v75, v28\n"
    "v_mfma_f32_16x16x32_bf16 v[172:175], a[240:243], a[176:179], 0\n"
    "ds_read_b64_tr_b16 a[240:241], v2 offset:32768\n"
    "ds_read_b64_tr_b16 a[242:243], v2 offset:34816\n"
    "v_mfma_f32_16x16x32_bf16 v[172:175], a[244:247], a[196:199], v[172:175]\n"
    "ds_read_b64_tr_b16 a[244:245], v2 offset:36864\n"
    "ds_read_b64_tr_b16 a[246:247], v2 offset:38912\n"
    "s_waitcnt lgkmcnt(4)\n"
    "v_mfma_f32_16x16x32_bf16 v[156:159], a[248:251], a[200:203], v[156:159]\n"
    "buffer_load_dwordx4 v17, s[48:51], s61 offen offset:3072 lds\n"
    "v_mfma_f32_16x16x32_bf16 v[156:159], a[252:255], a[220:223], v[156:159]\n"
    "v_mul_f32 v29, v29, v65\n"
    "v_fma_f32 v29, v49, v75, v29\n"
    "v_mfma_f32_16x16x32_bf16 v[160:163], a[248:251], a[204:207], v[160:163]\n"
    "v_mul_f32 v30, v30, v65\n"
    "v_fma_f32 v30, v50, v75, v30\n"
    "v_mfma_f32_16x16x32_bf16 v[160:163], a[252:255], a[224:227], v[160:163]\n"
    "v_mul_f32 v31, v31, v65\n"
    "v_fma_f32 v31, v51, v75, v31\n"
    "v_mfma_f32_16x16x32_bf16 v[164:167], a[248:251], a[208:211], v[164:167]\n"
    "s_add_u32 m0, s70, 20480\n"
    "s_add_u32 s61, s96, 258560\n"
    "v_mfma_f32_16x16x32_bf16 v[164:167], a[252:255], a[228:231], v[164:167]\n"
    "buffer_load_dwordx4 v14, s[48:51], s61 offen lds\n"
    "v_mfma_f32_16x16x32_bf16 v[168:171], a[248:251], a[212:215], v[168:171]\n"
    "v_cvt_pk_bf16_f32 v28, v28, v29\n"
    "v_cvt_pk_bf16_f32 v29, v30, v31\n"
    "v_mfma_f32_16x16x32_bf16 v[168:171], a[252:255], a[232:235], v[168:171]\n"
    "v_mul_f32 v32, v32, v67\n"
    "v_fma_f32 v32, v52, v77, v32\n"
    "v_mfma_f32_16x16x32_bf16 v[172:175], a[248:251], a[216:219], v[172:175]\n"
    "ds_read_b64_tr_b16 a[248:249], v2 offset:40960\n"
    "ds_read_b64_tr_b16 a[250:251], v2 offset:43008\n"
    "v_mfma_f32_16x16x32_bf16 v[172:175], a[252:255], a[236:239], v[172:175]\n"
    "buffer_load_dwordx4 v15, s[48:51], s61 offen offset:1024 lds\n"
    "s_waitcnt lgkmcnt(2)\n"
    "v_mfma_f32_16x16x32_bf16 v[156:159], a[240:243], v[96:99], v[156:159]\n"
    "ds_read_b64_tr_b16 a[252:253], v2 offset:45056\n"
    "ds_read_b64_tr_b16 a[254:255], v2 offset:47104\n"
    "v_mfma_f32_16x16x32_bf16 v[156:159], a[244:247], v[136:139], v[156:159]\n"
    "v_mul_f32 v33, v33, v67\n"
    "v_fma_f32 v33, v53, v77, v33\n"
    "v_mfma_f32_16x16x32_bf16 v[160:163], a[240:243], v[100:103], v[160:163]\n"
    "v_mul_f32 v34, v34, v67\n"
    "v_fma_f32 v34, v54, v77, v34\n"
    "v_mfma_f32_16x16x32_bf16 v[160:163], a[244:247], v[140:143], v[160:163]\n"
    "v_mul_f32 v35, v35, v67\n"
    "v_fma_f32 v35, v55, v77, v35\n"
    "v_mfma_f32_16x16x32_bf16 v[164:167], a[240:243], v[104:107], v[164:167]\n"
    "buffer_load_dwordx4 v16, s[48:51], s61 offen offset:2048 lds\n"
    "v_mfma_f32_16x16x32_bf16 v[164:167], a[244:247], v[144:147], v[164:167]\n"
    "v_cvt_pk_bf16_f32 v30, v32, v33\n"
    "v_cvt_pk_bf16_f32 v31, v34, v35\n"
    "v_mfma_f32_16x16x32_bf16 v[168:171], a[240:243], v[108:111], v[168:171]\n"
    "buffer_store_dwordx4 v[28:31], v1, s[52:55], s81 offen offset:1024 sc1\n"
    "v_mfma_f32_16x16x32_bf16 v[168:171], a[244:247], v[148:151], v[168:171]\n"
    "s_waitcnt vmcnt(9) lgkmcnt(0)\n"
    "s_barrier\n"
    "v_mul_f32 v36, v36, v69\n"
    "v_mfma_f32_16x16x32_bf16 v[172:175], a[240:243], v[112:115], v[172:175]\n"
    "buffer_load_dwordx4 v17, s[48:51], s61 offen offset:3072 lds\n"
    "v_mfma_f32_16x16x32_bf16 v[172:175], a[244:247], v[152:155], v[172:175]\n"
    "ds_read_b64_tr_b16 a[240:241], v3 offset:0\n"
    "ds_read_b64_tr_b16 a[242:243], v3 offset:2048\n"
    "v_mfma_f32_16x16x32_bf16 v[156:159], a[248:251], v[176:179], v[156:159]\n"
    "ds_read_b64_tr_b16 a[244:245], v3 offset:4096\n"
    "ds_read_b64_tr_b16 a[246:247], v3 offset:6144\n"
    "v_mfma_f32_16x16x32_bf16 v[156:159], a[252:255], v[216:219], v[156:159]\n"
    "v_fma_f32 v36, v56, v79, v36\n"
    "v_mul_f32 v37, v37, v69\n"
    "v_mfma_f32_16x16x32_bf16 v[160:163], a[248:251], v[180:183], v[160:163]\n"
    "s_add_u32 m0, s62, 32768\n"
    "s_add_u32 s61, s96, 640\n"
    "v_mfma_f32_16x16x32_bf16 v[160:163], a[252:255], v[220:223], v[160:163]\n"
    "buffer_load_dwordx4 v14, s[48:51], s61 offen lds\n"
    "v_mfma_f32_16x16x32_bf16 v[164:167], a[248:251], v[184:187], v[164:167]\n"
    "v_fma_f32 v37, v57, v79, v37\n"
    "v_mul_f32 v38, v38, v69\n"
    "v_mfma_f32_16x16x32_bf16 v[164:167], a[252:255], v[224:227], v[164:167]\n"
    "v_fma_f32 v38, v58, v79, v38\n"
    "v_mul_f32 v39, v39, v69\n"
    "v_mfma_f32_16x16x32_bf16 v[168:171], a[248:251], v[188:191], v[168:171]\n"
    "v_fma_f32 v39, v59, v79, v39\n"
    "v_cvt_pk_bf16_f32 v92, v36, v37\n"
    "v_mfma_f32_16x16x32_bf16 v[168:171], a[252:255], v[228:231], v[168:171]\n"
    "buffer_load_dwordx4 v15, s[48:51], s61 offen offset:1024 lds\n"
    "v_mfma_f32_16x16x32_bf16 v[172:175], a[248:251], v[192:195], v[172:175]\n"
    "ds_read_b64_tr_b16 a[248:249], v3 offset:8192\n"
    "ds_read_b64_tr_b16 a[250:251], v3 offset:10240\n"
    "v_mfma_f32_16x16x32_bf16 v[172:175], a[252:255], v[232:235], v[172:175]\n"
    "ds_read_b64_tr_b16 a[252:253], v3 offset:12288\n"
    "ds_read_b64_tr_b16 a[254:255], v3 offset:14336\n"
    "s_waitcnt lgkmcnt(4)\n"
    "v_mfma_f32_16x16x32_bf16 v[196:199], a[240:243], a[0:3], 0\n"
    "v_cvt_pk_bf16_f32 v93, v38, v39\n"
    "s_add_u32 s81, s81, 8192\n"
    "v_mfma_f32_16x16x32_bf16 v[196:199], a[244:247], a[20:23], v[196:199]\n"
    "v_mul_f32 v116, v116, v61\n"
    "v_fma_f32 v116, v156, v71, v116\n"
    "v_mfma_f32_16x16x32_bf16 v[200:203], a[240:243], a[4:7], 0\n"
    "buffer_load_dwordx4 v16, s[48:51], s61 offen offset:2048 lds\n"
    "v_mfma_f32_16x16x32_bf16 v[200:203], a[244:247], a[24:27], v[200:203]\n"
    "v_mul_f32 v117, v117, v61\n"
    "v_fma_f32 v117, v157, v71, v117\n"
    "v_mfma_f32_16x16x32_bf16 v[204:207], a[240:243], a[8:11], 0\n"
    "v_mul_f32 v118, v118, v61\n"
    "v_fma_f32 v118, v158, v71, v118\n"
    "v_mfma_f32_16x16x32_bf16 v[204:207], a[244:247], a[28:31], v[204:207]\n"
    "v_mul_f32 v119, v119, v61\n"
    "v_fma_f32 v119, v159, v71, v119\n"
    "v_mfma_f32_16x16x32_bf16 v[208:211], a[240:243], a[12:15], 0\n"
    "buffer_load_dwordx4 v17, s[48:51], s61 offen offset:3072 lds\n"
    "v_mfma_f32_16x16x32_bf16 v[208:211], a[244:247], a[32:35], v[208:211]\n"
    "v_cvt_pk_bf16_f32 v116, v116, v117\n"
    "v_cvt_pk_bf16_f32 v117, v118, v119\n"
    "v_mfma_f32_16x16x32_bf16 v[212:215], a[240:243], a[16:19], 0\n"
    "ds_read_b64_tr_b16 a[240:241], v3 offset:16384\n"
    "ds_read_b64_tr_b16 a[242:243], v3 offset:18432\n"
    "v_mfma_f32_16x16x32_bf16 v[212:215], a[244:247], a[36:39], v[212:215]\n"
    "ds_read_b64_tr_b16 a[244:245], v3 offset:20480\n"
    "ds_read_b64_tr_b16 a[246:247], v3 offset:22528\n"
    "s_waitcnt lgkmcnt(4)\n"
    "v_mfma_f32_16x16x32_bf16 v[196:199], a[248:251], a[40:43], v[196:199]\n"
    "s_add_u32 m0, s62, 36864\n"
    "s_add_u32 s61, s96, 29312\n"
    "v_mfma_f32_16x16x32_bf16 v[196:199], a[252:255], a[60:63], v[196:199]\n"
    "buffer_load_dwordx4 v14, s[48:51], s61 offen lds\n"
    "v_mfma_f32_16x16x32_bf16 v[200:203], a[248:251], a[44:47], v[200:203]\n"
    "v_mul_f32 v120, v120, v63\n"
    "v_fma_f32 v120, v160, v73, v120\n"
    "v_mfma_f32_16x16x32_bf16 v[200:203], a[252:255], a[64:67], v[200:203]\n"
    "v_mul_f32 v121, v121, v63\n"
    "v_fma_f32 v121, v161, v73, v121\n"
    "v_mfma_f32_16x16x32_bf16 v[204:207], a[248:251], a[48:51], v[204:207]\n"
    "v_mul_f32 v122, v122, v63\n"
    "v_fma_f32 v122, v162, v73, v122\n"
    "v_mfma_f32_16x16x32_bf16 v[204:207], a[252:255], a[68:71], v[204:207]\n"
    "buffer_load_dwordx4 v15, s[48:51], s61 offen offset:1024 lds\n"
    "v_mfma_f32_16x16x32_bf16 v[208:211], a[248:251], a[52:55], v[208:211]\n"
    "v_mul_f32 v123, v123, v63\n"
    "v_fma_f32 v123, v163, v73, v123\n"
    "v_mfma_f32_16x16x32_bf16 v[208:211], a[252:255], a[72:75], v[208:211]\n"
    "v_cvt_pk_bf16_f32 v118, v120, v121\n"
    "v_cvt_pk_bf16_f32 v119, v122, v123\n"
    "v_mfma_f32_16x16x32_bf16 v[212:215], a[248:251], a[56:59], v[212:215]\n"
    "ds_read_b64_tr_b16 a[248:249], v3 offset:24576\n"
    "ds_read_b64_tr_b16 a[250:251], v3 offset:26624\n"
    "v_mfma_f32_16x16x32_bf16 v[212:215], a[252:255], a[76:79], v[212:215]\n"
    "ds_read_b64_tr_b16 a[252:253], v3 offset:28672\n"
    "ds_read_b64_tr_b16 a[254:255], v3 offset:30720\n"
    "s_waitcnt lgkmcnt(4)\n"
    "v_mfma_f32_16x16x32_bf16 v[196:199], a[240:243], a[80:83], v[196:199]\n"
    "buffer_load_dwordx4 v16, s[48:51], s61 offen offset:2048 lds\n"
    "v_mfma_f32_16x16x32_bf16 v[196:199], a[244:247], a[100:103], v[196:199]\n"
    "buffer_store_dwordx4 v[116:119], v1, s[52:55], s81 offen offset:0 sc1\n"
    "v_mfma_f32_16x16x32_bf16 v[200:203], a[240:243], a[84:87], v[200:203]\n"
    "v_mul_f32 v124, v124, v65\n"
    "v_fma_f32 v124, v164, v75, v124\n"
    "v_mfma_f32_16x16x32_bf16 v[200:203], a[244:247], a[104:107], v[200:203]\n"
    "v_mul_f32 v125, v125, v65\n"
    "v_fma_f32 v125, v165, v75, v125\n"
    "v_mfma_f32_16x16x32_bf16 v[204:207], a[240:243], a[88:91], v[204:207]\n"
    "buffer_load_dwordx4 v17, s[48:51], s61 offen offset:3072 lds\n"
    "v_mfma_f32_16x16x32_bf16 v[204:207], a[244:247], a[108:111], v[204:207]\n"
    "v_mul_f32 v126, v126, v65\n"
    "v_fma_f32 v126, v166, v75, v126\n"
    "v_mfma_f32_16x16x32_bf16 v[208:211], a[240:243], a[92:95], v[208:211]\n"
    "v_mul_f32 v127, v127, v65\n"
    "v_fma_f32 v127, v167, v75, v127\n"
    "v_mfma_f32_16x16x32_bf16 v[208:211], a[244:247], a[112:115], v[208:211]\n"
    "s_waitcnt vmcnt(9) lgkmcnt(0)\n"
    "s_barrier\n"
    "v_cvt_pk_bf16_f32 v124, v124, v125\n"
    "v_mfma_f32_16x16x32_bf16 v[212:215], a[240:243], a[96:99], v[212:215]\n"
    "s_add_u32 m0, s62, 65536\n"
    "s_add_u32 s61, s96, 230016\n"
    "v_mfma_f32_16x16x32_bf16 v[212:215], a[244:247], a[116:119], v[212:215]\n"
    "buffer_load_dwordx4 v14, s[48:51], s61 offen lds\n"
    "v_mfma_f32_16x16x32_bf16 v[196:199], a[248:251], a[120:123], v[196:199]\n"
    "ds_read_b64_tr_b16 a[240:241], v2 offset:16384\n"
    "ds_read_b64_tr_b16 a[242:243], v2 offset:18432\n"
    "v_mfma_f32_16x16x32_bf16 v[196:199], a[252:255], a[140:143], v[196:199]\n"
    "ds_read_b64_tr_b16 a[244:245], v2 offset:20480\n"
    "ds_read_b64_tr_b16 a[246:247], v2 offset:22528\n"
    "v_mfma_f32_16x16x32_bf16 v[200:203], a[248:251], a[124:127], v[200:203]\n"
    "v_cvt_pk_bf16_f32 v125, v126, v127\n"
    "v_mul_f32 v128, v128, v67\n"
    "v_mfma_f32_16x16x32_bf16 v[200:203], a[252:255], a[144:147], v[200:203]\n"
    "buffer_load_dwordx4 v15, s[48:51], s61 offen offset:1024 lds\n"
    "v_mfma_f32_16x16x32_bf16 v[204:207], a[248:251], a[128:131], v[204:207]\n"
    "v_fma_f32 v128, v168, v77, v128\n"
    "v_mul_f32 v129, v129, v67\n"
    "v_mfma_f32_16x16x32_bf16 v[204:207], a[252:255], a[148:151], v[204:207]\n"
    "v_fma_f32 v129, v169, v77, v129\n"
    "v_mul_f32 v130, v130, v67\n"
    "v_mfma_f32_16x16x32_bf16 v[208:211], a[248:251], a[132:135], v[208:211]\n"
    "v_fma_f32 v130, v170, v77, v130\n"
    "v_mul_f32 v131, v131, v67\n"
    "v_mfma_f32_16x16x32_bf16 v[208:211], a[252:255], a[152:155], v[208:211]\n"
    "v_fma_f32 v131, v171, v77, v131\n"
    "v_cvt_pk_bf16_f32 v126, v128, v129\n"
    "v_mfma_f32_16x16x32_bf16 v[212:215], a[248:251], a[136:139], v[212:215]\n"
    "buffer_load_dwordx4 v16, s[48:51], s61 offen offset:2048 lds\n"
    "v_mfma_f32_16x16x32_bf16 v[212:215], a[252:255], a[156:159], v[212:215]\n"
    "ds_read_b64_tr_b16 a[248:249], v2 offset:24576\n"
    "ds_read_b64_tr_b16 a[250:251], v2 offset:26624\n"
    "s_waitcnt lgkmcnt(4)\n"
    "v_mfma_f32_16x16x32_bf16 v[236:239], a[240:243], a[160:163], 0\n"
    "ds_read_b64_tr_b16 a[252:253], v2 offset:28672\n"
    "ds_read_b64_tr_b16 a[254:255], v2 offset:30720\n"
    "s_waitcnt lgkmcnt(4)\n"
    "v_mfma_f32_16x16x32_bf16 v[236:239], a[244:247], a[180:183], v[236:239]\n"
    "v_cvt_pk_bf16_f32 v127, v130, v131\n"
    "s_lshl_b32 s79, s57, 2\n"
    "v_mfma_f32_16x16x32_bf16 v[240:243], a[240:243], a[164:167], 0\n"
    "buffer_load_dwordx4 v17, s[48:51], s61 offen offset:3072 lds\n"
    "v_mfma_f32_16x16x32_bf16 v[240:243], a[244:247], a[184:187], v[240:243]\n"
    "buffer_store_dwordx4 v[124:127], v1, s[52:55], s81 offen offset:1024 sc1\n"
    "v_mfma_f32_16x16x32_bf16 v[244:247], a[240:243], a[168:171], 0\n"
    "v_mul_f32 v132, v132, v69\n"
    "v_fma_f32 v132, v172, v79, v132\n"
    "v_mfma_f32_16x16x32_bf16 v[244:247], a[244:247], a[188:191], v[244:247]\n"
    "v_mul_f32 v133, v133, v69\n"
    "v_fma_f32 v133, v173, v79, v133\n"
    "v_mfma_f32_16x16x32_bf16 v[248:251], a[240:243], a[172:175], 0\n"
    "s_add_u32 m0, s62, 69632\n"
    "s_add_u32 s61, s96, 258688\n"
    "v_mfma_f32_16x16x32_bf16 v[248:251], a[244:247], a[192:195], v[248:251]\n"
    "buffer_load_dwordx4 v14, s[48:51], s61 offen lds\n"
    "v_mfma_f32_16x16x32_bf16 v[252:255], a[240:243], a[176:179], 0\n"
    "ds_read_b64_tr_b16 a[240:241], v2 offset:49152\n"
    "ds_read_b64_tr_b16 a[242:243], v2 offset:51200\n"
    "v_mfma_f32_16x16x32_bf16 v[252:255], a[244:247], a[196:199], v[252:255]\n"
    "ds_read_b64_tr_b16 a[244:245], v2 offset:53248\n"
    "ds_read_b64_tr_b16 a[246:247], v2 offset:55296\n"
    "s_waitcnt lgkmcnt(4)\n"
    "v_mfma_f32_16x16x32_bf16 v[236:239], a[248:251], a[200:203], v[236:239]\n"
    "v_mul_f32 v134, v134, v69\n"
    "v_fma_f32 v134, v174, v79, v134\n"
    "v_mfma_f32_16x16x32_bf16 v[236:239], a[252:255], a[220:223], v[236:239]\n"
    "buffer_load_dwordx4 v15, s[48:51], s61 offen offset:1024 lds\n"
    "v_mfma_f32_16x16x32_bf16 v[240:243], a[248:251], a[204:207], v[240:243]\n"
    "v_mul_f32 v135, v135, v69\n"
    "v_fma_f32 v135, v175, v79, v135\n"
    "v_mfma_f32_16x16x32_bf16 v[240:243], a[252:255], a[224:227], v[240:243]\n"
    "v_cvt_pk_bf16_f32 v94, v132, v133\n"
    "v_cvt_pk_bf16_f32 v95, v134, v135\n"
    "v_mfma_f32_16x16x32_bf16 v[244:247], a[248:251], a[208:211], v[244:247]\n"
    "buffer_store_dwordx4 v[92:95], v1, s[52:55], s84 offen sc1\n"
    "v_mfma_f32_16x16x32_bf16 v[244:247], a[252:255], a[228:231], v[244:247]\n"
    "s_add_u32 s84, s84, 20480\n"
    "s_add_u32 s81, s81, 12288\n"
    "v_mfma_f32_16x16x32_bf16 v[248:251], a[248:251], a[212:215], v[248:251]\n"
    "buffer_load_dwordx4 v16, s[48:51], s61 offen offset:2048 lds\n"
    "v_mfma_f32_16x16x32_bf16 v[248:251], a[252:255], a[232:235], v[248:251]\n"
    "v_cmp_ne_u32_e64 s[58:59], s79, v84\n"
    "s_and_b32 s92, s57, 1\n"
    "v_mfma_f32_16x16x32_bf16 v[252:255], a[248:251], a[216:219], v[252:255]\n"
    "ds_read_b64_tr_b16 a[248:249], v2 offset:57344\n"
    "ds_read_b64_tr_b16 a[250:251], v2 offset:59392\n"
    "v_mfma_f32_16x16x32_bf16 v[252:255], a[252:255], a[236:239], v[252:255]\n"
    "ds_read_b64_tr_b16 a[252:253], v2 offset:61440\n"
    "ds_read_b64_tr_b16 a[254:255], v2 offset:63488\n"
    "s_waitcnt lgkmcnt(4)\n"
    "v_mfma_f32_16x16x32_bf16 v[236:239], a[240:243], v[96:99], v[236:239]\n"
    "buffer_load_dwordx4 v17, s[48:51], s61 offen offset:3072 lds\n"
    "v_mfma_f32_16x16x32_bf16 v[236:239], a[244:247], v[136:139], v[236:239]\n"
    "s_lshl_b32 s92, s92, 1\n"
    "s_add_u32 s92, s92, s65\n"
    "v_mfma_f32_16x16x32_bf16 v[240:243], a[240:243], v[100:103], v[240:243]\n"
    "s_mov_b32 s79, 0x9080100\n"
    "s_mov_b32 s80, 0x1d1c1514\n"
    "v_mfma_f32_16x16x32_bf16 v[240:243], a[244:247], v[140:143], v[240:243]\n"
    "s_cmp_ge_u32 %2, 2\n"
    "s_cmov_b32 s79, 0x31302928\n"
    "s_cmov_b32 s80, 0x44383d3c\n"
    "v_mfma_f32_16x16x32_bf16 v[244:247], a[240:243], v[104:107], v[244:247]\n"
    "s_add_u32 m0, s70, 0\n"
    "s_add_u32 s61, s96, 768\n"
    "v_mfma_f32_16x16x32_bf16 v[244:247], a[244:247], v[144:147], v[244:247]\n"
    "buffer_load_dwordx4 v14, s[48:51], s61 offen lds\n"
    "v_mfma_f32_16x16x32_bf16 v[248:251], a[240:243], v[108:111], v[248:251]\n"
    "s_cmp_ge_u32 s57, 8\n"
    "s_cselect_b32 s79, s80, s79\n"
    "v_mfma_f32_16x16x32_bf16 v[248:251], a[244:247], v[148:151], v[248:251]\n"
    "s_waitcnt vmcnt(11) lgkmcnt(0)\n"
    "s_barrier\n"
    "s_and_b32 s80, s57, 6\n"
    "v_mfma_f32_16x16x32_bf16 v[252:255], a[240:243], v[112:115], v[252:255]\n"
    "ds_read_b64_tr_b16 a[240:241], v3 offset:32768\n"
    "ds_read_b64_tr_b16 a[242:243], v3 offset:34816\n"
    "v_mfma_f32_16x16x32_bf16 v[252:255], a[244:247], v[152:155], v[252:255]\n"
    "buffer_load_dwordx4 v15, s[48:51], s61 offen offset:1024 lds\n"
    "v_mfma_f32_16x16x32_bf16 v[236:239], a[248:251], v[176:179], v[236:239]\n"
    "ds_read_b64_tr_b16 a[244:245], v3 offset:36864\n"
    "ds_read_b64_tr_b16 a[246:247], v3 offset:38912\n"
    "v_mfma_f32_16x16x32_bf16 v[236:239], a[252:255], v[216:219], v[236:239]\n"
    "s_lshl_b32 s80, s80, 2\n"
    "s_lshr_b32 s79, s79, s80\n"
    "v_mfma_f32_16x16x32_bf16 v[240:243], a[248:251], v[180:183], v[240:243]\n"
    "s_and_b32 s79, s79, 0xff\n"
    "s_add_u32 s79, s79, s92\n"
    "v_mfma_f32_16x16x32_bf16 v[240:243], a[252:255], v[220:223], v[240:243]\n"
    "s_lshl_b32 s80, s64, 4\n"
    "s_add_u32 s80, s80, s57\n"
    "v_mfma_f32_16x16x32_bf16 v[244:247], a[248:251], v[184:187], v[244:247]\n"
    "buffer_load_dwordx4 v16, s[48:51], s61 offen offset:2048 lds\n"
    "v_mfma_f32_16x16x32_bf16 v[244:247], a[252:255], v[224:227], v[244:247]\n"
    "s_cmp_ge_u32 s80, 28\n"
    "s_cmov_b32 s92, 0\n"
    "v_mfma_f32_16x16x32_bf16 v[248:251], a[248:251], v[188:191], v[248:251]\n"
    "s_add_u32 s79, s79, s92\n"
    "s_lshl_b32 s88, s79, 10\n"
    "v_mfma_f32_16x16x32_bf16 v[248:251], a[252:255], v[228:231], v[248:251]\n"
    "s_mul_i32 s80, s79, 205\n"
    "s_lshr_b32 s80, s80, 12\n"
    "v_mfma_f32_16x16x32_bf16 v[252:255], a[248:251], v[192:195], v[252:255]\n"
    "buffer_load_dwordx4 v17, s[48:51], s61 offen offset:3072 lds\n"
    "v_mfma_f32_16x16x32_bf16 v[252:255], a[252:255], v[232:235], v[252:255]\n"
    "ds_read_b64_tr_b16 a[248:249], v3 offset:40960\n"
    "ds_read_b64_tr_b16 a[250:251], v3 offset:43008\n"
    "s_waitcnt lgkmcnt(2)\n"
    "v_mfma_f32_16x16x32_bf16 v[20:23], a[240:243], a[0:3], 0\n"
    "ds_read_b64_tr_b16 a[252:253], v3 offset:45056\n"
    "ds_read_b64_tr_b16 a[254:255], v3 offset:47104\n"
    "v_mfma_f32_16x16x32_bf16 v[20:23], a[244:247], a[20:23], v[20:23]\n"
    "s_mul_i32 s63, s80, 20\n"
    "s_sub_u32 s63, s79, s63\n"
    "v_mfma_f32_16x16x32_bf16 v[24:27], a[240:243], a[4:7], 0\n"
    "s_add_u32 m0, s70, 4096\n"
    "s_add_u32 s61, s96, 29440\n"
    "v_mfma_f32_16x16x32_bf16 v[24:27], a[244:247], a[24:27], v[24:27]\n"
    "buffer_load_dwordx4 v14, s[48:51], s61 offen lds\n"
    "v_mfma_f32_16x16x32_bf16 v[28:31], a[240:243], a[8:11], 0\n"
    "s_lshl_b32 s90, s80, 8\n"
    "v_mul_f32 v196, v196, v61\n"
    "v_mfma_f32_16x16x32_bf16 v[28:31], a[244:247], a[28:31], v[28:31]\n"
    "s_cmp_ge_u32 s79, 72\n"
    "s_cmov_b32 s88, 0x7f000000\n"
    "s_cmov_b32 s90, 0x7f000000\n"
    "v_mfma_f32_16x16x32_bf16 v[32:35], a[240:243], a[12:15], 0\n"
    "s_cmp_ge_u32 s79, 68\n"
    "s_cselect_b32 s92, 2, 1\n"
    "s_cselect_b32 s79, 8, 0\n"
    "v_mfma_f32_16x16x32_bf16 v[32:35], a[244:247], a[32:35], v[32:35]\n"
    "buffer_load_dwordx4 v15, s[48:51], s61 offen offset:1024 lds\n"
    "v_mfma_f32_16x16x32_bf16 v[36:39], a[240:243], a[16:19], 0\n"
    "ds_read_b64_tr_b16 a[240:241], v3 offset:49152\n"
    "ds_read_b64_tr_b16 a[242:243], v3 offset:51200\n"
    "v_mfma_f32_16x16x32_bf16 v[36:39], a[244:247], a[36:39], v[36:39]\n"
    "ds_read_b64_tr_b16 a[244:245], v3 offset:53248\n"
    "ds_read_b64_tr_b16 a[246:247], v3 offset:55296\n"
    "s_waitcnt lgkmcnt(4)\n"
    "v_mfma_f32_16x16x32_bf16 v[20:23], a[248:251], a[40:43], v[20:23]\n"
    "s_add_u32 s63, s63, s79\n"
    "s_and_b32 s79, s63, 1\n"
    "v_mfma_f32_16x16x32_bf16 v[20:23], a[252:255], a[60:63], v[20:23]\n"
    "s_mul_i32 s79, s79, 28672\n"
    "s_lshr_b32 s80, s63, 1\n"
    "v_mfma_f32_16x16x32_bf16 v[24:27], a[248:251], a[44:47], v[24:27]\n"
    "buffer_load_dwordx4 v16, s[48:51], s61 offen offset:2048 lds\n"
    "v_mfma_f32_16x16x32_bf16 v[24:27], a[252:255], a[64:67], v[24:27]\n"
    "s_lshl_b32 s80, s80, 5\n"
    "s_add_u32 s79, s79, s80\n"
    "v_mfma_f32_16x16x32_bf16 v[28:31], a[248:251], a[48:51], v[28:31]\n"
    "s_lshl_b32 s80, s63, 5\n"
    "s_add_u32 s80, s80, 56832\n"
    "v_mfma_f32_16x16x32_bf16 v[28:31], a[252:255], a[68:71], v[28:31]\n"
    "s_cmp_lt_u32 s63, 16\n"
    "s_cselect_b32 s79, s79, s80\n"
    "s_cmov_b32 s92, 0\n"
    "v_mfma_f32_16x16x32_bf16 v[32:35], a[248:251], a[52:55], v[32:35]\n"
    "buffer_load_dwordx4 v17, s[48:51], s61 offen offset:3072 lds\n"
    "v_mfma_f32_16x16x32_bf16 v[32:35], a[252:255], a[72:75], v[32:35]\n"
    "s_add_u32 s90, s90, s79\n"
    "s_and_b32 s93, s57, 1\n"
    "v_mfma_f32_16x16x32_bf16 v[36:39], a[248:251], a[56:59], v[36:39]\n"
    "s_lshl_b32 s93, s93, 1\n"
    "s_add_u32 s93, s93, s65\n"
    "v_mfma_f32_16x16x32_bf16 v[36:39], a[252:255], a[76:79], v[36:39]\n"
    "s_lshr_b32 s79, s57, 1\n"
    "s_mul_i32 s79, s79, 20\n"
    "s_waitcnt lgkmcnt(0)\n"
    "v_mfma_f32_16x16x32_bf16 v[20:23], a[240:243], a[80:83], v[20:23]\n"
    "s_add_u32 m0, s62, 0\n"
    "s_add_u32 s61, s96, 230144\n"
    "s_add_u32 s79, s79, 16\n"
    "s_add_u32 s79, s79, s93\n"
    "s_lshl_b32 s89, s79, 10\n"
    "s_mul_i32 s80, s79, 205\n"
    "s_lshr_b32 s80, s80, 12\n"
    "s_mul_i32 s63, s80, 20\n"
    "s_sub_u32 s63, s79, s63\n"
    "s_lshl_b32 s91, s80, 8\n"
    "s_cmp_ge_u32 s79, 40\n"
    "s_cmov_b32 s89, 0x7f000000\n"
    "s_cmov_b32 s91, 0x7f000000\n"
    "s_cmp_ge_u32 s79, 68\n"
    "s_cselect_b32 s93, 2, 1\n"
    "s_cselect_b32 s79, 8, 0\n"
    "s_add_u32 s63, s63, s79\n"
    "s_and_b32 s79, s63, 1\n"
    "s_mul_i32 s79, s79, 28672\n"
    "s_lshr_b32 s80, s63, 1\n"
    "s_lshl_b32 s80, s80, 5\n"
    "s_add_u32 s79, s79, s80\n"
    "s_lshl_b32 s80, s63, 5\n"
    "s_add_u32 s80, s80, 56832\n"
    "s_cmp_lt_u32 s63, 16\n"
    "s_cselect_b32 s79, s79, s80\n"
    "s_cmov_b32 s93, 0\n"
    "s_add_u32 s91, s91, s79\n"
    "v_and_b32 v88, 15, v0\n"
    "v_lshrrev_b32 v89, 4, v0\n"
    "v_and_b32 v90, 1, v89\n"
    "v_lshrrev_b32 v91, 1, v89\n"
    "v_mul_u32_u24 v81, 896, v88\n"
    "v_lshl_add_u32 v81, v91, 3, v81\n"
    "v_mul_u32_u24 v80, 14336, v90\n"
    "v_add_u32 v80, v80, v81\n"
    "v_lshl_add_u32 v82, v90, 30, v81\n"
    "v_lshl_add_u32 v81, v90, 7, v81\n"
    "v_mfma_f32_16x16x32_bf16 v[20:23], a[244:247], a[100:103], v[20:23]\n"
    "buffer_load_dwordx4 v14, s[48:51], s61 offen lds\n"
    "v_mfma_f32_16x16x32_bf16 v[24:27], a[240:243], a[84:87], v[24:27]\n"
    "ds_read_b64_tr_b16 a[248:249], v3 offset:57344\n"
    "ds_read_b64_tr_b16 a[250:251], v3 offset:59392\n"
    "v_mfma_f32_16x16x32_bf16 v[24:27], a[244:247], a[104:107], v[24:27]\n"
    "ds_read_b64_tr_b16 a[252:253], v3 offset:61440\n"
    "ds_read_b64_tr_b16 a[254:255], v3 offset:63488\n"
    "v_mfma_f32_16x16x32_bf16 v[28:31], a[240:243], a[88:91], v[28:31]\n"
    "v_fma_f32 v196, v236, v71, v196\n"
    "v_mul_f32 v197, v197, v61\n"
    "v_mfma_f32_16x16x32_bf16 v[28:31], a[244:247], a[108:111], v[28:31]\n"
    "buffer_load_dwordx4 v15, s[48:51], s61 offen offset:1024 lds\n"
    "v_mfma_f32_16x16x32_bf16 v[32:35], a[240:243], a[92:95], v[32:35]\n"
    "v_fma_f32 v197, v237, v71, v197\n"
    "v_mul_f32 v198, v198, v61\n"
    "v_mfma_f32_16x16x32_bf16 v[32:35], a[244:247], a[112:115], v[32:35]\n"
    "s_waitcnt vmcnt(10) lgkmcnt(0)\n"
    "s_barrier\n"
    "v_fma_f32 v198, v238, v71, v198\n"
    "v_mfma_f32_16x16x32_bf16 v[36:39], a[240:243], a[96:99], v[36:39]\n"
    "ds_read_b64_tr_b16 a[240:241], v5 offset:0\n"
    "ds_read_b64_tr_b16 a[242:243], v5 offset:2048\n"
    "v_mfma_f32_16x16x32_bf16 v[36:39], a[244:247], a[116:119], v[36:39]\n"
    "s_cmp_lg_u32 %2, 0\n"
    "s_cbranch_scc1 NEA_%=\n"
    "buffer_store_dword v83, v85, s[52:55], s67 offen offset:1024 sc1\n"
    "NEA_%=:\n"
    "v_mfma_f32_16x16x32_bf16 v[20:23], a[248:251], a[120:123], v[20:23]\n"
    "buffer_load_dwordx4 v16, s[48:51], s61 offen offset:2048 lds\n"
    "v_mfma_f32_16x16x32_bf16 v[20:23], a[252:255], a[140:143], v[20:23]\n"
    "ds_read_b64_tr_b16 a[244:245], v5 offset:4096\n"
    "ds_read_b64_tr_b16 a[246:247], v5 offset:6144\n"
    "v_mfma_f32_16x16x32_bf16 v[24:27], a[248:251], a[124:127], v[24:27]\n"
    "v_and_b32 v90, 1, v88\n"
    "v_lshlrev_b32 v90, 4, v90\n"
    "v_mfma_f32_16x16x32_bf16 v[24:27], a[252:255], a[144:147], v[24:27]\n"
    "s_movk_i32 s79, 0x3f80\n"
    "v_lshlrev_b32_e64 v90, v90, s79\n"
    "v_mfma_f32_16x16x32_bf16 v[28:31], a[248:251], a[128:131], v[28:31]\n"
    "buffer_load_dwordx4 v17, s[48:51], s61 offen offset:3072 lds\n"
    "v_mfma_f32_16x16x32_bf16 v[28:31], a[252:255], a[148:151], v[28:31]\n"
    "v_lshrrev_b32 v91, 3, v88\n"
    "v_bfe_u32 v88, v88, 1, 2\n"
    "v_mfma_f32_16x16x32_bf16 v[32:35], a[248:251], a[132:135], v[32:35]\n"
    "v_lshl_add_u32 v88, v91, 2, v88\n"
    "v_lshlrev_b32 v89, 2, v89\n"
    "v_sub_u32 v88, v89, v88\n"
    "v_mfma_f32_16x16x32_bf16 v[32:35], a[252:255], a[152:155], v[32:35]\n"
    "v_mul_f32 v199, v199, v61\n"
    "v_fma_f32 v199, v239, v71, v199\n"
    "v_mfma_f32_16x16x32_bf16 v[36:39], a[248:251], a[136:139], v[36:39]\n"
    "s_add_u32 m0, s62, 4096\n"
    "s_add_u32 s61, s96, 258816\n"
    "v_mfma_f32_16x16x32_bf16 v[36:39], a[252:255], a[156:159], v[36:39]\n"
    "buffer_load_dwordx4 v14, s[48:51], s61 offen lds\n"
    "s_waitcnt lgkmcnt(1)\n"
    "v_mfma_f32_16x16x32_bf16 v[40:43], a[240:243], a[160:163], 0\n"
    "ds_read_b64_tr_b16 a[248:249], v5 offset:8192\n"
    "ds_read_b64_tr_b16 a[250:251], v5 offset:10240\n"
    "s_waitcnt lgkmcnt(2)\n"
    "v_mfma_f32_16x16x32_bf16 v[40:43], a[244:247], a[180:183], v[40:43]\n"
    "ds_read_b64_tr_b16 a[252:253], v5 offset:12288\n"
    "ds_read_b64_tr_b16 a[254:255], v5 offset:14336\n"
    "v_mfma_f32_16x16x32_bf16 v[44:47], a[240:243], a[164:167], 0\n"
    "v_cvt_pk_bf16_f32 v196, v196, v197\n"
    "v_cvt_pk_bf16_f32 v197, v198, v199\n"
    "v_mfma_f32_16x16x32_bf16 v[44:47], a[244:247], a[184:187], v[44:47]\n"
    "buffer_load_dwordx4 v15, s[48:51], s61 offen offset:1024 lds\n"
    "v_mfma_f32_16x16x32_bf16 v[48:51], a[240:243], a[168:171], 0\n"
    "v_mul_f32 v200, v200, v63\n"
    "v_fma_f32 v200, v240, v73, v200\n"
    "v_mfma_f32_16x16x32_bf16 v[48:51], a[244:247], a[188:191], v[48:51]\n"
    "v_mul_f32 v201, v201, v63\n"
    "v_fma_f32 v201, v241, v73, v201\n"
    "v_mfma_f32_16x16x32_bf16 v[52:55], a[240:243], a[172:175], 0\n"
    "v_mul_f32 v202, v202, v63\n"
    "v_fma_f32 v202, v242, v73, v202\n"
    "v_mfma_f32_16x16x32_bf16 v[52:55], a[244:247], a[192:195], v[52:55]\n"
    "v_mul_f32 v203, v203, v63\n"
    "v_fma_f32 v203, v243, v73, v203\n"
    "v_mfma_f32_16x16x32_bf16 v[56:59], a[240:243], a[176:179], 0\n"
    "buffer_load_dwordx4 v16, s[48:51], s61 offen offset:2048 lds\n"
    "v_mfma_f32_16x16x32_bf16 v[56:59], a[244:247], a[196:199], v[56:59]\n"
    "ds_read_b64_tr_b16 a[240:241], v5 offset:16384\n"
    "ds_read_b64_tr_b16 a[242:243], v5 offset:18432\n"
    "s_waitcnt lgkmcnt(4)\n"
    "v_mfma_f32_16x16x32_bf16 v[40:43], a[248:251], a[200:203], v[40:43]\n"
    "ds_read_b64_tr_b16 a[244:245], v5 offset:20480\n"
    "ds_read_b64_tr_b16 a[246:247], v5 offset:22528\n"
    "s_waitcnt lgkmcnt(4)\n"
    "v_mfma_f32_16x16x32_bf16 v[40:43], a[252:255], a[220:223], v[40:43]\n"
    "v_cvt_pk_bf16_f32 v198, v200, v201\n"
    "v_cvt_pk_bf16_f32 v199, v202, v203\n"
    "v_mfma_f32_16x16x32_bf16 v[44:47], a[248:251], a[204:207], v[44:47]\n"
    "buffer_load_dwordx4 v17, s[48:51], s61 offen offset:3072 lds\n"
    "v_mfma_f32_16x16x32_bf16 v[44:47], a[252:255], a[224:227], v[44:47]\n"
    "buffer_store_dwordx4 v[196:199], v1, s[52:55], s81 offen offset:0 sc1\n"
    "v_mfma_f32_16x16x32_bf16 v[48:51], a[248:251], a[208:211], v[48:51]\n"
    "v_mul_f32 v204, v204, v65\n"
    "v_fma_f32 v204, v244, v75, v204\n"
    "v_mfma_f32_16x16x32_bf16 v[48:51], a[252:255], a[228:231], v[48:51]\n"
    "v_mul_f32 v205, v205, v65\n"
    "v_fma_f32 v205, v245, v75, v205\n"
    "v_mfma_f32_16x16x32_bf16 v[52:55], a[248:251], a[212:215], v[52:55]\n"
    "v_mul_f32 v206, v206, v65\n"
    "v_fma_f32 v206, v246, v75, v206\n"
    "v_mfma_f32_16x16x32_bf16 v[52:55], a[252:255], a[232:235], v[52:55]\n"
    "v_mul_f32 v207, v207, v65\n"
    "v_fma_f32 v207, v247, v75, v207\n"
    "v_mfma_f32_16x16x32_bf16 v[56:59], a[248:251], a[216:219], v[56:59]\n"
    "ds_read_b64_tr_b16 a[248:249], v5 offset:24576\n"
    "ds_read_b64_tr_b16 a[250:251], v5 offset:26624\n"
    "v_mfma_f32_16x16x32_bf16 v[56:59], a[252:255], a[236:239], v[56:59]\n"
    "ds_read_b64_tr_b16 a[252:253], v5 offset:28672\n"
    "ds_read_b64_tr_b16 a[254:255], v5 offset:30720\n"
    "s_waitcnt lgkmcnt(4)\n"
    "v_mfma_f32_16x16x32_bf16 v[40:43], a[240:243], v[96:99], v[40:43]\n"
    "v_cvt_pk_bf16_f32 v204, v204, v205\n"
    "v_cvt_pk_bf16_f32 v205, v206, v207\n"
    "v_mfma_f32_16x16x32_bf16 v[40:43], a[244:247], v[136:139], v[40:43]\n"
    "v_mul_f32 v208, v208, v67\n"
    "v_fma_f32 v208, v248, v77, v208\n"
    "v_mfma_f32_16x16x32_bf16 v[44:47], a[240:243], v[100:103], v[44:47]\n"
    "v_mul_f32 v209, v209, v67\n"
    "v_fma_f32 v209, v249, v77, v209\n"
    "v_mfma_f32_16x16x32_bf16 v[44:47], a[244:247], v[140:143], v[44:47]\n"
    "v_mul_f32 v210, v210, v67\n"
    "v_fma_f32 v210, v250, v77, v210\n"
    "v_mfma_f32_16x16x32_bf16 v[48:51], a[240:243], v[104:107], v[48:51]\n"
    "v_mul_f32 v211, v211, v67\n"
    "v_fma_f32 v211, v251, v77, v211\n"
    "v_mfma_f32_16x16x32_bf16 v[48:51], a[244:247], v[144:147], v[48:51]\n"
    "v_cvt_pk_bf16_f32 v206, v208, v209\n"
    "v_cvt_pk_bf16_f32 v207, v210, v211\n"
    "v_mfma_f32_16x16x32_bf16 v[52:55], a[240:243], v[108:111], v[52:55]\n"
    "buffer_store_dwordx4 v[204:207], v1, s[52:55], s81 offen offset:1024 sc1\n"
    "v_mfma_f32_16x16x32_bf16 v[52:55], a[244:247], v[148:151], v[52:55]\n"
    "s_waitcnt vmcnt(10) lgkmcnt(0)\n"
    "s_barrier\n"
    "v_mul_f32 v212, v212, v69\n"
    "v_mfma_f32_16x16x32_bf16 v[56:59], a[240:243], v[112:115], v[56:59]\n"
    "ds_read_b64_tr_b16 a[240:241], v2 offset:0\n"
    "ds_read_b64_tr_b16 a[242:243], v2 offset:2048\n"
    "v_mfma_f32_16x16x32_bf16 v[56:59], a[244:247], v[152:155], v[56:59]\n"
    "ds_read_b64_tr_b16 a[244:245], v2 offset:4096\n"
    "ds_read_b64_tr_b16 a[246:247], v2 offset:6144\n"
    "v_mfma_f32_16x16x32_bf16 v[40:43], a[248:251], v[176:179], v[40:43]\n"
    "v_fma_f32 v212, v252, v79, v212\n"
    "v_mul_f32 v213, v213, v69\n"
    "v_mfma_f32_16x16x32_bf16 v[40:43], a[252:255], v[216:219], v[40:43]\n"
    "v_fma_f32 v213, v253, v79, v213\n"
    "v_mul_f32 v214, v214, v69\n"
    "v_mfma_f32_16x16x32_bf16 v[44:47], a[248:251], v[180:183], v[44:47]\n"
    "v_fma_f32 v214, v254, v79, v214\n"
    "v_mul_f32 v215, v215, v69\n"
    "v_mfma_f32_16x16x32_bf16 v[44:47], a[252:255], v[220:223], v[44:47]\n"
    "v_fma_f32 v215, v255, v79, v215\n"
    "v_cvt_pk_bf16_f32 v92, v212, v213\n"
    "v_mfma_f32_16x16x32_bf16 v[48:51], a[248:251], v[184:187], v[48:51]\n"
    "v_cvt_pk_bf16_f32 v93, v214, v215\n"
    "s_add_u32 s81, s81, 8192\n"
    "v_mfma_f32_16x16x32_bf16 v[48:51], a[252:255], v[224:227], v[48:51]\n"
    "v_mfma_f32_16x16x32_bf16 v[52:55], a[248:251], v[188:191], v[52:55]\n"
    "v_mfma_f32_16x16x32_bf16 v[52:55], a[252:255], v[228:231], v[52:55]\n"
    "v_mfma_f32_16x16x32_bf16 v[56:59], a[248:251], v[192:195], v[56:59]\n"
    "ds_read_b64_tr_b16 a[248:249], v2 offset:8192\n"
    "ds_read_b64_tr_b16 a[250:251], v2 offset:10240\n"
    "v_mfma_f32_16x16x32_bf16 v[56:59], a[252:255], v[232:235], v[56:59]\n"
    "ds_read_b64_tr_b16 a[252:253], v2 offset:12288\n"
    "ds_read_b64_tr_b16 a[254:255], v2 offset:14336\n"
    "s_waitcnt lgkmcnt(4)\n"
    "v_mfma_f32_16x16x32_bf16 v[116:119], a[240:243], a[0:3], 0\n"
    "v_mul_f32 v20, v20, v61\n"
    "v_fma_f32 v20, v40, v71, v20\n"
    "v_mfma_f32_16x16x32_bf16 v[116:119], a[244:247], a[20:23], v[116:119]\n"
    "v_mul_f32 v21, v21, v61\n"
    "v_fma_f32 v21, v41, v71, v21\n"
    "v_mfma_f32_16x16x32_bf16 v[120:123], a[240:243], a[4:7], 0\n"
    "v_mul_f32 v22, v22, v61\n"
    "v_fma_f32 v22, v42, v71, v22\n"
    "v_mfma_f32_16x16x32_bf16 v[120:123], a[244:247], a[24:27], v[120:123]\n"
    "v_mul_f32 v23, v23, v61\n"
    "v_fma_f32 v23, v43, v71, v23\n"
    "v_mfma_f32_16x16x32_bf16 v[124:127], a[240:243], a[8:11], 0\n"
    "v_cvt_pk_bf16_f32 v20, v20, v21\n"
    "v_cvt_pk_bf16_f32 v21, v22, v23\n"
    "v_mfma_f32_16x16x32_bf16 v[124:127], a[244:247], a[28:31], v[124:127]\n"
    "v_mul_f32 v24, v24, v63\n"
    "v_fma_f32 v24, v44, v73, v24\n"
    "v_mfma_f32_16x16x32_bf16 v[128:131], a[240:243], a[12:15], 0\n"
    "v_mul_f32 v25, v25, v63\n"
    "v_fma_f32 v25, v45, v73, v25\n"
    "v_mfma_f32_16x16x32_bf16 v[128:131], a[244:247], a[32:35], v[128:131]\n"
    "v_mul_f32 v26, v26, v63\n"
    "v_fma_f32 v26, v46, v73, v26\n"
    "v_mfma_f32_16x16x32_bf16 v[132:135], a[240:243], a[16:19], 0\n"
    "ds_read_b64_tr_b16 a[240:241], v2 offset:32768\n"
    "ds_read_b64_tr_b16 a[242:243], v2 offset:34816\n"
    "v_mfma_f32_16x16x32_bf16 v[132:135], a[244:247], a[36:39], v[132:135]\n"
    "ds_read_b64_tr_b16 a[244:245], v2 offset:36864\n"
    "ds_read_b64_tr_b16 a[246:247], v2 offset:38912\n"
    "s_waitcnt lgkmcnt(4)\n"
    "v_mfma_f32_16x16x32_bf16 v[116:119], a[248:251], a[40:43], v[116:119]\n"
    "v_cmp_eq_u32 vcc, 0, v88\n"
    "s_nop 1\n"
    "v_cndmask_b32 v196, 0, v90, vcc\n"
    "v_mfma_f32_16x16x32_bf16 v[116:119], a[252:255], a[60:63], v[116:119]\n"
    "v_cmp_eq_u32 vcc, -1, v88\n"
    "s_nop 1\n"
    "v_cndmask_b32 v197, 0, v90, vcc\n"
    "v_mfma_f32_16x16x32_bf16 v[120:123], a[248:251], a[44:47], v[120:123]\n"
    "v_cmp_eq_u32 vcc, -2, v88\n"
    "s_nop 1\n"
    "v_cndmask_b32 v198, 0, v90, vcc\n"
    "v_mfma_f32_16x16x32_bf16 v[120:123], a[252:255], a[64:67], v[120:123]\n"
    "v_cmp_eq_u32 vcc, -3, v88\n"
    "s_nop 1\n"
    "v_cndmask_b32 v199, 0, v90, vcc\n"
    "v_mfma_f32_16x16x32_bf16 v[124:127], a[248:251], a[48:51], v[124:127]\n"
    "v_cmp_eq_u32 vcc, 8, v88\n"
    "s_nop 1\n"
    "v_cndmask_b32 v200, 0, v90, vcc\n"
    "v_mfma_f32_16x16x32_bf16 v[124:127], a[252:255], a[68:71], v[124:127]\n"
    "v_cmp_eq_u32 vcc, 7, v88\n"
    "s_nop 1\n"
    "v_cndmask_b32 v201, 0, v90, vcc\n"
    "v_mfma_f32_16x16x32_bf16 v[128:131], a[248:251], a[52:55], v[128:131]\n"
    "v_cmp_eq_u32 vcc, 6, v88\n"
    "s_nop 1\n"
    "v_cndmask_b32 v202, 0, v90, vcc\n"
    "v_mfma_f32_16x16x32_bf16 v[128:131], a[252:255], a[72:75], v[128:131]\n"
    "v_cmp_eq_u32 vcc, 5, v88\n"
    "s_nop 1\n"
    "v_cndmask_b32 v203, 0, v90, vcc\n"
    "v_mfma_f32_16x16x32_bf16 v[132:135], a[248:251], a[56:59], v[132:135]\n"
    "ds_read_b64_tr_b16 a[248:249], v2 offset:40960\n"
    "ds_read_b64_tr_b16 a[250:251], v2 offset:43008\n"
    "v_mfma_f32_16x16x32_bf16 v[132:135], a[252:255], a[76:79], v[132:135]\n"
    "s_waitcnt vmcnt(2) lgkmcnt(0)\n"
    "s_barrier\n"
    "ds_read_b64_tr_b16 a[252:253], v2 offset:45056\n"
    "v_mfma_f32_16x16x32_bf16 v[116:119], a[240:243], a[80:83], v[116:119]\n"
    "ds_read_b64_tr_b16 a[254:255], v2 offset:47104\n"
    "ds_read_b64_tr_b16 a[0:1], v3 offset:0\n"
    "v_mfma_f32_16x16x32_bf16 v[116:119], a[244:247], a[100:103], v[116:119]\n"
    "ds_read_b64_tr_b16 a[2:3], v3 offset:2048\n"
    "ds_read_b64_tr_b16 a[4:5], v3 offset:4096\n"
    "v_mfma_f32_16x16x32_bf16 v[120:123], a[240:243], a[84:87], v[120:123]\n"
    "ds_read_b64_tr_b16 a[6:7], v3 offset:6144\n"
    "ds_read_b64_tr_b16 a[8:9], v3 offset:8192\n"
    "v_mfma_f32_16x16x32_bf16 v[120:123], a[244:247], a[104:107], v[120:123]\n"
    "ds_read_b64_tr_b16 a[10:11], v3 offset:10240\n"
    "ds_read_b64_tr_b16 a[12:13], v3 offset:12288\n"
    "v_mfma_f32_16x16x32_bf16 v[124:127], a[240:243], a[88:91], v[124:127]\n"
    "ds_read_b64_tr_b16 a[14:15], v3 offset:14336\n"
    "ds_read_b64_tr_b16 a[16:17], v3 offset:16384\n"
    "v_mfma_f32_16x16x32_bf16 v[124:127], a[244:247], a[108:111], v[124:127]\n"
    "ds_read_b64_tr_b16 a[18:19], v3 offset:18432\n"
    "ds_read_b64_tr_b16 a[20:21], v3 offset:20480\n"
    "v_mfma_f32_16x16x32_bf16 v[128:131], a[240:243], a[92:95], v[128:131]\n"
    "ds_read_b64_tr_b16 a[22:23], v3 offset:22528\n"
    "ds_read_b64_tr_b16 a[24:25], v3 offset:24576\n"
    "v_mfma_f32_16x16x32_bf16 v[128:131], a[244:247], a[112:115], v[128:131]\n"
    "ds_read_b64_tr_b16 a[26:27], v3 offset:26624\n"
    "ds_read_b64_tr_b16 a[28:29], v3 offset:28672\n"
    "v_mfma_f32_16x16x32_bf16 v[132:135], a[240:243], a[96:99], v[132:135]\n"
    "ds_read_b64_tr_b16 a[30:31], v3 offset:30720\n"
    "v_mul_f32 v27, v27, v63\n"
    "v_mfma_f32_16x16x32_bf16 v[132:135], a[244:247], a[116:119], v[132:135]\n"
    "v_fma_f32 v27, v47, v73, v27\n"
    "v_cvt_pk_bf16_f32 v22, v24, v25\n"
    "v_mfma_f32_16x16x32_bf16 v[116:119], a[248:251], a[120:123], v[116:119]\n"
    "v_cvt_pk_bf16_f32 v23, v26, v27\n"
    "s_waitcnt lgkmcnt(13)\n"
    "v_mfma_f32_16x16x32_bf16 v[116:119], a[252:255], a[140:143], v[116:119]\n"
    "buffer_store_dwordx4 v[20:23], v1, s[52:55], s81 offen offset:0 sc1\n"
    "v_mfma_f32_16x16x32_bf16 v[120:123], a[248:251], a[124:127], v[120:123]\n"
    "v_mul_f32 v28, v28, v65\n"
    "v_fma_f32 v28, v48, v75, v28\n"
    "v_mfma_f32_16x16x32_bf16 v[120:123], a[252:255], a[144:147], v[120:123]\n"
    "v_mul_f32 v29, v29, v65\n"
    "v_fma_f32 v29, v49, v75, v29\n"
    "v_mfma_f32_16x16x32_bf16 v[124:127], a[248:251], a[128:131], v[124:127]\n"
    "v_mul_f32 v30, v30, v65\n"
    "v_fma_f32 v30, v50, v75, v30\n"
    "v_mfma_f32_16x16x32_bf16 v[124:127], a[252:255], a[148:151], v[124:127]\n"
    "buffer_load_dword v86, v84, s[52:55], s66 offen offset:1024 sc1\n"
    "v_mfma_f32_16x16x32_bf16 v[128:131], a[248:251], a[132:135], v[128:131]\n"
    "v_mul_f32 v31, v31, v65\n"
    "v_fma_f32 v31, v51, v75, v31\n"
    "v_mfma_f32_16x16x32_bf16 v[128:131], a[252:255], a[152:155], v[128:131]\n"
    "v_cvt_pk_bf16_f32 v28, v28, v29\n"
    "v_cvt_pk_bf16_f32 v29, v30, v31\n"
    "v_mfma_f32_16x16x32_bf16 v[132:135], a[248:251], a[136:139], v[132:135]\n"
    "v_mul_f32 v32, v32, v67\n"
    "v_fma_f32 v32, v52, v77, v32\n"
    "v_mul_f32 v33, v33, v67\n"
    "v_fma_f32 v33, v53, v77, v33\n"
    "v_mul_f32 v34, v34, v67\n"
    "v_fma_f32 v34, v54, v77, v34\n"
    "v_mul_f32 v35, v35, v67\n"
    "v_fma_f32 v35, v55, v77, v35\n"
    "v_cvt_pk_bf16_f32 v30, v32, v33\n"
    "v_cvt_pk_bf16_f32 v31, v34, v35\n"
    "buffer_store_dwordx4 v[28:31], v1, s[52:55], s81 offen offset:1024 sc1\n"
    "v_mul_f32 v36, v36, v69\n"
    "v_fma_f32 v36, v56, v79, v36\n"
    "v_mul_f32 v37, v37, v69\n"
    "v_fma_f32 v37, v57, v79, v37\n"
    "v_mul_f32 v38, v38, v69\n"
    "v_fma_f32 v38, v58, v79, v38\n"
    "v_mul_f32 v39, v39, v69\n"
    "v_fma_f32 v39, v59, v79, v39\n"
    "v_cvt_pk_bf16_f32 v94, v36, v37\n"
    "v_cvt_pk_bf16_f32 v95, v38, v39\n"
    "buffer_store_dwordx4 v[92:95], v1, s[52:55], s84 offen sc1\n"
    "s_add_u32 s84, s84, 20480\n"
    "s_add_u32 s81, s81, 12288\n"
    "v_mfma_f32_16x16x32_bf16 v[132:135], a[252:255], a[156:159], v[132:135]\n"
    "v_mul_f32 v116, v116, v61\n"
    "v_mul_f32 v117, v117, v61\n"
    "v_mfma_f32_16x16x32_bf16 v[156:159], a[0:3], a[160:163], 0\n"
    "v_mul_f32 v118, v118, v61\n"
    "v_mul_f32 v119, v119, v61\n"
    "s_waitcnt lgkmcnt(0)\n"
    "v_mfma_f32_16x16x32_bf16 v[156:159], a[4:7], a[180:183], v[156:159]\n"
    "v_mul_f32 v120, v120, v63\n"
    "v_mul_f32 v121, v121, v63\n"
    "v_mfma_f32_16x16x32_bf16 v[156:159], a[8:11], a[200:203], v[156:159]\n"
    "v_mul_f32 v122, v122, v63\n"
    "v_mul_f32 v123, v123, v63\n"
    "v_mfma_f32_16x16x32_bf16 v[156:159], a[12:15], a[220:223], v[156:159]\n"
    "v_mul_f32 v124, v124, v65\n"
    "v_mul_f32 v125, v125, v65\n"
    "v_mfma_f32_16x16x32_bf16 v[156:159], a[16:19], v[96:99], v[156:159]\n"
    "v_mul_f32 v126, v126, v65\n"
    "v_mul_f32 v127, v127, v65\n"
    "v_mfma_f32_16x16x32_bf16 v[156:159], a[20:23], v[136:139], v[156:159]\n"
    "v_mul_f32 v128, v128, v67\n"
    "v_mul_f32 v129, v129, v67\n"
    "v_mfma_f32_16x16x32_bf16 v[156:159], a[24:27], v[176:179], v[156:159]\n"
    "v_mul_f32 v130, v130, v67\n"
    "v_mul_f32 v131, v131, v67\n"
    "v_mfma_f32_16x16x32_bf16 v[156:159], a[28:31], v[216:219], v[156:159]\n"
    "v_mul_f32 v132, v132, v69\n"
    "v_mul_f32 v133, v133, v69\n"
    "v_mfma_f32_16x16x32_bf16 v[160:163], a[0:3], a[164:167], 0\n"
    "v_mul_f32 v134, v134, v69\n"
    "v_mul_f32 v135, v135, v69\n"
    "v_mfma_f32_16x16x32_bf16 v[160:163], a[4:7], a[184:187], v[160:163]\n"
    "v_mfma_f32_16x16x32_bf16 v[160:163], a[8:11], a[204:207], v[160:163]\n"
    "v_mfma_f32_16x16x32_bf16 v[160:163], a[12:15], a[224:227], v[160:163]\n"
    "v_fma_f32 v116, v156, v71, v116\n"
    "v_fma_f32 v117, v157, v71, v117\n"
    "v_mfma_f32_16x16x32_bf16 v[160:163], a[16:19], v[100:103], v[160:163]\n"
    "v_fma_f32 v118, v158, v71, v118\n"
    "v_fma_f32 v119, v159, v71, v119\n"
    "v_mfma_f32_16x16x32_bf16 v[160:163], a[20:23], v[140:143], v[160:163]\n"
    "v_cvt_pk_bf16_f32 v116, v116, v117\n"
    "v_cvt_pk_bf16_f32 v117, v118, v119\n"
    "v_mfma_f32_16x16x32_bf16 v[160:163], a[24:27], v[180:183], v[160:163]\n"
    "v_mfma_f32_16x16x32_bf16 v[160:163], a[28:31], v[220:223], v[160:163]\n"
    "v_mfma_f32_16x16x32_bf16 v[164:167], a[0:3], a[168:171], 0\n"
    "v_mfma_f32_16x16x32_bf16 v[164:167], a[4:7], a[188:191], v[164:167]\n"
    "v_mfma_f32_16x16x32_bf16 v[164:167], a[8:11], a[208:211], v[164:167]\n"
    "v_mfma_f32_16x16x32_bf16 v[164:167], a[12:15], a[228:231], v[164:167]\n"
    "s_nop 3\n"
    "v_fma_f32 v120, v160, v73, v120\n"
    "v_fma_f32 v121, v161, v73, v121\n"
    "v_mfma_f32_16x16x32_bf16 v[164:167], a[16:19], v[104:107], v[164:167]\n"
    "v_fma_f32 v122, v162, v73, v122\n"
    "v_fma_f32 v123, v163, v73, v123\n"
    "v_mfma_f32_16x16x32_bf16 v[164:167], a[20:23], v[144:147], v[164:167]\n"
    "v_cvt_pk_bf16_f32 v118, v120, v121\n"
    "v_cvt_pk_bf16_f32 v119, v122, v123\n"
    "v_mfma_f32_16x16x32_bf16 v[164:167], a[24:27], v[184:187], v[164:167]\n"
    "buffer_store_dwordx4 v[116:119], v1, s[52:55], s81 offen offset:0 sc1\n"
    "v_mfma_f32_16x16x32_bf16 v[164:167], a[28:31], v[224:227], v[164:167]\n"
    "v_mfma_f32_16x16x32_bf16 v[168:171], a[0:3], a[172:175], 0\n"
    "v_mfma_f32_16x16x32_bf16 v[168:171], a[4:7], a[192:195], v[168:171]\n"
    "v_mfma_f32_16x16x32_bf16 v[168:171], a[8:11], a[212:215], v[168:171]\n"
    "v_mfma_f32_16x16x32_bf16 v[168:171], a[12:15], a[232:235], v[168:171]\n"
    "s_nop 3\n"
    "v_fma_f32 v124, v164, v75, v124\n"
    "v_fma_f32 v125, v165, v75, v125\n"
    "v_mfma_f32_16x16x32_bf16 v[168:171], a[16:19], v[108:111], v[168:171]\n"
    "v_fma_f32 v126, v166, v75, v126\n"
    "v_fma_f32 v127, v167, v75, v127\n"
    "v_mfma_f32_16x16x32_bf16 v[168:171], a[20:23], v[148:151], v[168:171]\n"
    "v_cvt_pk_bf16_f32 v124, v124, v125\n"
    "v_cvt_pk_bf16_f32 v125, v126, v127\n"
    "v_mfma_f32_16x16x32_bf16 v[168:171], a[24:27], v[188:191], v[168:171]\n"
    "v_mfma_f32_16x16x32_bf16 v[168:171], a[28:31], v[228:231], v[168:171]\n"
    "v_mfma_f32_16x16x32_bf16 v[172:175], a[0:3], a[176:179], 0\n"
    "v_mfma_f32_16x16x32_bf16 v[172:175], a[4:7], a[196:199], v[172:175]\n"
    "v_mfma_f32_16x16x32_bf16 v[172:175], a[8:11], a[216:219], v[172:175]\n"
    "v_mfma_f32_16x16x32_bf16 v[172:175], a[12:15], a[236:239], v[172:175]\n"
    "s_nop 3\n"
    "v_fma_f32 v128, v168, v77, v128\n"
    "v_fma_f32 v129, v169, v77, v129\n"
    "v_mfma_f32_16x16x32_bf16 v[172:175], a[16:19], v[112:115], v[172:175]\n"
    "v_fma_f32 v130, v170, v77, v130\n"
    "v_fma_f32 v131, v171, v77, v131\n"
    "v_mfma_f32_16x16x32_bf16 v[172:175], a[20:23], v[152:155], v[172:175]\n"
    "v_cvt_pk_bf16_f32 v126, v128, v129\n"
    "v_cvt_pk_bf16_f32 v127, v130, v131\n"
    "v_mfma_f32_16x16x32_bf16 v[172:175], a[24:27], v[192:195], v[172:175]\n"
    "buffer_store_dwordx4 v[124:127], v1, s[52:55], s81 offen offset:1024 sc1\n"
    "v_mfma_f32_16x16x32_bf16 v[172:175], a[28:31], v[232:235], v[172:175]\n"
    "s_nop 7\n"
    "v_fma_f32 v132, v172, v79, v132\n"
    "v_fma_f32 v133, v173, v79, v133\n"
    "v_fma_f32 v134, v174, v79, v134\n"
    "v_fma_f32 v135, v175, v79, v135\n"
    "v_cvt_pk_bf16_f32 v92, v132, v133\n"
    "v_cvt_pk_bf16_f32 v93, v134, v135\n"
    "s_sub_u32 s84, s84, 8192\n"
    "buffer_store_dwordx4 v[92:95], v1, s[52:55], s84 offen sc1\n"
    "s_add_u32 s84, s84, 20480\n"
    "s_add_u32 s81, s81, 8192\n"
    "s_waitcnt vmcnt(0)\n"
    "s_barrier\n"
    "v_add_u32 v83, 1, v83\n"
    "s_cmp_lg_u32 %2, 0\n"
    "s_cbranch_scc1 NEB_%=\n"
    "buffer_store_dword v83, v85, s[52:55], s67 offen offset:1024 sc1\n"
    "NEB_%=:\n"
    "s_cmp_ge_u32 %2, 2\n"
    "s_cbranch_scc1 STB_%=\n"
    "v_sub_u32 v86, v86, v9\n"
    "v_cmp_lt_u32 vcc, 1, v86\n"
    "s_cbranch_vccnz SPA_%=\n"
    "SPAR_%=:\n"
    "buffer_load_dwordx4 a[0:3], v1, s[52:55], s88 offen sc1\n"
    "s_add_u32 s88, s88, 73728\n"
    "buffer_load_dwordx4 a[4:7], v1, s[52:55], s88 offen sc1\n"
    "s_add_u32 s88, s88, 73728\n"
    "buffer_load_dwordx4 a[8:11], v1, s[52:55], s88 offen sc1\n"
    "s_add_u32 s88, s88, 73728\n"
    "buffer_load_dwordx4 a[12:15], v1, s[52:55], s88 offen sc1\n"
    "s_add_u32 s88, s88, 73728\n"
    "buffer_load_dwordx4 a[16:19], v1, s[52:55], s88 offen sc1\n"
    "s_add_u32 s88, s88, 73728\n"
    "buffer_load_dwordx4 a[20:23], v1, s[52:55], s88 offen sc1\n"
    "s_add_u32 s88, s88, 73728\n"
    "buffer_load_dwordx4 a[24:27], v1, s[52:55], s88 offen sc1\n"
    "s_add_u32 s88, s88, 73728\n"
    "buffer_load_dwordx4 a[28:31], v1, s[52:55], s88 offen sc1\n"
    "s_add_u32 s88, s88, 73728\n"
    "buffer_load_dwordx4 a[32:35], v1, s[52:55], s88 offen sc1\n"
    "s_add_u32 s88, s88, 73728\n"
    "buffer_load_dwordx4 a[36:39], v1, s[52:55], s88 offen sc1\n"
    "s_add_u32 s88, s88, 73728\n"
    "buffer_load_dwordx4 a[40:43], v1, s[52:55], s88 offen sc1\n"
    "s_add_u32 s88, s88, 73728\n"
    "buffer_load_dwordx4 a[44:47], v1, s[52:55], s88 offen sc1\n"
    "s_add_u32 s88, s88, 73728\n"
    "buffer_load_dwordx4 a[48:51], v1, s[52:55], s88 offen sc1\n"
    "s_add_u32 s88, s88, 73728\n"
    "buffer_load_dwordx4 a[52:55], v1, s[52:55], s88 offen sc1\n"
    "s_add_u32 s88, s88, 73728\n"
    "buffer_load_dwordx4 a[56:59], v1, s[52:55], s88 offen sc1\n"
    "s_add_u32 s88, s88, 73728\n"
    "buffer_load_dwordx4 a[60:63], v1, s[52:55], s88 offen sc1\n"
    "s_add_u32 s88, s88, 73728\n"
    "buffer_load_dwordx4 a[64:67], v1, s[52:55], s89 offen sc1\n"
    "s_add_u32 s89, s89, 73728\n"
    "buffer_load_dwordx4 a[68:71], v1, s[52:55], s89 offen sc1\n"
    "s_add_u32 s89, s89, 73728\n"
    "buffer_load_dwordx4 a[72:75], v1, s[52:55], s89 offen sc1\n"
    "s_add_u32 s89, s89, 73728\n"
    "buffer_load_dwordx4 a[76:79], v1, s[52:55], s89 offen sc1\n"
    "s_add_u32 s89, s89, 73728\n"
    "buffer_load_dwordx4 a[80:83], v1, s[52:55], s89 offen sc1\n"
    "s_add_u32 s89, s89, 73728\n"
    "buffer_load_dwordx4 a[84:87], v1, s[52:55], s89 offen sc1\n"
    "s_add_u32 s89, s89, 73728\n"
    "buffer_load_dwordx4 a[88:91], v1, s[52:55], s89 offen sc1\n"
    "s_add_u32 s89, s89, 73728\n"
    "buffer_load_dwordx4 a[92:95], v1, s[52:55], s89 offen sc1\n"
    "s_add_u32 s89, s89, 73728\n"
    "buffer_load_dwordx4 a[96:99], v1, s[52:55], s89 offen sc1\n"
    "s_add_u32 s89, s89, 73728\n"
    "buffer_load_dwordx4 a[100:103], v1, s[52:55], s89 offen sc1\n"
    "s_add_u32 s89, s89, 73728\n"
    "buffer_load_dwordx4 a[104:107], v1, s[52:55], s89 offen sc1\n"
    "s_add_u32 s89, s89, 73728\n"
    "buffer_load_dwordx4 a[108:111], v1, s[52:55], s89 offen sc1\n"
    "s_add_u32 s89, s89, 73728\n"
    "buffer_load_dwordx4 a[112:115], v1, s[52:55], s89 offen sc1\n"
    "s_add_u32 s89, s89, 73728\n"
    "buffer_load_dwordx4 a[116:119], v1, s[52:55], s89 offen sc1\n"
    "s_add_u32 s89, s89, 73728\n"
    "buffer_load_dwordx4 a[120:123], v1, s[52:55], s89 offen sc1\n"
    "s_add_u32 s89, s89, 73728\n"
    "buffer_load_dwordx4 a[124:127], v1, s[52:55], s89 offen sc1\n"
    "s_add_u32 s89, s89, 73728\n"
    "buffer_load_dword v86, v84, s[52:55], s66 offen sc1\n"
    "s_waitcnt vmcnt(32)\n"
    "v_mfma_f32_16x16x32_bf16 v[204:207], v[196:199], a[0:3], 0\n"
    "v_mfma_f32_16x16x32_bf16 v[208:211], v[200:203], a[0:3], 0\n"
    "s_waitcnt vmcnt(31)\n"
    "v_mfma_f32_16x16x32_bf16 v[204:207], v[196:199], a[4:7], v[204:207]\n"
    "v_mfma_f32_16x16x32_bf16 v[208:211], v[200:203], a[4:7], v[208:211]\n"
    "s_waitcnt vmcnt(30)\n"
    "v_mfma_f32_16x16x32_bf16 v[204:207], v[196:199], a[8:11], v[204:207]\n"
    "v_mfma_f32_16x16x32_bf16 v[208:211], v[200:203], a[8:11], v[208:211]\n"
    "s_waitcnt vmcnt(29)\n"
    "v_mfma_f32_16x16x32_bf16 v[204:207], v[196:199], a[12:15], v[204:207]\n"
    "v_mfma_f32_16x16x32_bf16 v[208:211], v[200:203], a[12:15], v[208:211]\n"
    "s_waitcnt vmcnt(28)\n"
    "v_mfma_f32_16x16x32_bf16 v[204:207], v[196:199], a[16:19], v[204:207]\n"
    "v_mfma_f32_16x16x32_bf16 v[208:211], v[200:203], a[16:19], v[208:211]\n"
    "s_waitcnt vmcnt(27)\n"
    "v_mfma_f32_16x16x32_bf16 v[204:207], v[196:199], a[20:23], v[204:207]\n"
    "v_mfma_f32_16x16x32_bf16 v[208:211], v[200:203], a[20:23], v[208:211]\n"
    "s_waitcnt vmcnt(26)\n"
    "v_mfma_f32_16x16x32_bf16 v[204:207], v[196:199], a[24:27], v[204:207]\n"
    "v_mfma_f32_16x16x32_bf16 v[208:211], v[200:203], a[24:27], v[208:211]\n"
    "s_waitcnt vmcnt(25)\n"
    "v_mfma_f32_16x16x32_bf16 v[204:207], v[196:199], a[28:31], v[204:207]\n"
    "v_mfma_f32_16x16x32_bf16 v[208:211], v[200:203], a[28:31], v[208:211]\n"
    "s_waitcnt vmcnt(24)\n"
    "v_mfma_f32_16x16x32_bf16 v[204:207], v[196:199], a[32:35], v[204:207]\n"
    "v_mfma_f32_16x16x32_bf16 v[208:211], v[200:203], a[32:35], v[208:211]\n"
    "s_waitcnt vmcnt(23)\n"
    "v_mfma_f32_16x16x32_bf16 v[204:207], v[196:199], a[36:39], v[204:207]\n"
    "v_mfma_f32_16x16x32_bf16 v[208:211], v[200:203], a[36:39], v[208:211]\n"
    "s_waitcnt vmcnt(22)\n"
    "v_mfma_f32_16x16x32_bf16 v[204:207], v[196:199], a[40:43], v[204:207]\n"
    "v_mfma_f32_16x16x32_bf16 v[208:211], v[200:203], a[40:43], v[208:211]\n"
    "s_waitcnt vmcnt(21)\n"
    "v_mfma_f32_16x16x32_bf16 v[204:207], v[196:199], a[44:47], v[204:207]\n"
    "v_mfma_f32_16x16x32_bf16 v[208:211], v[200:203], a[44:47], v[208:211]\n"
    "s_waitcnt vmcnt(20)\n"
    "v_mfma_f32_16x16x32_bf16 v[204:207], v[196:199], a[48:51], v[204:207]\n"
    "v_mfma_f32_16x16x32_bf16 v[208:211], v[200:203], a[48:51], v[208:211]\n"
    "s_waitcnt vmcnt(19)\n"
    "v_mfma_f32_16x16x32_bf16 v[204:207], v[196:199], a[52:55], v[204:207]\n"
    "v_mfma_f32_16x16x32_bf16 v[208:211], v[200:203], a[52:55], v[208:211]\n"
    "s_waitcnt vmcnt(18)\n"
    "v_mfma_f32_16x16x32_bf16 v[204:207], v[196:199], a[56:59], v[204:207]\n"
    "v_mfma_f32_16x16x32_bf16 v[208:211], v[200:203], a[56:59], v[208:211]\n"
    "s_waitcnt vmcnt(17)\n"
    "v_mfma_f32_16x16x32_bf16 v[204:207], v[196:199], a[60:63], v[204:207]\n"
    "v_mfma_f32_16x16x32_bf16 v[208:211], v[200:203], a[60:63], v[208:211]\n"
    "s_cmp_eq_u32 s92, 0\n"
    "s_cselect_b64 vcc, -1, 0\n"
    "v_cndmask_b32 v87, v81, v80, vcc\n"
    "s_cmp_eq_u32 s92, 2\n"
    "s_cselect_b64 vcc, -1, 0\n"
    "v_cndmask_b32 v87, v87, v82, vcc\n"
    "s_nop 0\n"
    "v_cvt_pk_bf16_f32 v204, v204, v205\n"
    "v_cvt_pk_bf16_f32 v205, v206, v207\n"
    "v_cvt_pk_bf16_f32 v208, v208, v209\n"
    "v_cvt_pk_bf16_f32 v209, v210, v211\n"
    "buffer_store_dwordx2 v[204:205], v87, s[8:11], s90 offen\n"
    "buffer_store_dwordx2 v[208:209], v87, s[8:11], s90 offen offset:16\n"
    "s_nop 1\n"
    "s_waitcnt vmcnt(18)\n"
    "v_mfma_f32_16x16x32_bf16 v[204:207], v[196:199], a[64:67], 0\n"
    "v_mfma_f32_16x16x32_bf16 v[208:211], v[200:203], a[64:67], 0\n"
    "s_waitcnt vmcnt(17)\n"
    "v_mfma_f32_16x16x32_bf16 v[204:207], v[196:199], a[68:71], v[204:207]\n"
    "v_mfma_f32_16x16x32_bf16 v[208:211], v[200:203], a[68:71], v[208:211]\n"
    "s_waitcnt vmcnt(16)\n"
    "v_mfma_f32_16x16x32_bf16 v[204:207], v[196:199], a[72:75], v[204:207]\n"
    "v_mfma_f32_16x16x32_bf16 v[208:211], v[200:203], a[72:75], v[208:211]\n"
    "s_waitcnt vmcnt(15)\n"
    "v_mfma_f32_16x16x32_bf16 v[204:207], v[196:199], a[76:79], v[204:207]\n"
    "v_mfma_f32_16x16x32_bf16 v[208:211], v[200:203], a[76:79], v[208:211]\n"
    "s_waitcnt vmcnt(14)\n"
    "v_mfma_f32_16x16x32_bf16 v[204:207], v[196:199], a[80:83], v[204:207]\n"
    "v_mfma_f32_16x16x32_bf16 v[208:211], v[200:203], a[80:83], v[208:211]\n"
    "s_waitcnt vmcnt(13)\n"
    "v_mfma_f32_16x16x32_bf16 v[204:207], v[196:199], a[84:87], v[204:207]\n"
    "v_mfma_f32_16x16x32_bf16 v[208:211], v[200:203], a[84:87], v[208:211]\n"
    "s_waitcnt vmcnt(12)\n"
    "v_mfma_f32_16x16x32_bf16 v[204:207], v[196:199], a[88:91], v[204:207]\n"
    "v_mfma_f32_16x16x32_bf16 v[208:211], v[200:203], a[88:91], v[208:211]\n"
    "s_waitcnt vmcnt(11)\n"
    "v_mfma_f32_16x16x32_bf16 v[204:207], v[196:199], a[92:95], v[204:207]\n"
    "v_mfma_f32_16x16x32_bf16 v[208:211], v[200:203], a[92:95], v[208:211]\n"
    "s_waitcnt vmcnt(10)\n"
    "v_mfma_f32_16x16x32_bf16 v[204:207], v[196:199], a[96:99], v[204:207]\n"
    "v_mfma_f32_16x16x32_bf16 v[208:211], v[200:203], a[96:99], v[208:211]\n"
    "s_waitcnt vmcnt(9)\n"
    "v_mfma_f32_16x16x32_bf16 v[204:207], v[196:199], a[100:103], v[204:207]\n"
    "v_mfma_f32_16x16x32_bf16 v[208:211], v[200:203], a[100:103], v[208:211]\n"
    "s_waitcnt vmcnt(8)\n"
    "v_mfma_f32_16x16x32_bf16 v[204:207], v[196:199], a[104:107], v[204:207]\n"
    "v_mfma_f32_16x16x32_bf16 v[208:211], v[200:203], a[104:107], v[208:211]\n"
    "s_waitcnt vmcnt(7)\n"
    "v_mfma_f32_16x16x32_bf16 v[204:207], v[196:199], a[108:111], v[204:207]\n"
    "v_mfma_f32_16x16x32_bf16 v[208:211], v[200:203], a[108:111], v[208:211]\n"
    "s_waitcnt vmcnt(6)\n"
    "v_mfma_f32_16x16x32_bf16 v[204:207], v[196:199], a[112:115], v[204:207]\n"
    "v_mfma_f32_16x16x32_bf16 v[208:211], v[200:203], a[112:115], v[208:211]\n"
    "s_waitcnt vmcnt(5)\n"
    "v_mfma_f32_16x16x32_bf16 v[204:207], v[196:199], a[116:119], v[204:207]\n"
    "v_mfma_f32_16x16x32_bf16 v[208:211], v[200:203], a[116:119], v[208:211]\n"
    "s_waitcnt vmcnt(4)\n"
    "v_mfma_f32_16x16x32_bf16 v[204:207], v[196:199], a[120:123], v[204:207]\n"
    "v_mfma_f32_16x16x32_bf16 v[208:211], v[200:203], a[120:123], v[208:211]\n"
    "s_waitcnt vmcnt(3)\n"
    "v_mfma_f32_16x16x32_bf16 v[204:207], v[196:199], a[124:127], v[204:207]\n"
    "v_mfma_f32_16x16x32_bf16 v[208:211], v[200:203], a[124:127], v[208:211]\n"
    "s_cmp_eq_u32 s93, 0\n"
    "s_cselect_b64 vcc, -1, 0\n"
    "v_cndmask_b32 v87, v81, v80, vcc\n"
    "s_cmp_eq_u32 s93, 2\n"
    "s_cselect_b64 vcc, -1, 0\n"
    "v_cndmask_b32 v87, v87, v82, vcc\n"
    "s_nop 0\n"
    "v_cvt_pk_bf16_f32 v204, v204, v205\n"
    "v_cvt_pk_bf16_f32 v205, v206, v207\n"
    "v_cvt_pk_bf16_f32 v208, v208, v209\n"
    "v_cvt_pk_bf16_f32 v209, v210, v211\n"
    "buffer_store_dwordx2 v[204:205], v87, s[8:11], s91 offen\n"
    "buffer_store_dwordx2 v[208:209], v87, s[8:11], s91 offen offset:16\n"
    "s_nop 1\n"
    "s_waitcnt vmcnt(4)\n"
    "v_add_u32 v86, 1, v86\n"
    "v_cmp_ne_u32 vcc, v86, v83\n"
    "s_cbranch_vccz FIN_%=\n"
    "s_cmp_lg_u32 %2, 0\n"
    "s_cbranch_scc1 FIN_%=\n"
    "buffer_store_dword v86, v85, s[52:55], s67 offen offset:1024 sc1\n"
    "s_branch FIN_%=\n"
    "STB_%=:\n"
    "v_add_u32 v9, 1, v9\n"
    "SPB_%=:\n"
    "buffer_load_dword v86, v84, s[52:55], s66 offen offset:1024 sc1\n"
    "s_waitcnt vmcnt(0)\n"
    "v_cmp_ne_u32 vcc, v86, v9\n"
    "s_and_b64 vcc, vcc, s[58:59]\n"
    "s_cbranch_vccz SPBR_%=\n"
    "s_sub_u32 s75, s75, 1\n"
    "s_cmp_lg_u32 s75, 0xfff00000\n"
    "s_cbranch_scc1 SPB_%=\n"
    "SPBR_%=:\n"
    "buffer_load_dwordx4 a[0:3], v1, s[52:55], s88 offen sc1\n"
    "s_add_u32 s88, s88, 73728\n"
    "buffer_load_dwordx4 a[4:7], v1, s[52:55], s88 offen sc1\n"
    "s_add_u32 s88, s88, 73728\n"
    "buffer_load_dwordx4 a[8:11], v1, s[52:55], s88 offen sc1\n"
    "s_add_u32 s88, s88, 73728\n"
    "buffer_load_dwordx4 a[12:15], v1, s[52:55], s88 offen sc1\n"
    "s_add_u32 s88, s88, 73728\n"
    "buffer_load_dwordx4 a[16:19], v1, s[52:55], s88 offen sc1\n"
    "s_add_u32 s88, s88, 73728\n"
    "buffer_load_dwordx4 a[20:23], v1, s[52:55], s88 offen sc1\n"
    "s_add_u32 s88, s88, 73728\n"
    "buffer_load_dwordx4 a[24:27], v1, s[52:55], s88 offen sc1\n"
    "s_add_u32 s88, s88, 73728\n"
    "buffer_load_dwordx4 a[28:31], v1, s[52:55], s88 offen sc1\n"
    "s_add_u32 s88, s88, 73728\n"
    "buffer_load_dwordx4 a[32:35], v1, s[52:55], s88 offen sc1\n"
    "s_add_u32 s88, s88, 73728\n"
    "buffer_load_dwordx4 a[36:39], v1, s[52:55], s88 offen sc1\n"
    "s_add_u32 s88, s88, 73728\n"
    "buffer_load_dwordx4 a[40:43], v1, s[52:55], s88 offen sc1\n"
    "s_add_u32 s88, s88, 73728\n"
    "buffer_load_dwordx4 a[44:47], v1, s[52:55], s88 offen sc1\n"
    "s_add_u32 s88, s88, 73728\n"
    "buffer_load_dwordx4 a[48:51], v1, s[52:55], s88 offen sc1\n"
    "s_add_u32 s88, s88, 73728\n"
    "buffer_load_dwordx4 a[52:55], v1, s[52:55], s88 offen sc1\n"
    "s_add_u32 s88, s88, 73728\n"
    "buffer_load_dwordx4 a[56:59], v1, s[52:55], s88 offen sc1\n"
    "s_add_u32 s88, s88, 73728\n"
    "buffer_load_dwordx4 a[60:63], v1, s[52:55], s88 offen sc1\n"
    "s_add_u32 s88, s88, 73728\n"
    "s_waitcnt vmcnt(15)\n"
    "v_mfma_f32_16x16x32_bf16 v[204:207], v[196:199], a[0:3], 0\n"
    "v_mfma_f32_16x16x32_bf16 v[208:211], v[200:203], a[0:3], 0\n"
    "s_waitcnt vmcnt(14)\n"
    "v_mfma_f32_16x16x32_bf16 v[204:207], v[196:199], a[4:7], v[204:207]\n"
    "v_mfma_f32_16x16x32_bf16 v[208:211], v[200:203], a[4:7], v[208:211]\n"
    "s_waitcnt vmcnt(13)\n"
    "v_mfma_f32_16x16x32_bf16 v[204:207], v[196:199], a[8:11], v[204:207]\n"
    "v_mfma_f32_16x16x32_bf16 v[208:211], v[200:203], a[8:11], v[208:211]\n"
    "s_waitcnt vmcnt(12)\n"
    "v_mfma_f32_16x16x32_bf16 v[204:207], v[196:199], a[12:15], v[204:207]\n"
    "v_mfma_f32_16x16x32_bf16 v[208:211], v[200:203], a[12:15], v[208:211]\n"
    "s_waitcnt vmcnt(11)\n"
    "v_mfma_f32_16x16x32_bf16 v[204:207], v[196:199], a[16:19], v[204:207]\n"
    "v_mfma_f32_16x16x32_bf16 v[208:211], v[200:203], a[16:19], v[208:211]\n"
    "s_waitcnt vmcnt(10)\n"
    "v_mfma_f32_16x16x32_bf16 v[204:207], v[196:199], a[20:23], v[204:207]\n"
    "v_mfma_f32_16x16x32_bf16 v[208:211], v[200:203], a[20:23], v[208:211]\n"
    "s_waitcnt vmcnt(9)\n"
    "v_mfma_f32_16x16x32_bf16 v[204:207], v[196:199], a[24:27], v[204:207]\n"
    "v_mfma_f32_16x16x32_bf16 v[208:211], v[200:203], a[24:27], v[208:211]\n"
    "s_waitcnt vmcnt(8)\n"
    "v_mfma_f32_16x16x32_bf16 v[204:207], v[196:199], a[28:31], v[204:207]\n"
    "v_mfma_f32_16x16x32_bf16 v[208:211], v[200:203], a[28:31], v[208:211]\n"
    "s_waitcnt vmcnt(7)\n"
    "v_mfma_f32_16x16x32_bf16 v[204:207], v[196:199], a[32:35], v[204:207]\n"
    "v_mfma_f32_16x16x32_bf16 v[208:211], v[200:203], a[32:35], v[208:211]\n"
    "s_waitcnt vmcnt(6)\n"
    "v_mfma_f32_16x16x32_bf16 v[204:207], v[196:199], a[36:39], v[204:207]\n"
    "v_mfma_f32_16x16x32_bf16 v[208:211], v[200:203], a[36:39], v[208:211]\n"
    "s_waitcnt vmcnt(5)\n"
    "v_mfma_f32_16x16x32_bf16 v[204:207], v[196:199], a[40:43], v[204:207]\n"
    "v_mfma_f32_16x16x32_bf16 v[208:211], v[200:203], a[40:43], v[208:211]\n"
    "s_waitcnt vmcnt(4)\n"
    "v_mfma_f32_16x16x32_bf16 v[204:207], v[196:199], a[44:47], v[204:207]\n"
    "v_mfma_f32_16x16x32_bf16 v[208:211], v[200:203], a[44:47], v[208:211]\n"
    "s_waitcnt vmcnt(3)\n"
    "v_mfma_f32_16x16x32_bf16 v[204:207], v[196:199], a[48:51], v[204:207]\n"
    "v_mfma_f32_16x16x32_bf16 v[208:211], v[200:203], a[48:51], v[208:211]\n"
    "s_waitcnt vmcnt(2)\n"
    "v_mfma_f32_16x16x32_bf16 v[204:207], v[196:199], a[52:55], v[204:207]\n"
    "v_mfma_f32_16x16x32_bf16 v[208:211], v[200:203], a[52:55], v[208:211]\n"
    "s_waitcnt vmcnt(1)\n"
    "v_mfma_f32_16x16x32_bf16 v[204:207], v[196:199], a[56:59], v[204:207]\n"
    "v_mfma_f32_16x16x32_bf16 v[208:211], v[200:203], a[56:59], v[208:211]\n"
    "s_waitcnt vmcnt(0)\n"
    "v_mfma_f32_16x16x32_bf16 v[204:207], v[196:199], a[60:63], v[204:207]\n"
    "v_mfma_f32_16x16x32_bf16 v[208:211], v[200:203], a[60:63], v[208:211]\n"
    "s_cmp_eq_u32 s92, 0\n"
    "s_cselect_b64 vcc, -1, 0\n"
    "v_cndmask_b32 v87, v81, v80, vcc\n"
    "s_cmp_eq_u32 s92, 2\n"
    "s_cselect_b64 vcc, -1, 0\n"
    "v_cndmask_b32 v87, v87, v82, vcc\n"
    "s_nop 0\n"
    "v_cvt_pk_bf16_f32 v204, v204, v205\n"
    "v_cvt_pk_bf16_f32 v205, v206, v207\n"
    "v_cvt_pk_bf16_f32 v208, v208, v209\n"
    "v_cvt_pk_bf16_f32 v209, v210, v211\n"
    "buffer_store_dwordx2 v[204:205], v87, s[8:11], s90 offen\n"
    "buffer_store_dwordx2 v[208:209], v87, s[8:11], s90 offen offset:16\n"
    "s_nop 1\n"
    "FIN_%=:\n"
    "s_branch END_%=\n"
    "R0_o_%=:\n"
    "s_mov_b32 s78, 0\n"
    "s_branch R0_BODY_%=\n"
    "R0_e_%=:\n"
    "s_mov_b32 s78, 1\n"
    "s_branch R0_BODY_%=\n"
    "R0_x_%=:\n"
    "s_mov_b32 s78, 2\n"
    "s_branch R0_BODY_%=\n"
    "R0_y_%=:\n"
    "s_mov_b32 s78, 3\n"
    "s_branch R0_BODY_%=\n"
    "R0_BODY_%=:\n"
    "s_nop 15\n"
    "s_waitcnt lgkmcnt(0)\n"
    "ds_read_b32 v86, v13 offset:0\n"
    "v_max3_f32 v84, v40, v41, v42\n"
    "v_max_f32 v84, v84, v43\n"
    "v_mov_b32 v89, v84\n"
    "s_nop 1\n"
    "v_permlane16_swap_b32 v84, v89\n"
    "v_max_f32 v84, v84, v89\n"
    "v_mov_b32 v89, v84\n"
    "s_nop 1\n"
    "v_permlane32_swap_b32 v84, v89\n"
    "v_max_f32 v84, v84, v89\n"
    "v_cmp_lt_f32 vcc, s74, v84\n"
    "s_nop 1\n"
    "v_cndmask_b32 v85, 0, v84, vcc\n"
    "s_cmp_lg_u64 vcc, 0\n"
    "s_cselect_b32 s77, 1, s77\n"
    "s_waitcnt lgkmcnt(0)\n"
    "v_sub_f32 v86, v20, v86\n"
    "v_max_f32 v85, v85, v86\n"
    "v_sub_f32 v87, 0, v85\n"
    "v_exp_f32 v87, v87\n"
    "v_sub_f32 v88, v86, v85\n"
    "v_exp_f32 v88, v88\n"
    "v_sub_f32 v40, v40, v85\n"
    "v_sub_f32 v41, v41, v85\n"
    "v_sub_f32 v42, v42, v85\n"
    "v_sub_f32 v43, v43, v85\n"
    "v_sub_f32 v60, v60, v85\n"
    "v_sub_f32 v61, v61, v85\n"
    "v_sub_f32 v62, v62, v85\n"
    "v_sub_f32 v63, v63, v85\n"
    "v_sub_f32 v20, v20, v85\n"
    "v_sub_f32 v21, v21, v85\n"
    "v_sub_f32 v22, v22, v85\n"
    "v_sub_f32 v23, v23, v85\n"
    "v_mul_f32 v96, v96, v87\n"
    "v_mul_f32 v97, v97, v87\n"
    "v_mul_f32 v98, v98, v87\n"
    "v_mul_f32 v99, v99, v87\n"
    "v_mul_f32 v116, v116, v87\n"
    "v_mul_f32 v117, v117, v87\n"
    "v_mul_f32 v118, v118, v87\n"
    "v_mul_f32 v119, v119, v87\n"
    "v_mul_f32 v136, v136, v87\n"
    "v_mul_f32 v137, v137, v87\n"
    "v_mul_f32 v138, v138, v87\n"
    "v_mul_f32 v139, v139, v87\n"
    "v_mul_f32 v156, v156, v87\n"
    "v_mul_f32 v157, v157, v87\n"
    "v_mul_f32 v158, v158, v87\n"
    "v_mul_f32 v159, v159, v87\n"
    "v_mul_f32 v176, v176, v87\n"
    "v_mul_f32 v177, v177, v87\n"
    "v_mul_f32 v178, v178, v87\n"
    "v_mul_f32 v179, v179, v87\n"
    "v_mul_f32 v196, v196, v87\n"
    "v_mul_f32 v197, v197, v87\n"
    "v_mul_f32 v198, v198, v87\n"
    "v_mul_f32 v199, v199, v87\n"
    "v_mul_f32 v216, v216, v87\n"
    "v_mul_f32 v217, v217, v87\n"
    "v_mul_f32 v218, v218, v87\n"
    "v_mul_f32 v219, v219, v87\n"
    "v_mul_f32 v236, v236, v87\n"
    "v_mul_f32 v237, v237, v87\n"
    "v_mul_f32 v238, v238, v87\n"
    "v_mul_f32 v239, v239, v87\n"
    "v_accvgpr_read_b32 v89, a160\n"
    "v_mul_f32 v89, v89, v87\n"
    "v_accvgpr_write_b32 a160, v89\n"
    "v_accvgpr_read_b32 v89, a161\n"
    "v_mul_f32 v89, v89, v87\n"
    "v_accvgpr_write_b32 a161, v89\n"
    "v_accvgpr_read_b32 v89, a162\n"
    "v_mul_f32 v89, v89, v87\n"
    "v_accvgpr_write_b32 a162, v89\n"
    "v_accvgpr_read_b32 v89, a163\n"
    "v_mul_f32 v89, v89, v87\n"
    "v_accvgpr_write_b32 a163, v89\n"
    "v_lshlrev_b32 v89, 16, v80\n"
    "v_and_b32 v90, 0xffff0000, v80\n"
    "v_mul_f32 v89, v89, v88\n"
    "v_mul_f32 v90, v90, v88\n"
    "v_cvt_pk_bf16_f32 v80, v89, v90\n"
    "v_lshlrev_b32 v89, 16, v81\n"
    "v_and_b32 v90, 0xffff0000, v81\n"
    "v_mul_f32 v89, v89, v88\n"
    "v_mul_f32 v90, v90, v88\n"
    "v_cvt_pk_bf16_f32 v81, v89, v90\n"
    "v_lshlrev_b32 v89, 16, v82\n"
    "v_and_b32 v90, 0xffff0000, v82\n"
    "v_mul_f32 v89, v89, v88\n"
    "v_mul_f32 v90, v90, v88\n"
    "v_cvt_pk_bf16_f32 v82, v89, v90\n"
    "v_lshlrev_b32 v89, 16, v83\n"
    "v_and_b32 v90, 0xffff0000, v83\n"
    "v_mul_f32 v89, v89, v88\n"
    "v_mul_f32 v90, v90, v88\n"
    "v_cvt_pk_bf16_f32 v83, v89, v90\n"
    "ds_read_b32 v86, v13 offset:256\n"
    "v_max3_f32 v84, v44, v45, v46\n"
    "v_max_f32 v84, v84, v47\n"
    "v_mov_b32 v89, v84\n"
    "s_nop 1\n"
    "v_permlane16_swap_b32 v84, v89\n"
    "v_max_f32 v84, v84, v89\n"
    "v_mov_b32 v89, v84\n"
    "s_nop 1\n"
    "v_permlane32_swap_b32 v84, v89\n"
    "v_max_f32 v84, v84, v89\n"
    "v_cmp_lt_f32 vcc, s74, v84\n"
    "s_nop 1\n"
    "v_cndmask_b32 v85, 0, v84, vcc\n"
    "s_cmp_lg_u64 vcc, 0\n"
    "s_cselect_b32 s77, 1, s77\n"
    "s_waitcnt lgkmcnt(0)\n"
    "v_sub_f32 v86, v24, v86\n"
    "v_max_f32 v85, v85, v86\n"
    "v_sub_f32 v87, 0, v85\n"
    "v_exp_f32 v87, v87\n"
    "v_sub_f32 v88, v86, v85\n"
    "v_exp_f32 v88, v88\n"
    "v_sub_f32 v44, v44, v85\n"
    "v_sub_f32 v45, v45, v85\n"
    "v_sub_f32 v46, v46, v85\n"
    "v_sub_f32 v47, v47, v85\n"
    "v_sub_f32 v64, v64, v85\n"
    "v_sub_f32 v65, v65, v85\n"
    "v_sub_f32 v66, v66, v85\n"
    "v_sub_f32 v67, v67, v85\n"
    "v_sub_f32 v24, v24, v85\n"
    "v_sub_f32 v25, v25, v85\n"
    "v_sub_f32 v26, v26, v85\n"
    "v_sub_f32 v27, v27, v85\n"
    "v_mul_f32 v100, v100, v87\n"
    "v_mul_f32 v101, v101, v87\n"
    "v_mul_f32 v102, v102, v87\n"
    "v_mul_f32 v103, v103, v87\n"
    "v_mul_f32 v120, v120, v87\n"
    "v_mul_f32 v121, v121, v87\n"
    "v_mul_f32 v122, v122, v87\n"
    "v_mul_f32 v123, v123, v87\n"
    "v_mul_f32 v140, v140, v87\n"
    "v_mul_f32 v141, v141, v87\n"
    "v_mul_f32 v142, v142, v87\n"
    "v_mul_f32 v143, v143, v87\n"
    "v_mul_f32 v160, v160, v87\n"
    "v_mul_f32 v161, v161, v87\n"
    "v_mul_f32 v162, v162, v87\n"
    "v_mul_f32 v163, v163, v87\n"
    "v_mul_f32 v180, v180, v87\n"
    "v_mul_f32 v181, v181, v87\n"
    "v_mul_f32 v182, v182, v87\n"
    "v_mul_f32 v183, v183, v87\n"
    "v_mul_f32 v200, v200, v87\n"
    "v_mul_f32 v201, v201, v87\n"
    "v_mul_f32 v202, v202, v87\n"
    "v_mul_f32 v203, v203, v87\n"
    "v_mul_f32 v220, v220, v87\n"
    "v_mul_f32 v221, v221, v87\n"
    "v_mul_f32 v222, v222, v87\n"
    "v_mul_f32 v223, v223, v87\n"
    "v_mul_f32 v240, v240, v87\n"
    "v_mul_f32 v241, v241, v87\n"
    "v_mul_f32 v242, v242, v87\n"
    "v_mul_f32 v243, v243, v87\n"
    "v_accvgpr_read_b32 v89, a164\n"
    "v_mul_f32 v89, v89, v87\n"
    "v_accvgpr_write_b32 a164, v89\n"
    "v_accvgpr_read_b32 v89, a165\n"
    "v_mul_f32 v89, v89, v87\n"
    "v_accvgpr_write_b32 a165, v89\n"
    "v_accvgpr_read_b32 v89, a166\n"
    "v_mul_f32 v89, v89, v87\n"
    "v_accvgpr_write_b32 a166, v89\n"
    "v_accvgpr_read_b32 v89, a167\n"
    "v_mul_f32 v89, v89, v87\n"
    "v_accvgpr_write_b32 a167, v89\n"
    "v_accvgpr_read_b32 v90, a228\n"
    "v_lshlrev_b32 v89, 16, v90\n"
    "v_and_b32 v90, 0xffff0000, v90\n"
    "v_mul_f32 v89, v89, v88\n"
    "v_mul_f32 v90, v90, v88\n"
    "v_cvt_pk_bf16_f32 v89, v89, v90\n"
    "v_accvgpr_write_b32 a228, v89\n"
    "v_accvgpr_read_b32 v90, a229\n"
    "v_lshlrev_b32 v89, 16, v90\n"
    "v_and_b32 v90, 0xffff0000, v90\n"
    "v_mul_f32 v89, v89, v88\n"
    "v_mul_f32 v90, v90, v88\n"
    "v_cvt_pk_bf16_f32 v89, v89, v90\n"
    "v_accvgpr_write_b32 a229, v89\n"
    "v_accvgpr_read_b32 v90, a230\n"
    "v_lshlrev_b32 v89, 16, v90\n"
    "v_and_b32 v90, 0xffff0000, v90\n"
    "v_mul_f32 v89, v89, v88\n"
    "v_mul_f32 v90, v90, v88\n"
    "v_cvt_pk_bf16_f32 v89, v89, v90\n"
    "v_accvgpr_write_b32 a230, v89\n"
    "v_accvgpr_read_b32 v90, a231\n"
    "v_lshlrev_b32 v89, 16, v90\n"
    "v_and_b32 v90, 0xffff0000, v90\n"
    "v_mul_f32 v89, v89, v88\n"
    "v_mul_f32 v90, v90, v88\n"
    "v_cvt_pk_bf16_f32 v89, v89, v90\n"
    "v_accvgpr_write_b32 a231, v89\n"
    "ds_read_b32 v86, v13 offset:512\n"
    "v_max3_f32 v84, v48, v49, v50\n"
    "v_max_f32 v84, v84, v51\n"
    "v_mov_b32 v89, v84\n"
    "s_nop 1\n"
    "v_permlane16_swap_b32 v84, v89\n"
    "v_max_f32 v84, v84, v89\n"
    "v_mov_b32 v89, v84\n"
    "s_nop 1\n"
    "v_permlane32_swap_b32 v84, v89\n"
    "v_max_f32 v84, v84, v89\n"
    "v_cmp_lt_f32 vcc, s74, v84\n"
    "s_nop 1\n"
    "v_cndmask_b32 v85, 0, v84, vcc\n"
    "s_cmp_lg_u64 vcc, 0\n"
    "s_cselect_b32 s77, 1, s77\n"
    "s_waitcnt lgkmcnt(0)\n"
    "v_sub_f32 v86, v28, v86\n"
    "v_max_f32 v85, v85, v86\n"
    "v_sub_f32 v87, 0, v85\n"
    "v_exp_f32 v87, v87\n"
    "v_sub_f32 v88, v86, v85\n"
    "v_exp_f32 v88, v88\n"
    "v_sub_f32 v48, v48, v85\n"
    "v_sub_f32 v49, v49, v85\n"
    "v_sub_f32 v50, v50, v85\n"
    "v_sub_f32 v51, v51, v85\n"
    "v_sub_f32 v68, v68, v85\n"
    "v_sub_f32 v69, v69, v85\n"
    "v_sub_f32 v70, v70, v85\n"
    "v_sub_f32 v71, v71, v85\n"
    "v_sub_f32 v28, v28, v85\n"
    "v_sub_f32 v29, v29, v85\n"
    "v_sub_f32 v30, v30, v85\n"
    "v_sub_f32 v31, v31, v85\n"
    "v_mul_f32 v104, v104, v87\n"
    "v_mul_f32 v105, v105, v87\n"
    "v_mul_f32 v106, v106, v87\n"
    "v_mul_f32 v107, v107, v87\n"
    "v_mul_f32 v124, v124, v87\n"
    "v_mul_f32 v125, v125, v87\n"
    "v_mul_f32 v126, v126, v87\n"
    "v_mul_f32 v127, v127, v87\n"
    "v_mul_f32 v144, v144, v87\n"
    "v_mul_f32 v145, v145, v87\n"
    "v_mul_f32 v146, v146, v87\n"
    "v_mul_f32 v147, v147, v87\n"
    "v_mul_f32 v164, v164, v87\n"
    "v_mul_f32 v165, v165, v87\n"
    "v_mul_f32 v166, v166, v87\n"
    "v_mul_f32 v167, v167, v87\n"
    "v_mul_f32 v184, v184, v87\n"
    "v_mul_f32 v185, v185, v87\n"
    "v_mul_f32 v186, v186, v87\n"
    "v_mul_f32 v187, v187, v87\n"
    "v_mul_f32 v204, v204, v87\n"
    "v_mul_f32 v205, v205, v87\n"
    "v_mul_f32 v206, v206, v87\n"
    "v_mul_f32 v207, v207, v87\n"
    "v_mul_f32 v224, v224, v87\n"
    "v_mul_f32 v225, v225, v87\n"
    "v_mul_f32 v226, v226, v87\n"
    "v_mul_f32 v227, v227, v87\n"
    "v_mul_f32 v244, v244, v87\n"
    "v_mul_f32 v245, v245, v87\n"
    "v_mul_f32 v246, v246, v87\n"
    "v_mul_f32 v247, v247, v87\n"
    "v_accvgpr_read_b32 v89, a168\n"
    "v_mul_f32 v89, v89, v87\n"
    "v_accvgpr_write_b32 a168, v89\n"
    "v_accvgpr_read_b32 v89, a169\n"
    "v_mul_f32 v89, v89, v87\n"
    "v_accvgpr_write_b32 a169, v89\n"
    "v_accvgpr_read_b32 v89, a170\n"
    "v_mul_f32 v89, v89, v87\n"
    "v_accvgpr_write_b32 a170, v89\n"
    "v_accvgpr_read_b32 v89, a171\n"
    "v_mul_f32 v89, v89, v87\n"
    "v_accvgpr_write_b32 a171, v89\n"
    "v_accvgpr_read_b32 v90, a232\n"
    "v_lshlrev_b32 v89, 16, v90\n"
    "v_and_b32 v90, 0xffff0000, v90\n"
    "v_mul_f32 v89, v89, v88\n"
    "v_mul_f32 v90, v90, v88\n"
    "v_cvt_pk_bf16_f32 v89, v89, v90\n"
    "v_accvgpr_write_b32 a232, v89\n"
    "v_accvgpr_read_b32 v90, a233\n"
    "v_lshlrev_b32 v89, 16, v90\n"
    "v_and_b32 v90, 0xffff0000, v90\n"
    "v_mul_f32 v89, v89, v88\n"
    "v_mul_f32 v90, v90, v88\n"
    "v_cvt_pk_bf16_f32 v89, v89, v90\n"
    "v_accvgpr_write_b32 a233, v89\n"
    "v_accvgpr_read_b32 v90, a234\n"
    "v_lshlrev_b32 v89, 16, v90\n"
    "v_and_b32 v90, 0xffff0000, v90\n"
    "v_mul_f32 v89, v89, v88\n"
    "v_mul_f32 v90, v90, v88\n"
    "v_cvt_pk_bf16_f32 v89, v89, v90\n"
    "v_accvgpr_write_b32 a234, v89\n"
    "v_accvgpr_read_b32 v90, a235\n"
    "v_lshlrev_b32 v89, 16, v90\n"
    "v_and_b32 v90, 0xffff0000, v90\n"
    "v_mul_f32 v89, v89, v88\n"
    "v_mul_f32 v90, v90, v88\n"
    "v_cvt_pk_bf16_f32 v89, v89, v90\n"
    "v_accvgpr_write_b32 a235, v89\n"
    "ds_read_b32 v86, v13 offset:768\n"
    "v_max3_f32 v84, v52, v53, v54\n"
    "v_max_f32 v84, v84, v55\n"
    "v_mov_b32 v89, v84\n"
    "s_nop 1\n"
    "v_permlane16_swap_b32 v84, v89\n"
    "v_max_f32 v84, v84, v89\n"
    "v_mov_b32 v89, v84\n"
    "s_nop 1\n"
    "v_permlane32_swap_b32 v84, v89\n"
    "v_max_f32 v84, v84, v89\n"
    "v_cmp_lt_f32 vcc, s74, v84\n"
    "s_nop 1\n"
    "v_cndmask_b32 v85, 0, v84, vcc\n"
    "s_cmp_lg_u64 vcc, 0\n"
    "s_cselect_b32 s77, 1, s77\n"
    "s_waitcnt lgkmcnt(0)\n"
    "v_sub_f32 v86, v32, v86\n"
    "v_max_f32 v85, v85, v86\n"
    "v_sub_f32 v87, 0, v85\n"
    "v_exp_f32 v87, v87\n"
    "v_sub_f32 v88, v86, v85\n"
    "v_exp_f32 v88, v88\n"
    "v_sub_f32 v52, v52, v85\n"
    "v_sub_f32 v53, v53, v85\n"
    "v_sub_f32 v54, v54, v85\n"
    "v_sub_f32 v55, v55, v85\n"
    "v_sub_f32 v72, v72, v85\n"
    "v_sub_f32 v73, v73, v85\n"
    "v_sub_f32 v74, v74, v85\n"
    "v_sub_f32 v75, v75, v85\n"
    "v_sub_f32 v32, v32, v85\n"
    "v_sub_f32 v33, v33, v85\n"
    "v_sub_f32 v34, v34, v85\n"
    "v_sub_f32 v35, v35, v85\n"
    "v_mul_f32 v108, v108, v87\n"
    "v_mul_f32 v109, v109, v87\n"
    "v_mul_f32 v110, v110, v87\n"
    "v_mul_f32 v111, v111, v87\n"
    "v_mul_f32 v128, v128, v87\n"
    "v_mul_f32 v129, v129, v87\n"
    "v_mul_f32 v130, v130, v87\n"
    "v_mul_f32 v131, v131, v87\n"
    "v_mul_f32 v148, v148, v87\n"
    "v_mul_f32 v149, v149, v87\n"
    "v_mul_f32 v150, v150, v87\n"
    "v_mul_f32 v151, v151, v87\n"
    "v_mul_f32 v168, v168, v87\n"
    "v_mul_f32 v169, v169, v87\n"
    "v_mul_f32 v170, v170, v87\n"
    "v_mul_f32 v171, v171, v87\n"
    "v_mul_f32 v188, v188, v87\n"
    "v_mul_f32 v189, v189, v87\n"
    "v_mul_f32 v190, v190, v87\n"
    "v_mul_f32 v191, v191, v87\n"
    "v_mul_f32 v208, v208, v87\n"
    "v_mul_f32 v209, v209, v87\n"
    "v_mul_f32 v210, v210, v87\n"
    "v_mul_f32 v211, v211, v87\n"
    "v_mul_f32 v228, v228, v87\n"
    "v_mul_f32 v229, v229, v87\n"
    "v_mul_f32 v230, v230, v87\n"
    "v_mul_f32 v231, v231, v87\n"
    "v_mul_f32 v248, v248, v87\n"
    "v_mul_f32 v249, v249, v87\n"
    "v_mul_f32 v250, v250, v87\n"
    "v_mul_f32 v251, v251, v87\n"
    "v_accvgpr_read_b32 v89, a172\n"
    "v_mul_f32 v89, v89, v87\n"
    "v_accvgpr_write_b32 a172, v89\n"
    "v_accvgpr_read_b32 v89, a173\n"
    "v_mul_f32 v89, v89, v87\n"
    "v_accvgpr_write_b32 a173, v89\n"
    "v_accvgpr_read_b32 v89, a174\n"
    "v_mul_f32 v89, v89, v87\n"
    "v_accvgpr_write_b32 a174, v89\n"
    "v_accvgpr_read_b32 v89, a175\n"
    "v_mul_f32 v89, v89, v87\n"
    "v_accvgpr_write_b32 a175, v89\n"
    "v_accvgpr_read_b32 v90, a236\n"
    "v_lshlrev_b32 v89, 16, v90\n"
    "v_and_b32 v90, 0xffff0000, v90\n"
    "v_mul_f32 v89, v89, v88\n"
    "v_mul_f32 v90, v90, v88\n"
    "v_cvt_pk_bf16_f32 v89, v89, v90\n"
    "v_accvgpr_write_b32 a236, v89\n"
    "v_accvgpr_read_b32 v90, a237\n"
    "v_lshlrev_b32 v89, 16, v90\n"
    "v_and_b32 v90, 0xffff0000, v90\n"
    "v_mul_f32 v89, v89, v88\n"
    "v_mul_f32 v90, v90, v88\n"
    "v_cvt_pk_bf16_f32 v89, v89, v90\n"
    "v_accvgpr_write_b32 a237, v89\n"
    "v_accvgpr_read_b32 v90, a238\n"
    "v_lshlrev_b32 v89, 16, v90\n"
    "v_and_b32 v90, 0xffff0000, v90\n"
    "v_mul_f32 v89, v89, v88\n"
    "v_mul_f32 v90, v90, v88\n"
    "v_cvt_pk_bf16_f32 v89, v89, v90\n"
    "v_accvgpr_write_b32 a238, v89\n"
    "v_accvgpr_read_b32 v90, a239\n"
    "v_lshlrev_b32 v89, 16, v90\n"
    "v_and_b32 v90, 0xffff0000, v90\n"
    "v_mul_f32 v89, v89, v88\n"
    "v_mul_f32 v90, v90, v88\n"
    "v_cvt_pk_bf16_f32 v89, v89, v90\n"
    "v_accvgpr_write_b32 a239, v89\n"
    "ds_read_b32 v86, v13 offset:1024\n"
    "v_max3_f32 v84, v56, v57, v58\n"
    "v_max_f32 v84, v84, v59\n"
    "v_mov_b32 v89, v84\n"
    "s_nop 1\n"
    "v_permlane16_swap_b32 v84, v89\n"
    "v_max_f32 v84, v84, v89\n"
    "v_mov_b32 v89, v84\n"
    "s_nop 1\n"
    "v_permlane32_swap_b32 v84, v89\n"
    "v_max_f32 v84, v84, v89\n"
    "v_cmp_lt_f32 vcc, s74, v84\n"
    "s_nop 1\n"
    "v_cndmask_b32 v85, 0, v84, vcc\n"
    "s_cmp_lg_u64 vcc, 0\n"
    "s_cselect_b32 s77, 1, s77\n"
    "s_waitcnt lgkmcnt(0)\n"
    "v_sub_f32 v86, v36, v86\n"
    "v_max_f32 v85, v85, v86\n"
    "v_sub_f32 v87, 0, v85\n"
    "v_exp_f32 v87, v87\n"
    "v_sub_f32 v88, v86, v85\n"
    "v_exp_f32 v88, v88\n"
    "v_sub_f32 v56, v56, v85\n"
    "v_sub_f32 v57, v57, v85\n"
    "v_sub_f32 v58, v58, v85\n"
    "v_sub_f32 v59, v59, v85\n"
    "v_sub_f32 v76, v76, v85\n"
    "v_sub_f32 v77, v77, v85\n"
    "v_sub_f32 v78, v78, v85\n"
    "v_sub_f32 v79, v79, v85\n"
    "v_sub_f32 v36, v36, v85\n"
    "v_sub_f32 v37, v37, v85\n"
    "v_sub_f32 v38, v38, v85\n"
    "v_sub_f32 v39, v39, v85\n"
    "v_mul_f32 v112, v112, v87\n"
    "v_mul_f32 v113, v113, v87\n"
    "v_mul_f32 v114, v114, v87\n"
    "v_mul_f32 v115, v115, v87\n"
    "v_mul_f32 v132, v132, v87\n"
    "v_mul_f32 v133, v133, v87\n"
    "v_mul_f32 v134, v134, v87\n"
    "v_mul_f32 v135, v135, v87\n"
    "v_mul_f32 v152, v152, v87\n"
    "v_mul_f32 v153, v153, v87\n"
    "v_mul_f32 v154, v154, v87\n"
    "v_mul_f32 v155, v155, v87\n"
    "v_mul_f32 v172, v172, v87\n"
    "v_mul_f32 v173, v173, v87\n"
    "v_mul_f32 v174, v174, v87\n"
    "v_mul_f32 v175, v175, v87\n"
    "v_mul_f32 v192, v192, v87\n"
    "v_mul_f32 v193, v193, v87\n"
    "v_mul_f32 v194, v194, v87\n"
    "v_mul_f32 v195, v195, v87\n"
    "v_mul_f32 v212, v212, v87\n"
    "v_mul_f32 v213, v213, v87\n"
    "v_mul_f32 v214, v214, v87\n"
    "v_mul_f32 v215, v215, v87\n"
    "v_mul_f32 v232, v232, v87\n"
    "v_mul_f32 v233, v233, v87\n"
    "v_mul_f32 v234, v234, v87\n"
    "v_mul_f32 v235, v235, v87\n"
    "v_mul_f32 v252, v252, v87\n"
    "v_mul_f32 v253, v253, v87\n"
    "v_mul_f32 v254, v254, v87\n"
    "v_mul_f32 v255, v255, v87\n"
    "v_accvgpr_read_b32 v89, a176\n"
    "v_mul_f32 v89, v89, v87\n"
    "v_accvgpr_write_b32 a176, v89\n"
    "v_accvgpr_read_b32 v89, a177\n"
    "v_mul_f32 v89, v89, v87\n"
    "v_accvgpr_write_b32 a177, v89\n"
    "v_accvgpr_read_b32 v89, a178\n"
    "v_mul_f32 v89, v89, v87\n"
    "v_accvgpr_write_b32 a178, v89\n"
    "v_accvgpr_read_b32 v89, a179\n"
    "v_mul_f32 v89, v89, v87\n"
    "v_accvgpr_write_b32 a179, v89\n"
    "v_accvgpr_read_b32 v90, a240\n"
    "v_lshlrev_b32 v89, 16, v90\n"
    "v_and_b32 v90, 0xffff0000, v90\n"
    "v_mul_f32 v89, v89, v88\n"
    "v_mul_f32 v90, v90, v88\n"
    "v_cvt_pk_bf16_f32 v89, v89, v90\n"
    "v_accvgpr_write_b32 a240, v89\n"
    "v_accvgpr_read_b32 v90, a241\n"
    "v_lshlrev_b32 v89, 16, v90\n"
    "v_and_b32 v90, 0xffff0000, v90\n"
    "v_mul_f32 v89, v89, v88\n"
    "v_mul_f32 v90, v90, v88\n"
    "v_cvt_pk_bf16_f32 v89, v89, v90\n"
    "v_accvgpr_write_b32 a241, v89\n"
    "v_accvgpr_read_b32 v90, a242\n"
    "v_lshlrev_b32 v89, 16, v90\n"
    "v_and_b32 v90, 0xffff0000, v90\n"
    "v_mul_f32 v89, v89, v88\n"
    "v_mul_f32 v90, v90, v88\n"
    "v_cvt_pk_bf16_f32 v89, v89, v90\n"
    "v_accvgpr_write_b32 a242, v89\n"
    "v_accvgpr_read_b32 v90, a243\n"
    "v_lshlrev_b32 v89, 16, v90\n"
    "v_and_b32 v90, 0xffff0000, v90\n"
    "v_mul_f32 v89, v89, v88\n"
    "v_mul_f32 v90, v90, v88\n"
    "v_cvt_pk_bf16_f32 v89, v89, v90\n"
    "v_accvgpr_write_b32 a243, v89\n"
    "s_mov_b32 s73, s74\n"
    "s_mov_b32 s85, 0xff800000\n"
    "s_waitcnt lgkmcnt(0)\n"
    "s_nop 7\n"
    "s_cmp_eq_u32 s78, 0\n"
    "s_cbranch_scc1 R0R_o_%=\n"
    "s_cmp_eq_u32 s78, 1\n"
    "s_cbranch_scc1 R0R_e_%=\n"
    "s_cmp_eq_u32 s78, 2\n"
    "s_cbranch_scc1 R0R_x_%=\n"
    "s_branch R0R_y_%=\n"
    "RD_d_%=:\n"
    "s_mov_b32 s78, 0\n"
    "s_branch RD_BODY_%=\n"
    "RD_BODY_%=:\n"
    "s_nop 15\n"
    "s_waitcnt lgkmcnt(0)\n"
    "ds_read_b32 v86, v13 offset:0\n"
    "v_mov_b32 v85, 0\n"
    "s_waitcnt lgkmcnt(0)\n"
    "v_sub_f32 v86, v20, v86\n"
    "v_max_f32 v85, v85, v86\n"
    "v_sub_f32 v87, 0, v85\n"
    "v_exp_f32 v87, v87\n"
    "v_sub_f32 v88, v86, v85\n"
    "v_exp_f32 v88, v88\n"
    "v_sub_f32 v20, v20, v85\n"
    "v_sub_f32 v21, v21, v85\n"
    "v_sub_f32 v22, v22, v85\n"
    "v_sub_f32 v23, v23, v85\n"
    "v_mul_f32 v96, v96, v87\n"
    "v_mul_f32 v97, v97, v87\n"
    "v_mul_f32 v98, v98, v87\n"
    "v_mul_f32 v99, v99, v87\n"
    "v_mul_f32 v116, v116, v87\n"
    "v_mul_f32 v117, v117, v87\n"
    "v_mul_f32 v118, v118, v87\n"
    "v_mul_f32 v119, v119, v87\n"
    "v_mul_f32 v136, v136, v87\n"
    "v_mul_f32 v137, v137, v87\n"
    "v_mul_f32 v138, v138, v87\n"
    "v_mul_f32 v139, v139, v87\n"
    "v_mul_f32 v156, v156, v87\n"
    "v_mul_f32 v157, v157, v87\n"
    "v_mul_f32 v158, v158, v87\n"
    "v_mul_f32 v159, v159, v87\n"
    "v_mul_f32 v176, v176, v87\n"
    "v_mul_f32 v177, v177, v87\n"
    "v_mul_f32 v178, v178, v87\n"
    "v_mul_f32 v179, v179, v87\n"
    "v_mul_f32 v196, v196, v87\n"
    "v_mul_f32 v197, v197, v87\n"
    "v_mul_f32 v198, v198, v87\n"
    "v_mul_f32 v199, v199, v87\n"
    "v_mul_f32 v216, v216, v87\n"
    "v_mul_f32 v217, v217, v87\n"
    "v_mul_f32 v218, v218, v87\n"
    "v_mul_f32 v219, v219, v87\n"
    "v_mul_f32 v236, v236, v87\n"
    "v_mul_f32 v237, v237, v87\n"
    "v_mul_f32 v238, v238, v87\n"
    "v_mul_f32 v239, v239, v87\n"
    "v_accvgpr_read_b32 v89, a160\n"
    "v_mul_f32 v89, v89, v87\n"
    "v_accvgpr_write_b32 a160, v89\n"
    "v_accvgpr_read_b32 v89, a161\n"
    "v_mul_f32 v89, v89, v87\n"
    "v_accvgpr_write_b32 a161, v89\n"
    "v_accvgpr_read_b32 v89, a162\n"
    "v_mul_f32 v89, v89, v87\n"
    "v_accvgpr_write_b32 a162, v89\n"
    "v_accvgpr_read_b32 v89, a163\n"
    "v_mul_f32 v89, v89, v87\n"
    "v_accvgpr_write_b32 a163, v89\n"
    "v_lshlrev_b32 v89, 16, v80\n"
    "v_and_b32 v90, 0xffff0000, v80\n"
    "v_mul_f32 v89, v89, v88\n"
    "v_mul_f32 v90, v90, v88\n"
    "v_cvt_pk_bf16_f32 v80, v89, v90\n"
    "v_lshlrev_b32 v89, 16, v81\n"
    "v_and_b32 v90, 0xffff0000, v81\n"
    "v_mul_f32 v89, v89, v88\n"
    "v_mul_f32 v90, v90, v88\n"
    "v_cvt_pk_bf16_f32 v81, v89, v90\n"
    "v_lshlrev_b32 v89, 16, v82\n"
    "v_and_b32 v90, 0xffff0000, v82\n"
    "v_mul_f32 v89, v89, v88\n"
    "v_mul_f32 v90, v90, v88\n"
    "v_cvt_pk_bf16_f32 v82, v89, v90\n"
    "v_lshlrev_b32 v89, 16, v83\n"
    "v_and_b32 v90, 0xffff0000, v83\n"
    "v_mul_f32 v89, v89, v88\n"
    "v_mul_f32 v90, v90, v88\n"
    "v_cvt_pk_bf16_f32 v83, v89, v90\n"
    "ds_read_b32 v86, v13 offset:256\n"
    "v_mov_b32 v85, 0\n"
    "s_waitcnt lgkmcnt(0)\n"
    "v_sub_f32 v86, v24, v86\n"
    "v_max_f32 v85, v85, v86\n"
    "v_sub_f32 v87, 0, v85\n"
    "v_exp_f32 v87, v87\n"
    "v_sub_f32 v88, v86, v85\n"
    "v_exp_f32 v88, v88\n"
    "v_sub_f32 v24, v24, v85\n"
    "v_sub_f32 v25, v25, v85\n"
    "v_sub_f32 v26, v26, v85\n"
    "v_sub_f32 v27, v27, v85\n"
    "v_mul_f32 v100, v100, v87\n"
    "v_mul_f32 v101, v101, v87\n"
    "v_mul_f32 v102, v102, v87\n"
    "v_mul_f32 v103, v103, v87\n"
    "v_mul_f32 v120, v120, v87\n"
    "v_mul_f32 v121, v121, v87\n"
    "v_mul_f32 v122, v122, v87\n"
    "v_mul_f32 v123, v123, v87\n"
    "v_mul_f32 v140, v140, v87\n"
    "v_mul_f32 v141, v141, v87\n"
    "v_mul_f32 v142, v142, v87\n"
    "v_mul_f32 v143, v143, v87\n"
    "v_mul_f32 v160, v160, v87\n"
    "v_mul_f32 v161, v161, v87\n"
    "v_mul_f32 v162, v162, v87\n"
    "v_mul_f32 v163, v163, v87\n"
    "v_mul_f32 v180, v180, v87\n"
    "v_mul_f32 v181, v181, v87\n"
    "v_mul_f32 v182, v182, v87\n"
    "v_mul_f32 v183, v183, v87\n"
    "v_mul_f32 v200, v200, v87\n"
    "v_mul_f32 v201, v201, v87\n"
    "v_mul_f32 v202, v202, v87\n"
    "v_mul_f32 v203, v203, v87\n"
    "v_mul_f32 v220, v220, v87\n"
    "v_mul_f32 v221, v221, v87\n"
    "v_mul_f32 v222, v222, v87\n"
    "v_mul_f32 v223, v223, v87\n"
    "v_mul_f32 v240, v240, v87\n"
    "v_mul_f32 v241, v241, v87\n"
    "v_mul_f32 v242, v242, v87\n"
    "v_mul_f32 v243, v243, v87\n"
    "v_accvgpr_read_b32 v89, a164\n"
    "v_mul_f32 v89, v89, v87\n"
    "v_accvgpr_write_b32 a164, v89\n"
    "v_accvgpr_read_b32 v89, a165\n"
    "v_mul_f32 v89, v89, v87\n"
    "v_accvgpr_write_b32 a165, v89\n"
    "v_accvgpr_read_b32 v89, a166\n"
    "v_mul_f32 v89, v89, v87\n"
    "v_accvgpr_write_b32 a166, v89\n"
    "v_accvgpr_read_b32 v89, a167\n"
    "v_mul_f32 v89, v89, v87\n"
    "v_accvgpr_write_b32 a167, v89\n"
    "v_accvgpr_read_b32 v90, a228\n"
    "v_lshlrev_b32 v89, 16, v90\n"
    "v_and_b32 v90, 0xffff0000, v90\n"
    "v_mul_f32 v89, v89, v88\n"
    "v_mul_f32 v90, v90, v88\n"
    "v_cvt_pk_bf16_f32 v89, v89, v90\n"
    "v_accvgpr_write_b32 a228, v89\n"
    "v_accvgpr_read_b32 v90, a229\n"
    "v_lshlrev_b32 v89, 16, v90\n"
    "v_and_b32 v90, 0xffff0000, v90\n"
    "v_mul_f32 v89, v89, v88\n"
    "v_mul_f32 v90, v90, v88\n"
    "v_cvt_pk_bf16_f32 v89, v89, v90\n"
    "v_accvgpr_write_b32 a229, v89\n"
    "v_accvgpr_read_b32 v90, a230\n"
    "v_lshlrev_b32 v89, 16, v90\n"
    "v_and_b32 v90, 0xffff0000, v90\n"
    "v_mul_f32 v89, v89, v88\n"
    "v_mul_f32 v90, v90, v88\n"
    "v_cvt_pk_bf16_f32 v89, v89, v90\n"
    "v_accvgpr_write_b32 a230, v89\n"
    "v_accvgpr_read_b32 v90, a231\n"
    "v_lshlrev_b32 v89, 16, v90\n"
    "v_and_b32 v90, 0xffff0000, v90\n"
    "v_mul_f32 v89, v89, v88\n"
    "v_mul_f32 v90, v90, v88\n"
    "v_cvt_pk_bf16_f32 v89, v89, v90\n"
    "v_accvgpr_write_b32 a231, v89\n"
    "ds_read_b32 v86, v13 offset:512\n"
    "v_mov_b32 v85, 0\n"
    "s_waitcnt lgkmcnt(0)\n"
    "v_sub_f32 v86, v28, v86\n"
    "v_max_f32 v85, v85, v86\n"
    "v_sub_f32 v87, 0, v85\n"
    "v_exp_f32 v87, v87\n"
    "v_sub_f32 v88, v86, v85\n"
    "v_exp_f32 v88, v88\n"
    "v_sub_f32 v28, v28, v85\n"
    "v_sub_f32 v29, v29, v85\n"
    "v_sub_f32 v30, v30, v85\n"
    "v_sub_f32 v31, v31, v85\n"
    "v_mul_f32 v104, v104, v87\n"
    "v_mul_f32 v105, v105, v87\n"
    "v_mul_f32 v106, v106, v87\n"
    "v_mul_f32 v107, v107, v87\n"
    "v_mul_f32 v124, v124, v87\n"
    "v_mul_f32 v125, v125, v87\n"
    "v_mul_f32 v126, v126, v87\n"
    "v_mul_f32 v127, v127, v87\n"
    "v_mul_f32 v144, v144, v87\n"
    "v_mul_f32 v145, v145, v87\n"
    "v_mul_f32 v146, v146, v87\n"
    "v_mul_f32 v147, v147, v87\n"
    "v_mul_f32 v164, v164, v87\n"
    "v_mul_f32 v165, v165, v87\n"
    "v_mul_f32 v166, v166, v87\n"
    "v_mul_f32 v167, v167, v87\n"
    "v_mul_f32 v184, v184, v87\n"
    "v_mul_f32 v185, v185, v87\n"
    "v_mul_f32 v186, v186, v87\n"
    "v_mul_f32 v187, v187, v87\n"
    "v_mul_f32 v204, v204, v87\n"
    "v_mul_f32 v205, v205, v87\n"
    "v_mul_f32 v206, v206, v87\n"
    "v_mul_f32 v207, v207, v87\n"
    "v_mul_f32 v224, v224, v87\n"
    "v_mul_f32 v225, v225, v87\n"
    "v_mul_f32 v226, v226, v87\n"
    "v_mul_f32 v227, v227, v87\n"
    "v_mul_f32 v244, v244, v87\n"
    "v_mul_f32 v245, v245, v87\n"
    "v_mul_f32 v246, v246, v87\n"
    "v_mul_f32 v247, v247, v87\n"
    "v_accvgpr_read_b32 v89, a168\n"
    "v_mul_f32 v89, v89, v87\n"
    "v_accvgpr_write_b32 a168, v89\n"
    "v_accvgpr_read_b32 v89, a169\n"
    "v_mul_f32 v89, v89, v87\n"
    "v_accvgpr_write_b32 a169, v89\n"
    "v_accvgpr_read_b32 v89, a170\n"
    "v_mul_f32 v89, v89, v87\n"
    "v_accvgpr_write_b32 a170, v89\n"
    "v_accvgpr_read_b32 v89, a171\n"
    "v_mul_f32 v89, v89, v87\n"
    "v_accvgpr_write_b32 a171, v89\n"
    "v_accvgpr_read_b32 v90, a232\n"
    "v_lshlrev_b32 v89, 16, v90\n"
    "v_and_b32 v90, 0xffff0000, v90\n"
    "v_mul_f32 v89, v89, v88\n"
    "v_mul_f32 v90, v90, v88\n"
    "v_cvt_pk_bf16_f32 v89, v89, v90\n"
    "v_accvgpr_write_b32 a232, v89\n"
    "v_accvgpr_read_b32 v90, a233\n"
    "v_lshlrev_b32 v89, 16, v90\n"
    "v_and_b32 v90, 0xffff0000, v90\n"
    "v_mul_f32 v89, v89, v88\n"
    "v_mul_f32 v90, v90, v88\n"
    "v_cvt_pk_bf16_f32 v89, v89, v90\n"
    "v_accvgpr_write_b32 a233, v89\n"
    "v_accvgpr_read_b32 v90, a234\n"
    "v_lshlrev_b32 v89, 16, v90\n"
    "v_and_b32 v90, 0xffff0000, v90\n"
    "v_mul_f32 v89, v89, v88\n"
    "v_mul_f32 v90, v90, v88\n"
    "v_cvt_pk_bf16_f32 v89, v89, v90\n"
    "v_accvgpr_write_b32 a234, v89\n"
    "v_accvgpr_read_b32 v90, a235\n"
    "v_lshlrev_b32 v89, 16, v90\n"
    "v_and_b32 v90, 0xffff0000, v90\n"
    "v_mul_f32 v89, v89, v88\n"
    "v_mul_f32 v90, v90, v88\n"
    "v_cvt_pk_bf16_f32 v89, v89, v90\n"
    "v_accvgpr_write_b32 a235, v89\n"
    "ds_read_b32 v86, v13 offset:768\n"
    "v_mov_b32 v85, 0\n"
    "s_waitcnt lgkmcnt(0)\n"
    "v_sub_f32 v86, v32, v86\n"
    "v_max_f32 v85, v85, v86\n"
    "v_sub_f32 v87, 0, v85\n"
    "v_exp_f32 v87, v87\n"
    "v_sub_f32 v88, v86, v85\n"
    "v_exp_f32 v88, v88\n"
    "v_sub_f32 v32, v32, v85\n"
    "v_sub_f32 v33, v33, v85\n"
    "v_sub_f32 v34, v34, v85\n"
    "v_sub_f32 v35, v35, v85\n"
    "v_mul_f32 v108, v108, v87\n"
    "v_mul_f32 v109, v109, v87\n"
    "v_mul_f32 v110, v110, v87\n"
    "v_mul_f32 v111, v111, v87\n"
    "v_mul_f32 v128, v128, v87\n"
    "v_mul_f32 v129, v129, v87\n"
    "v_mul_f32 v130, v130, v87\n"
    "v_mul_f32 v131, v131, v87\n"
    "v_mul_f32 v148, v148, v87\n"
    "v_mul_f32 v149, v149, v87\n"
    "v_mul_f32 v150, v150, v87\n"
    "v_mul_f32 v151, v151, v87\n"
    "v_mul_f32 v168, v168, v87\n"
    "v_mul_f32 v169, v169, v87\n"
    "v_mul_f32 v170, v170, v87\n"
    "v_mul_f32 v171, v171, v87\n"
    "v_mul_f32 v188, v188, v87\n"
    "v_mul_f32 v189, v189, v87\n"
    "v_mul_f32 v190, v190, v87\n"
    "v_mul_f32 v191, v191, v87\n"
    "v_mul_f32 v208, v208, v87\n"
    "v_mul_f32 v209, v209, v87\n"
    "v_mul_f32 v210, v210, v87\n"
    "v_mul_f32 v211, v211, v87\n"
    "v_mul_f32 v228, v228, v87\n"
    "v_mul_f32 v229, v229, v87\n"
    "v_mul_f32 v230, v230, v87\n"
    "v_mul_f32 v231, v231, v87\n"
    "v_mul_f32 v248, v248, v87\n"
    "v_mul_f32 v249, v249, v87\n"
    "v_mul_f32 v250, v250, v87\n"
    "v_mul_f32 v251, v251, v87\n"
    "v_accvgpr_read_b32 v89, a172\n"
    "v_mul_f32 v89, v89, v87\n"
    "v_accvgpr_write_b32 a172, v89\n"
    "v_accvgpr_read_b32 v89, a173\n"
    "v_mul_f32 v89, v89, v87\n"
    "v_accvgpr_write_b32 a173, v89\n"
    "v_accvgpr_read_b32 v89, a174\n"
    "v_mul_f32 v89, v89, v87\n"
    "v_accvgpr_write_b32 a174, v89\n"
    "v_accvgpr_read_b32 v89, a175\n"
    "v_mul_f32 v89, v89, v87\n"
    "v_accvgpr_write_b32 a175, v89\n"
    "v_accvgpr_read_b32 v90, a236\n"
    "v_lshlrev_b32 v89, 16, v90\n"
    "v_and_b32 v90, 0xffff0000, v90\n"
    "v_mul_f32 v89, v89, v88\n"
    "v_mul_f32 v90, v90, v88\n"
    "v_cvt_pk_bf16_f32 v89, v89, v90\n"
    "v_accvgpr_write_b32 a236, v89\n"
    "v_accvgpr_read_b32 v90, a237\n"
    "v_lshlrev_b32 v89, 16, v90\n"
    "v_and_b32 v90, 0xffff0000, v90\n"
    "v_mul_f32 v89, v89, v88\n"
    "v_mul_f32 v90, v90, v88\n"
    "v_cvt_pk_bf16_f32 v89, v89, v90\n"
    "v_accvgpr_write_b32 a237, v89\n"
    "v_accvgpr_read_b32 v90, a238\n"
    "v_lshlrev_b32 v89, 16, v90\n"
    "v_and_b32 v90, 0xffff0000, v90\n"
    "v_mul_f32 v89, v89, v88\n"
    "v_mul_f32 v90, v90, v88\n"
    "v_cvt_pk_bf16_f32 v89, v89, v90\n"
    "v_accvgpr_write_b32 a238, v89\n"
    "v_accvgpr_read_b32 v90, a239\n"
    "v_lshlrev_b32 v89, 16, v90\n"
    "v_and_b32 v90, 0xffff0000, v90\n"
    "v_mul_f32 v89, v89, v88\n"
    "v_mul_f32 v90, v90, v88\n"
    "v_cvt_pk_bf16_f32 v89, v89, v90\n"
    "v_accvgpr_write_b32 a239, v89\n"
    "ds_read_b32 v86, v13 offset:1024\n"
    "v_mov_b32 v85, 0\n"
    "s_waitcnt lgkmcnt(0)\n"
    "v_sub_f32 v86, v36, v86\n"
    "v_max_f32 v85, v85, v86\n"
    "v_sub_f32 v87, 0, v85\n"
    "v_exp_f32 v87, v87\n"
    "v_sub_f32 v88, v86, v85\n"
    "v_exp_f32 v88, v88\n"
    "v_sub_f32 v36, v36, v85\n"
    "v_sub_f32 v37, v37, v85\n"
    "v_sub_f32 v38, v38, v85\n"
    "v_sub_f32 v39, v39, v85\n"
    "v_mul_f32 v112, v112, v87\n"
    "v_mul_f32 v113, v113, v87\n"
    "v_mul_f32 v114, v114, v87\n"
    "v_mul_f32 v115, v115, v87\n"
    "v_mul_f32 v132, v132, v87\n"
    "v_mul_f32 v133, v133, v87\n"
    "v_mul_f32 v134, v134, v87\n"
    "v_mul_f32 v135, v135, v87\n"
    "v_mul_f32 v152, v152, v87\n"
    "v_mul_f32 v153, v153, v87\n"
    "v_mul_f32 v154, v154, v87\n"
    "v_mul_f32 v155, v155, v87\n"
    "v_mul_f32 v172, v172, v87\n"
    "v_mul_f32 v173, v173, v87\n"
    "v_mul_f32 v174, v174, v87\n"
    "v_mul_f32 v175, v175, v87\n"
    "v_mul_f32 v192, v192, v87\n"
    "v_mul_f32 v193, v193, v87\n"
    "v_mul_f32 v194, v194, v87\n"
    "v_mul_f32 v195, v195, v87\n"
    "v_mul_f32 v212, v212, v87\n"
    "v_mul_f32 v213, v213, v87\n"
    "v_mul_f32 v214, v214, v87\n"
    "v_mul_f32 v215, v215, v87\n"
    "v_mul_f32 v232, v232, v87\n"
    "v_mul_f32 v233, v233, v87\n"
    "v_mul_f32 v234, v234, v87\n"
    "v_mul_f32 v235, v235, v87\n"
    "v_mul_f32 v252, v252, v87\n"
    "v_mul_f32 v253, v253, v87\n"
    "v_mul_f32 v254, v254, v87\n"
    "v_mul_f32 v255, v255, v87\n"
    "v_accvgpr_read_b32 v89, a176\n"
    "v_mul_f32 v89, v89, v87\n"
    "v_accvgpr_write_b32 a176, v89\n"
    "v_accvgpr_read_b32 v89, a177\n"
    "v_mul_f32 v89, v89, v87\n"
    "v_accvgpr_write_b32 a177, v89\n"
    "v_accvgpr_read_b32 v89, a178\n"
    "v_mul_f32 v89, v89, v87\n"
    "v_accvgpr_write_b32 a178, v89\n"
    "v_accvgpr_read_b32 v89, a179\n"
    "v_mul_f32 v89, v89, v87\n"
    "v_accvgpr_write_b32 a179, v89\n"
    "v_accvgpr_read_b32 v90, a240\n"
    "v_lshlrev_b32 v89, 16, v90\n"
    "v_and_b32 v90, 0xffff0000, v90\n"
    "v_mul_f32 v89, v89, v88\n"
    "v_mul_f32 v90, v90, v88\n"
    "v_cvt_pk_bf16_f32 v89, v89, v90\n"
    "v_accvgpr_write_b32 a240, v89\n"
    "v_accvgpr_read_b32 v90, a241\n"
    "v_lshlrev_b32 v89, 16, v90\n"
    "v_and_b32 v90, 0xffff0000, v90\n"
    "v_mul_f32 v89, v89, v88\n"
    "v_mul_f32 v90, v90, v88\n"
    "v_cvt_pk_bf16_f32 v89, v89, v90\n"
    "v_accvgpr_write_b32 a241, v89\n"
    "v_accvgpr_read_b32 v90, a242\n"
    "v_lshlrev_b32 v89, 16, v90\n"
    "v_and_b32 v90, 0xffff0000, v90\n"
    "v_mul_f32 v89, v89, v88\n"
    "v_mul_f32 v90, v90, v88\n"
    "v_cvt_pk_bf16_f32 v89, v89, v90\n"
    "v_accvgpr_write_b32 a242, v89\n"
    "v_accvgpr_read_b32 v90, a243\n"
    "v_lshlrev_b32 v89, 16, v90\n"
    "v_and_b32 v90, 0xffff0000, v90\n"
    "v_mul_f32 v89, v89, v88\n"
    "v_mul_f32 v90, v90, v88\n"
    "v_cvt_pk_bf16_f32 v89, v89, v90\n"
    "v_accvgpr_write_b32 a243, v89\n"
    "s_mov_b32 s73, s74\n"
    "s_mov_b32 s85, 0xff800000\n"
    "s_waitcnt lgkmcnt(0)\n"
    "s_nop 7\n"
    "s_branch RDR_d_%=\n"
    "R1_f_%=:\n"
    "s_mov_b32 s78, 0\n"
    "s_branch R1_BODY_%=\n"
    "R1_o_%=:\n"
    "s_mov_b32 s78, 1\n"
    "s_branch R1_BODY_%=\n"
    "R1_e_%=:\n"
    "s_mov_b32 s78, 2\n"
    "s_branch R1_BODY_%=\n"
    "R1_x_%=:\n"
    "s_mov_b32 s78, 3\n"
    "s_branch R1_BODY_%=\n"
    "R1_y_%=:\n"
    "s_mov_b32 s78, 4\n"
    "s_branch R1_BODY_%=\n"
    "R1_BODY_%=:\n"
    "s_nop 15\n"
    "s_waitcnt lgkmcnt(0)\n"
    "v_max3_f32 v84, v60, v61, v62\n"
    "v_max_f32 v84, v84, v63\n"
    "v_mov_b32 v89, v84\n"
    "s_nop 1\n"
    "v_permlane16_swap_b32 v84, v89\n"
    "v_max_f32 v84, v84, v89\n"
    "v_mov_b32 v89, v84\n"
    "s_nop 1\n"
    "v_permlane32_swap_b32 v84, v89\n"
    "v_max_f32 v84, v84, v89\n"
    "v_cmp_lt_f32 vcc, s74, v84\n"
    "s_nop 1\n"
    "v_cndmask_b32 v85, 0, v84, vcc\n"
    "s_cmp_lg_u64 vcc, 0\n"
    "s_cselect_b32 s77, 1, s77\n"
    "v_sub_f32 v87, 0, v85\n"
    "v_exp_f32 v87, v87\n"
    "v_sub_f32 v60, v60, v85\n"
    "v_sub_f32 v61, v61, v85\n"
    "v_sub_f32 v62, v62, v85\n"
    "v_sub_f32 v63, v63, v85\n"
    "v_sub_f32 v20, v20, v85\n"
    "v_sub_f32 v21, v21, v85\n"
    "v_sub_f32 v22, v22, v85\n"
    "v_sub_f32 v23, v23, v85\n"
    "v_mul_f32 v96, v96, v87\n"
    "v_mul_f32 v97, v97, v87\n"
    "v_mul_f32 v98, v98, v87\n"
    "v_mul_f32 v99, v99, v87\n"
    "v_mul_f32 v116, v116, v87\n"
    "v_mul_f32 v117, v117, v87\n"
    "v_mul_f32 v118, v118, v87\n"
    "v_mul_f32 v119, v119, v87\n"
    "v_mul_f32 v136, v136, v87\n"
    "v_mul_f32 v137, v137, v87\n"
    "v_mul_f32 v138, v138, v87\n"
    "v_mul_f32 v139, v139, v87\n"
    "v_mul_f32 v156, v156, v87\n"
    "v_mul_f32 v157, v157, v87\n"
    "v_mul_f32 v158, v158, v87\n"
    "v_mul_f32 v159, v159, v87\n"
    "v_mul_f32 v176, v176, v87\n"
    "v_mul_f32 v177, v177, v87\n"
    "v_mul_f32 v178, v178, v87\n"
    "v_mul_f32 v179, v179, v87\n"
    "v_mul_f32 v196, v196, v87\n"
    "v_mul_f32 v197, v197, v87\n"
    "v_mul_f32 v198, v198, v87\n"
    "v_mul_f32 v199, v199, v87\n"
    "v_mul_f32 v216, v216, v87\n"
    "v_mul_f32 v217, v217, v87\n"
    "v_mul_f32 v218, v218, v87\n"
    "v_mul_f32 v219, v219, v87\n"
    "v_mul_f32 v236, v236, v87\n"
    "v_mul_f32 v237, v237, v87\n"
    "v_mul_f32 v238, v238, v87\n"
    "v_mul_f32 v239, v239, v87\n"
    "v_accvgpr_read_b32 v89, a160\n"
    "v_mul_f32 v89, v89, v87\n"
    "v_accvgpr_write_b32 a160, v89\n"
    "v_accvgpr_read_b32 v89, a161\n"
    "v_mul_f32 v89, v89, v87\n"
    "v_accvgpr_write_b32 a161, v89\n"
    "v_accvgpr_read_b32 v89, a162\n"
    "v_mul_f32 v89, v89, v87\n"
    "v_accvgpr_write_b32 a162, v89\n"
    "v_accvgpr_read_b32 v89, a163\n"
    "v_mul_f32 v89, v89, v87\n"
    "v_accvgpr_write_b32 a163, v89\n"
    "v_lshlrev_b32 v89, 16, v80\n"
    "v_and_b32 v90, 0xffff0000, v80\n"
    "v_mul_f32 v89, v89, v87\n"
    "v_mul_f32 v90, v90, v87\n"
    "v_cvt_pk_bf16_f32 v80, v89, v90\n"
    "v_lshlrev_b32 v89, 16, v81\n"
    "v_and_b32 v90, 0xffff0000, v81\n"
    "v_mul_f32 v89, v89, v87\n"
    "v_mul_f32 v90, v90, v87\n"
    "v_cvt_pk_bf16_f32 v81, v89, v90\n"
    "v_lshlrev_b32 v89, 16, v82\n"
    "v_and_b32 v90, 0xffff0000, v82\n"
    "v_mul_f32 v89, v89, v87\n"
    "v_mul_f32 v90, v90, v87\n"
    "v_cvt_pk_bf16_f32 v82, v89, v90\n"
    "v_lshlrev_b32 v89, 16, v83\n"
    "v_and_b32 v90, 0xffff0000, v83\n"
    "v_mul_f32 v89, v89, v87\n"
    "v_mul_f32 v90, v90, v87\n"
    "v_cvt_pk_bf16_f32 v83, v89, v90\n"
    "v_lshlrev_b32 v89, 16, v40\n"
    "v_and_b32 v90, 0xffff0000, v40\n"
    "v_mul_f32 v89, v89, v87\n"
    "v_mul_f32 v90, v90, v87\n"
    "v_cvt_pk_bf16_f32 v40, v89, v90\n"
    "v_lshlrev_b32 v89, 16, v41\n"
    "v_and_b32 v90, 0xffff0000, v41\n"
    "v_mul_f32 v89, v89, v87\n"
    "v_mul_f32 v90, v90, v87\n"
    "v_cvt_pk_bf16_f32 v41, v89, v90\n"
    "v_max3_f32 v84, v64, v65, v66\n"
    "v_max_f32 v84, v84, v67\n"
    "v_mov_b32 v89, v84\n"
    "s_nop 1\n"
    "v_permlane16_swap_b32 v84, v89\n"
    "v_max_f32 v84, v84, v89\n"
    "v_mov_b32 v89, v84\n"
    "s_nop 1\n"
    "v_permlane32_swap_b32 v84, v89\n"
    "v_max_f32 v84, v84, v89\n"
    "v_cmp_lt_f32 vcc, s74, v84\n"
    "s_nop 1\n"
    "v_cndmask_b32 v85, 0, v84, vcc\n"
    "s_cmp_lg_u64 vcc, 0\n"
    "s_cselect_b32 s77, 1, s77\n"
    "v_sub_f32 v87, 0, v85\n"
    "v_exp_f32 v87, v87\n"
    "v_sub_f32 v64, v64, v85\n"
    "v_sub_f32 v65, v65, v85\n"
    "v_sub_f32 v66, v66, v85\n"
    "v_sub_f32 v67, v67, v85\n"
    "v_sub_f32 v24, v24, v85\n"
    "v_sub_f32 v25, v25, v85\n"
    "v_sub_f32 v26, v26, v85\n"
    "v_sub_f32 v27, v27, v85\n"
    "v_mul_f32 v100, v100, v87\n"
    "v_mul_f32 v101, v101, v87\n"
    "v_mul_f32 v102, v102, v87\n"
    "v_mul_f32 v103, v103, v87\n"
    "v_mul_f32 v120, v120, v87\n"
    "v_mul_f32 v121, v121, v87\n"
    "v_mul_f32 v122, v122, v87\n"
    "v_mul_f32 v123, v123, v87\n"
    "v_mul_f32 v140, v140, v87\n"
    "v_mul_f32 v141, v141, v87\n"
    "v_mul_f32 v142, v142, v87\n"
    "v_mul_f32 v143, v143, v87\n"
    "v_mul_f32 v160, v160, v87\n"
    "v_mul_f32 v161, v161, v87\n"
    "v_mul_f32 v162, v162, v87\n"
    "v_mul_f32 v163, v163, v87\n"
    "v_mul_f32 v180, v180, v87\n"
    "v_mul_f32 v181, v181, v87\n"
    "v_mul_f32 v182, v182, v87\n"
    "v_mul_f32 v183, v183, v87\n"
    "v_mul_f32 v200, v200, v87\n"
    "v_mul_f32 v201, v201, v87\n"
    "v_mul_f32 v202, v202, v87\n"
    "v_mul_f32 v203, v203, v87\n"
    "v_mul_f32 v220, v220, v87\n"
    "v_mul_f32 v221, v221, v87\n"
    "v_mul_f32 v222, v222, v87\n"
    "v_mul_f32 v223, v223, v87\n"
    "v_mul_f32 v240, v240, v87\n"
    "v_mul_f32 v241, v241, v87\n"
    "v_mul_f32 v242, v242, v87\n"
    "v_mul_f32 v243, v243, v87\n"
    "v_accvgpr_read_b32 v89, a164\n"
    "v_mul_f32 v89, v89, v87\n"
    "v_accvgpr_write_b32 a164, v89\n"
    "v_accvgpr_read_b32 v89, a165\n"
    "v_mul_f32 v89, v89, v87\n"
    "v_accvgpr_write_b32 a165, v89\n"
    "v_accvgpr_read_b32 v89, a166\n"
    "v_mul_f32 v89, v89, v87\n"
    "v_accvgpr_write_b32 a166, v89\n"
    "v_accvgpr_read_b32 v89, a167\n"
    "v_mul_f32 v89, v89, v87\n"
    "v_accvgpr_write_b32 a167, v89\n"
    "v_accvgpr_read_b32 v90, a228\n"
    "v_lshlrev_b32 v89, 16, v90\n"
    "v_and_b32 v90, 0xffff0000, v90\n"
    "v_mul_f32 v89, v89, v87\n"
    "v_mul_f32 v90, v90, v87\n"
    "v_cvt_pk_bf16_f32 v89, v89, v90\n"
    "v_accvgpr_write_b32 a228, v89\n"
    "v_accvgpr_read_b32 v90, a229\n"
    "v_lshlrev_b32 v89, 16, v90\n"
    "v_and_b32 v90, 0xffff0000, v90\n"
    "v_mul_f32 v89, v89, v87\n"
    "v_mul_f32 v90, v90, v87\n"
    "v_cvt_pk_bf16_f32 v89, v89, v90\n"
    "v_accvgpr_write_b32 a229, v89\n"
    "v_accvgpr_read_b32 v90, a230\n"
    "v_lshlrev_b32 v89, 16, v90\n"
    "v_and_b32 v90, 0xffff0000, v90\n"
    "v_mul_f32 v89, v89, v87\n"
    "v_mul_f32 v90, v90, v87\n"
    "v_cvt_pk_bf16_f32 v89, v89, v90\n"
    "v_accvgpr_write_b32 a230, v89\n"
    "v_accvgpr_read_b32 v90, a231\n"
    "v_lshlrev_b32 v89, 16, v90\n"
    "v_and_b32 v90, 0xffff0000, v90\n"
    "v_mul_f32 v89, v89, v87\n"
    "v_mul_f32 v90, v90, v87\n"
    "v_cvt_pk_bf16_f32 v89, v89, v90\n"
    "v_accvgpr_write_b32 a231, v89\n"
    "v_lshlrev_b32 v89, 16, v44\n"
    "v_and_b32 v90, 0xffff0000, v44\n"
    "v_mul_f32 v89, v89, v87\n"
    "v_mul_f32 v90, v90, v87\n"
    "v_cvt_pk_bf16_f32 v44, v89, v90\n"
    "v_lshlrev_b32 v89, 16, v45\n"
    "v_and_b32 v90, 0xffff0000, v45\n"
    "v_mul_f32 v89, v89, v87\n"
    "v_mul_f32 v90, v90, v87\n"
    "v_cvt_pk_bf16_f32 v45, v89, v90\n"
    "v_max3_f32 v84, v68, v69, v70\n"
    "v_max_f32 v84, v84, v71\n"
    "v_mov_b32 v89, v84\n"
    "s_nop 1\n"
    "v_permlane16_swap_b32 v84, v89\n"
    "v_max_f32 v84, v84, v89\n"
    "v_mov_b32 v89, v84\n"
    "s_nop 1\n"
    "v_permlane32_swap_b32 v84, v89\n"
    "v_max_f32 v84, v84, v89\n"
    "v_cmp_lt_f32 vcc, s74, v84\n"
    "s_nop 1\n"
    "v_cndmask_b32 v85, 0, v84, vcc\n"
    "s_cmp_lg_u64 vcc, 0\n"
    "s_cselect_b32 s77, 1, s77\n"
    "v_sub_f32 v87, 0, v85\n"
    "v_exp_f32 v87, v87\n"
    "v_sub_f32 v68, v68, v85\n"
    "v_sub_f32 v69, v69, v85\n"
    "v_sub_f32 v70, v70, v85\n"
    "v_sub_f32 v71, v71, v85\n"
    "v_sub_f32 v28, v28, v85\n"
    "v_sub_f32 v29, v29, v85\n"
    "v_sub_f32 v30, v30, v85\n"
    "v_sub_f32 v31, v31, v85\n"
    "v_mul_f32 v104, v104, v87\n"
    "v_mul_f32 v105, v105, v87\n"
    "v_mul_f32 v106, v106, v87\n"
    "v_mul_f32 v107, v107, v87\n"
    "v_mul_f32 v124, v124, v87\n"
    "v_mul_f32 v125, v125, v87\n"
    "v_mul_f32 v126, v126, v87\n"
    "v_mul_f32 v127, v127, v87\n"
    "v_mul_f32 v144, v144, v87\n"
    "v_mul_f32 v145, v145, v87\n"
    "v_mul_f32 v146, v146, v87\n"
    "v_mul_f32 v147, v147, v87\n"
    "v_mul_f32 v164, v164, v87\n"
    "v_mul_f32 v165, v165, v87\n"
    "v_mul_f32 v166, v166, v87\n"
    "v_mul_f32 v167, v167, v87\n"
    "v_mul_f32 v184, v184, v87\n"
    "v_mul_f32 v185, v185, v87\n"
    "v_mul_f32 v186, v186, v87\n"
    "v_mul_f32 v187, v187, v87\n"
    "v_mul_f32 v204, v204, v87\n"
    "v_mul_f32 v205, v205, v87\n"
    "v_mul_f32 v206, v206, v87\n"
    "v_mul_f32 v207, v207, v87\n"
    "v_mul_f32 v224, v224, v87\n"
    "v_mul_f32 v225, v225, v87\n"
    "v_mul_f32 v226, v226, v87\n"
    "v_mul_f32 v227, v227, v87\n"
    "v_mul_f32 v244, v244, v87\n"
    "v_mul_f32 v245, v245, v87\n"
    "v_mul_f32 v246, v246, v87\n"
    "v_mul_f32 v247, v247, v87\n"
    "v_accvgpr_read_b32 v89, a168\n"
    "v_mul_f32 v89, v89, v87\n"
    "v_accvgpr_write_b32 a168, v89\n"
    "v_accvgpr_read_b32 v89, a169\n"
    "v_mul_f32 v89, v89, v87\n"
    "v_accvgpr_write_b32 a169, v89\n"
    "v_accvgpr_read_b32 v89, a170\n"
    "v_mul_f32 v89, v89, v87\n"
    "v_accvgpr_write_b32 a170, v89\n"
    "v_accvgpr_read_b32 v89, a171\n"
    "v_mul_f32 v89, v89, v87\n"
    "v_accvgpr_write_b32 a171, v89\n"
    "v_accvgpr_read_b32 v90, a232\n"
    "v_lshlrev_b32 v89, 16, v90\n"
    "v_and_b32 v90, 0xffff0000, v90\n"
    "v_mul_f32 v89, v89, v87\n"
    "v_mul_f32 v90, v90, v87\n"
    "v_cvt_pk_bf16_f32 v89, v89, v90\n"
    "v_accvgpr_write_b32 a232, v89\n"
    "v_accvgpr_read_b32 v90, a233\n"
    "v_lshlrev_b32 v89, 16, v90\n"
    "v_and_b32 v90, 0xffff0000, v90\n"
    "v_mul_f32 v89, v89, v87\n"
    "v_mul_f32 v90, v90, v87\n"
    "v_cvt_pk_bf16_f32 v89, v89, v90\n"
    "v_accvgpr_write_b32 a233, v89\n"
    "v_accvgpr_read_b32 v90, a234\n"
    "v_lshlrev_b32 v89, 16, v90\n"
    "v_and_b32 v90, 0xffff0000, v90\n"
    "v_mul_f32 v89, v89, v87\n"
    "v_mul_f32 v90, v90, v87\n"
    "v_cvt_pk_bf16_f32 v89, v89, v90\n"
    "v_accvgpr_write_b32 a234, v89\n"
    "v_accvgpr_read_b32 v90, a235\n"
    "v_lshlrev_b32 v89, 16, v90\n"
    "v_and_b32 v90, 0xffff0000, v90\n"
    "v_mul_f32 v89, v89, v87\n"
    "v_mul_f32 v90, v90, v87\n"
    "v_cvt_pk_bf16_f32 v89, v89, v90\n"
    "v_accvgpr_write_b32 a235, v89\n"
    "v_lshlrev_b32 v89, 16, v48\n"
    "v_and_b32 v90, 0xffff0000, v48\n"
    "v_mul_f32 v89, v89, v87\n"
    "v_mul_f32 v90, v90, v87\n"
    "v_cvt_pk_bf16_f32 v48, v89, v90\n"
    "v_lshlrev_b32 v89, 16, v49\n"
    "v_and_b32 v90, 0xffff0000, v49\n"
    "v_mul_f32 v89, v89, v87\n"
    "v_mul_f32 v90, v90, v87\n"
    "v_cvt_pk_bf16_f32 v49, v89, v90\n"
    "v_max3_f32 v84, v72, v73, v74\n"
    "v_max_f32 v84, v84, v75\n"
    "v_mov_b32 v89, v84\n"
    "s_nop 1\n"
    "v_permlane16_swap_b32 v84, v89\n"
    "v_max_f32 v84, v84, v89\n"
    "v_mov_b32 v89, v84\n"
    "s_nop 1\n"
    "v_permlane32_swap_b32 v84, v89\n"
    "v_max_f32 v84, v84, v89\n"
    "v_cmp_lt_f32 vcc, s74, v84\n"
    "s_nop 1\n"
    "v_cndmask_b32 v85, 0, v84, vcc\n"
    "s_cmp_lg_u64 vcc, 0\n"
    "s_cselect_b32 s77, 1, s77\n"
    "v_sub_f32 v87, 0, v85\n"
    "v_exp_f32 v87, v87\n"
    "v_sub_f32 v72, v72, v85\n"
    "v_sub_f32 v73, v73, v85\n"
    "v_sub_f32 v74, v74, v85\n"
    "v_sub_f32 v75, v75, v85\n"
    "v_sub_f32 v32, v32, v85\n"
    "v_sub_f32 v33, v33, v85\n"
    "v_sub_f32 v34, v34, v85\n"
    "v_sub_f32 v35, v35, v85\n"
    "v_mul_f32 v108, v108, v87\n"
    "v_mul_f32 v109, v109, v87\n"
    "v_mul_f32 v110, v110, v87\n"
    "v_mul_f32 v111, v111, v87\n"
    "v_mul_f32 v128, v128, v87\n"
    "v_mul_f32 v129, v129, v87\n"
    "v_mul_f32 v130, v130, v87\n"
    "v_mul_f32 v131, v131, v87\n"
    "v_mul_f32 v148, v148, v87\n"
    "v_mul_f32 v149, v149, v87\n"
    "v_mul_f32 v150, v150, v87\n"
    "v_mul_f32 v151, v151, v87\n"
    "v_mul_f32 v168, v168, v87\n"
    "v_mul_f32 v169, v169, v87\n"
    "v_mul_f32 v170, v170, v87\n"
    "v_mul_f32 v171, v171, v87\n"
    "v_mul_f32 v188, v188, v87\n"
    "v_mul_f32 v189, v189, v87\n"
    "v_mul_f32 v190, v190, v87\n"
    "v_mul_f32 v191, v191, v87\n"
    "v_mul_f32 v208, v208, v87\n"
    "v_mul_f32 v209, v209, v87\n"
    "v_mul_f32 v210, v210, v87\n"
    "v_mul_f32 v211, v211, v87\n"
    "v_mul_f32 v228, v228, v87\n"
    "v_mul_f32 v229, v229, v87\n"
    "v_mul_f32 v230, v230, v87\n"
    "v_mul_f32 v231, v231, v87\n"
    "v_mul_f32 v248, v248, v87\n"
    "v_mul_f32 v249, v249, v87\n"
    "v_mul_f32 v250, v250, v87\n"
    "v_mul_f32 v251, v251, v87\n"
    "v_accvgpr_read_b32 v89, a172\n"
    "v_mul_f32 v89, v89, v87\n"
    "v_accvgpr_write_b32 a172, v89\n"
    "v_accvgpr_read_b32 v89, a173\n"
    "v_mul_f32 v89, v89, v87\n"
    "v_accvgpr_write_b32 a173, v89\n"
    "v_accvgpr_read_b32 v89, a174\n"
    "v_mul_f32 v89, v89, v87\n"
    "v_accvgpr_write_b32 a174, v89\n"
    "v_accvgpr_read_b32 v89, a175\n"
    "v_mul_f32 v89, v89, v87\n"
    "v_accvgpr_write_b32 a175, v89\n"
    "v_accvgpr_read_b32 v90, a236\n"
    "v_lshlrev_b32 v89, 16, v90\n"
    "v_and_b32 v90, 0xffff0000, v90\n"
    "v_mul_f32 v89, v89, v87\n"
    "v_mul_f32 v90, v90, v87\n"
    "v_cvt_pk_bf16_f32 v89, v89, v90\n"
    "v_accvgpr_write_b32 a236, v89\n"
    "v_accvgpr_read_b32 v90, a237\n"
    "v_lshlrev_b32 v89, 16, v90\n"
    "v_and_b32 v90, 0xffff0000, v90\n"
    "v_mul_f32 v89, v89, v87\n"
    "v_mul_f32 v90, v90, v87\n"
    "v_cvt_pk_bf16_f32 v89, v89, v90\n"
    "v_accvgpr_write_b32 a237, v89\n"
    "v_accvgpr_read_b32 v90, a238\n"
    "v_lshlrev_b32 v89, 16, v90\n"
    "v_and_b32 v90, 0xffff0000, v90\n"
    "v_mul_f32 v89, v89, v87\n"
    "v_mul_f32 v90, v90, v87\n"
    "v_cvt_pk_bf16_f32 v89, v89, v90\n"
    "v_accvgpr_write_b32 a238, v89\n"
    "v_accvgpr_read_b32 v90, a239\n"
    "v_lshlrev_b32 v89, 16, v90\n"
    "v_and_b32 v90, 0xffff0000, v90\n"
    "v_mul_f32 v89, v89, v87\n"
    "v_mul_f32 v90, v90, v87\n"
    "v_cvt_pk_bf16_f32 v89, v89, v90\n"
    "v_accvgpr_write_b32 a239, v89\n"
    "v_lshlrev_b32 v89, 16, v52\n"
    "v_and_b32 v90, 0xffff0000, v52\n"
    "v_mul_f32 v89, v89, v87\n"
    "v_mul_f32 v90, v90, v87\n"
    "v_cvt_pk_bf16_f32 v52, v89, v90\n"
    "v_lshlrev_b32 v89, 16, v53\n"
    "v_and_b32 v90, 0xffff0000, v53\n"
    "v_mul_f32 v89, v89, v87\n"
    "v_mul_f32 v90, v90, v87\n"
    "v_cvt_pk_bf16_f32 v53, v89, v90\n"
    "v_max3_f32 v84, v76, v77, v78\n"
    "v_max_f32 v84, v84, v79\n"
    "v_mov_b32 v89, v84\n"
    "s_nop 1\n"
    "v_permlane16_swap_b32 v84, v89\n"
    "v_max_f32 v84, v84, v89\n"
    "v_mov_b32 v89, v84\n"
    "s_nop 1\n"
    "v_permlane32_swap_b32 v84, v89\n"
    "v_max_f32 v84, v84, v89\n"
    "v_cmp_lt_f32 vcc, s74, v84\n"
    "s_nop 1\n"
    "v_cndmask_b32 v85, 0, v84, vcc\n"
    "s_cmp_lg_u64 vcc, 0\n"
    "s_cselect_b32 s77, 1, s77\n"
    "v_sub_f32 v87, 0, v85\n"
    "v_exp_f32 v87, v87\n"
    "v_sub_f32 v76, v76, v85\n"
    "v_sub_f32 v77, v77, v85\n"
    "v_sub_f32 v78, v78, v85\n"
    "v_sub_f32 v79, v79, v85\n"
    "v_sub_f32 v36, v36, v85\n"
    "v_sub_f32 v37, v37, v85\n"
    "v_sub_f32 v38, v38, v85\n"
    "v_sub_f32 v39, v39, v85\n"
    "v_mul_f32 v112, v112, v87\n"
    "v_mul_f32 v113, v113, v87\n"
    "v_mul_f32 v114, v114, v87\n"
    "v_mul_f32 v115, v115, v87\n"
    "v_mul_f32 v132, v132, v87\n"
    "v_mul_f32 v133, v133, v87\n"
    "v_mul_f32 v134, v134, v87\n"
    "v_mul_f32 v135, v135, v87\n"
    "v_mul_f32 v152, v152, v87\n"
    "v_mul_f32 v153, v153, v87\n"
    "v_mul_f32 v154, v154, v87\n"
    "v_mul_f32 v155, v155, v87\n"
    "v_mul_f32 v172, v172, v87\n"
    "v_mul_f32 v173, v173, v87\n"
    "v_mul_f32 v174, v174, v87\n"
    "v_mul_f32 v175, v175, v87\n"
    "v_mul_f32 v192, v192, v87\n"
    "v_mul_f32 v193, v193, v87\n"
    "v_mul_f32 v194, v194, v87\n"
    "v_mul_f32 v195, v195, v87\n"
    "v_mul_f32 v212, v212, v87\n"
    "v_mul_f32 v213, v213, v87\n"
    "v_mul_f32 v214, v214, v87\n"
    "v_mul_f32 v215, v215, v87\n"
    "v_mul_f32 v232, v232, v87\n"
    "v_mul_f32 v233, v233, v87\n"
    "v_mul_f32 v234, v234, v87\n"
    "v_mul_f32 v235, v235, v87\n"
    "v_mul_f32 v252, v252, v87\n"
    "v_mul_f32 v253, v253, v87\n"
    "v_mul_f32 v254, v254, v87\n"
    "v_mul_f32 v255, v255, v87\n"
    "v_accvgpr_read_b32 v89, a176\n"
    "v_mul_f32 v89, v89, v87\n"
    "v_accvgpr_write_b32 a176, v89\n"
    "v_accvgpr_read_b32 v89, a177\n"
    "v_mul_f32 v89, v89, v87\n"
    "v_accvgpr_write_b32 a177, v89\n"
    "v_accvgpr_read_b32 v89, a178\n"
    "v_mul_f32 v89, v89, v87\n"
    "v_accvgpr_write_b32 a178, v89\n"
    "v_accvgpr_read_b32 v89, a179\n"
    "v_mul_f32 v89, v89, v87\n"
    "v_accvgpr_write_b32 a179, v89\n"
    "v_accvgpr_read_b32 v90, a240\n"
    "v_lshlrev_b32 v89, 16, v90\n"
    "v_and_b32 v90, 0xffff0000, v90\n"
    "v_mul_f32 v89, v89, v87\n"
    "v_mul_f32 v90, v90, v87\n"
    "v_cvt_pk_bf16_f32 v89, v89, v90\n"
    "v_accvgpr_write_b32 a240, v89\n"
    "v_accvgpr_read_b32 v90, a241\n"
    "v_lshlrev_b32 v89, 16, v90\n"
    "v_and_b32 v90, 0xffff0000, v90\n"
    "v_mul_f32 v89, v89, v87\n"
    "v_mul_f32 v90, v90, v87\n"
    "v_cvt_pk_bf16_f32 v89, v89, v90\n"
    "v_accvgpr_write_b32 a241, v89\n"
    "v_accvgpr_read_b32 v90, a242\n"
    "v_lshlrev_b32 v89, 16, v90\n"
    "v_and_b32 v90, 0xffff0000, v90\n"
    "v_mul_f32 v89, v89, v87\n"
    "v_mul_f32 v90, v90, v87\n"
    "v_cvt_pk_bf16_f32 v89, v89, v90\n"
    "v_accvgpr_write_b32 a242, v89\n"
    "v_accvgpr_read_b32 v90, a243\n"
    "v_lshlrev_b32 v89, 16, v90\n"
    "v_and_b32 v90, 0xffff0000, v90\n"
    "v_mul_f32 v89, v89, v87\n"
    "v_mul_f32 v90, v90, v87\n"
    "v_cvt_pk_bf16_f32 v89, v89, v90\n"
    "v_accvgpr_write_b32 a243, v89\n"
    "v_lshlrev_b32 v89, 16, v56\n"
    "v_and_b32 v90, 0xffff0000, v56\n"
    "v_mul_f32 v89, v89, v87\n"
    "v_mul_f32 v90, v90, v87\n"
    "v_cvt_pk_bf16_f32 v56, v89, v90\n"
    "v_lshlrev_b32 v89, 16, v57\n"
    "v_and_b32 v90, 0xffff0000, v57\n"
    "v_mul_f32 v89, v89, v87\n"
    "v_mul_f32 v90, v90, v87\n"
    "v_cvt_pk_bf16_f32 v57, v89, v90\n"
    "ds_write_b32 v12, v20 offset:0\n"
    "ds_write_b32 v12, v24 offset:256\n"
    "ds_write_b32 v12, v28 offset:512\n"
    "ds_write_b32 v12, v32 offset:768\n"
    "ds_write_b32 v12, v36 offset:1024\n"
    "s_cmp_lg_u32 s77, 0\n"
    "s_cselect_b32 s79, 0x7f800000, 0\n"
    "s_cmov_b32 s73, 0xff800000\n"
    "v_mov_b32 v89, s79\n"
    "ds_write_b32 v12, v89 offset:1280\n"
    "s_mov_b32 s77, 0\n"
    "s_mov_b32 s85, s74\n"
    "s_waitcnt lgkmcnt(0)\n"
    "s_nop 7\n"
    "s_cmp_eq_u32 s78, 0\n"
    "s_cbranch_scc1 R1R_f_%=\n"
    "s_cmp_eq_u32 s78, 1\n"
    "s_cbranch_scc1 R1R_o_%=\n"
    "s_cmp_eq_u32 s78, 2\n"
    "s_cbranch_scc1 R1R_e_%=\n"
    "s_cmp_eq_u32 s78, 3\n"
    "s_cbranch_scc1 R1R_x_%=\n"
    "s_branch R1R_y_%=\n"
    "SPIN_%=:\n"
    "s_waitcnt lgkmcnt(0)\n"
    "s_mov_b32 s99, 0x100000\n"
    "SPINL_%=:\n"
    "buffer_load_dword v7, off, s[44:47], s98 sc1\n"
    "s_waitcnt vmcnt(0)\n"
    "v_cmp_ne_u32 vcc, v7, v6\n"
    "s_cbranch_vccnz RP2_%=\n"
    "RP2R_%=:\n"
    "buffer_load_dwordx2 v[80:81], v8, s[44:47], s67 offen offset:0 sc1\n"
    "buffer_load_dwordx2 v[82:83], v8, s[44:47], s67 offen offset:128 sc1\n"
    "buffer_load_dwordx2 v[84:85], v8, s[44:47], s67 offen offset:256 sc1\n"
    "buffer_load_dwordx2 v[86:87], v8, s[44:47], s67 offen offset:384 sc1\n"
    "buffer_load_dwordx2 v[88:89], v8, s[44:47], s67 offen offset:512 sc1\n"
    "buffer_load_dwordx2 v[90:91], v8, s[44:47], s67 offen offset:640 sc1\n"
    "buffer_load_dwordx2 v[92:93], v8, s[44:47], s67 offen offset:768 sc1\n"
    "buffer_load_dwordx2 v[94:95], v8, s[44:47], s67 offen offset:896 sc1\n"
    "buffer_load_dwordx2 v[18:19], v8, s[44:47], s67 offen offset:1024 sc1\n"
    "buffer_load_dwordx2 v[12:13], v8, s[44:47], s67 offen offset:1152 sc1\n"
    "s_waitcnt vmcnt(9)\n"
    "v_xor_b32 v5, s76, v81\n"
    "s_waitcnt vmcnt(8)\n"
    "v_xor_b32 v83, s76, v83\n"
    "v_or_b32 v5, v5, v83\n"
    "s_waitcnt vmcnt(7)\n"
    "v_xor_b32 v85, s76, v85\n"
    "v_or_b32 v5, v5, v85\n"
    "s_waitcnt vmcnt(6)\n"
    "v_xor_b32 v87, s76, v87\n"
    "v_or_b32 v5, v5, v87\n"
    "s_waitcnt vmcnt(5)\n"
    "v_xor_b32 v89, s76, v89\n"
    "v_or_b32 v5, v5, v89\n"
    "s_waitcnt vmcnt(4)\n"
    "v_xor_b32 v91, s76, v91\n"
    "v_or_b32 v5, v5, v91\n"
    "s_waitcnt vmcnt(3)\n"
    "v_xor_b32 v93, s76, v93\n"
    "v_or_b32 v5, v5, v93\n"
    "s_waitcnt vmcnt(2)\n"
    "v_xor_b32 v95, s76, v95\n"
    "v_or_b32 v5, v5, v95\n"
    "s_waitcnt vmcnt(1)\n"
    "v_xor_b32 v19, s76, v19\n"
    "v_or_b32 v5, v5, v19\n"
    "s_waitcnt vmcnt(0)\n"
    "v_xor_b32 v13, s76, v13\n"
    "v_or_b32 v5, v5, v13\n"
    "v_cmp_ne_u32 vcc, 0, v5\n"
    "s_cbranch_vccz SPINR_%=\n"
    "s_sub_u32 s99, s99, 1\n"
    "s_cmp_lg_u32 s99, 0\n"
    "s_cbranch_scc1 SPINL_%=\n"
    "s_branch SPINR_%=\n"
    "RP_%=:\n"
    "v_mov_b32 v6, v7\n"
    "s_lshl_b32 s79, s22, 11\n"
    "s_xor_b32 s79, s79, 4096\n"
    "v_mov_b32 v19, v7\n"
    "v_mov_b32 v18, v60\n"
    "buffer_store_dwordx2 v[18:19], v8, s[44:47], s79 offen offset:0 sc1\n"
    "s_nop 1\n"
    "v_mov_b32 v18, v62\n"
    "buffer_store_dwordx2 v[18:19], v8, s[44:47], s79 offen offset:128 sc1\n"
    "s_nop 1\n"
    "v_mov_b32 v18, v64\n"
    "buffer_store_dwordx2 v[18:19], v8, s[44:47], s79 offen offset:256 sc1\n"
    "s_nop 1\n"
    "v_mov_b32 v18, v66\n"
    "buffer_store_dwordx2 v[18:19], v8, s[44:47], s79 offen offset:384 sc1\n"
    "s_nop 1\n"
    "v_mov_b32 v18, v68\n"
    "buffer_store_dwordx2 v[18:19], v8, s[44:47], s79 offen offset:512 sc1\n"
    "s_nop 1\n"
    "v_mov_b32 v18, v70\n"
    "buffer_store_dwordx2 v[18:19], v8, s[44:47], s79 offen offset:640 sc1\n"
    "s_nop 1\n"
    "v_mov_b32 v18, v72\n"
    "buffer_store_dwordx2 v[18:19], v8, s[44:47], s79 offen offset:768 sc1\n"
    "s_nop 1\n"
    "v_mov_b32 v18, v74\n"
    "buffer_store_dwordx2 v[18:19], v8, s[44:47], s79 offen offset:896 sc1\n"
    "s_nop 1\n"
    "v_mov_b32 v18, v76\n"
    "buffer_store_dwordx2 v[18:19], v8, s[44:47], s79 offen offset:1024 sc1\n"
    "s_nop 1\n"
    "v_mov_b32 v18, v78\n"
    "buffer_store_dwordx2 v[18:19], v8, s[44:47], s79 offen offset:1152 sc1\n"
    "s_nop 1\n"
    "s_branch RPR_%=\n"
    "RP2_%=:\n"
    "v_mov_b32 v6, v7\n"
    "s_lshl_b32 s79, s22, 11\n"
    "s_xor_b32 s79, s79, 4096\n"
    "v_mov_b32 v19, v7\n"
    "v_mov_b32 v18, v60\n"
    "buffer_store_dwordx2 v[18:19], v8, s[44:47], s79 offen offset:0 sc1\n"
    "s_nop 1\n"
    "v_mov_b32 v18, v62\n"
    "buffer_store_dwordx2 v[18:19], v8, s[44:47], s79 offen offset:128 sc1\n"
    "s_nop 1\n"
    "v_mov_b32 v18, v64\n"
    "buffer_store_dwordx2 v[18:19], v8, s[44:47], s79 offen offset:256 sc1\n"
    "s_nop 1\n"
    "v_mov_b32 v18, v66\n"
    "buffer_store_dwordx2 v[18:19], v8, s[44:47], s79 offen offset:384 sc1\n"
    "s_nop 1\n"
    "v_mov_b32 v18, v68\n"
    "buffer_store_dwordx2 v[18:19], v8, s[44:47], s79 offen offset:512 sc1\n"
    "s_nop 1\n"
    "v_mov_b32 v18, v70\n"
    "buffer_store_dwordx2 v[18:19], v8, s[44:47], s79 offen offset:640 sc1\n"
    "s_nop 1\n"
    "v_mov_b32 v18, v72\n"
    "buffer_store_dwordx2 v[18:19], v8, s[44:47], s79 offen offset:768 sc1\n"
    "s_nop 1\n"
    "v_mov_b32 v18, v74\n"
    "buffer_store_dwordx2 v[18:19], v8, s[44:47], s79 offen offset:896 sc1\n"
    "s_nop 1\n"
    "v_mov_b32 v18, v76\n"
    "buffer_store_dwordx2 v[18:19], v8, s[44:47], s79 offen offset:1024 sc1\n"
    "s_nop 1\n"
    "v_mov_b32 v18, v78\n"
    "buffer_store_dwordx2 v[18:19], v8, s[44:47], s79 offen offset:1152 sc1\n"
    "s_nop 1\n"
    "s_branch RP2R_%=\n"
    "SPA_%=:\n"
    "s_mov_b32 s99, 0x100000\n"
    "SPAL_%=:\n"
    "buffer_load_dword v86, v84, s[52:55], s66 offen offset:1024 sc1\n"
    "s_waitcnt vmcnt(0)\n"
    "v_sub_u32 v86, v86, v9\n"
    "v_cmp_lt_u32 vcc, 1, v86\n"
    "s_cbranch_vccz SPAD_%=\n"
    "s_sub_u32 s99, s99, 1\n"
    "s_cmp_lg_u32 s99, 0\n"
    "s_cbranch_scc1 SPAL_%=\n"
    "SPAD_%=:\n"
    "s_branch SPAR_%=\n"
    "END_%=:\n"
    :: "s"(__builtin_amdgcn_kernarg_segment_ptr()),"s"(blockIdx.x),"s"(wid),"s"(ldsb)
    : "memory","vcc","scc", SCLOB, VCLOB, ACLOB);
}

extern "C" void kernel_launch(void* const* d_in, const int* in_sizes, int n_in, void* d_out, int out_size, void* d_ws, size_t ws_size, hipStream_t stream){
  attn_main<<<256,256,0,stream>>>((const char*)d_in[0],(const char*)d_in[1],(const char*)d_in[2],(const char*)d_in[3],(const char*)d_in[4],(char*)d_ws,(char*)d_out);
}
```
